```python
import torch

def _softmax(x, axis=-1):
    return torch.softmax(x, dim=axis)
def _gelu(x):
    return torch.nn.functional.gelu(x, approximate="tanh")
def _silu(x):
    return torch.nn.functional.silu(x)
def _sigmoid(x):
    return torch.sigmoid(x)
def _layernorm(x, axis=-1, eps=1e-5):
    m = x.mean(dim=axis, keepdim=True)
    v = x.var(dim=axis, keepdim=True, unbiased=False)
    return (x - m) / torch.sqrt(v + eps)
def _scatter_add(tgt, idx, src, axis=0):
    return tgt.clone().index_add_(axis, idx, src)
def _segment_sum(src, seg, n):
    out = torch.zeros((n,) + src.shape[1:], dtype=src.dtype)
    return out.index_add_(0, seg, src)
def _pad(x, pw):
    flat = tuple(v for pair in reversed(pw) for v in pair)
    return torch.nn.functional.pad(x, flat)

kernel_name = 'standalone__mha_softmax__near_ridge'

def setup_inputs():

    _w = (lambda a: (lambda t: t.to(torch.bfloat16) if t.is_floating_point() else t)(torch.as_tensor(a)))
    B, H, Sq, Sk, D = 32, 16, 384, 384, 256
    return {
        "q": _w((lambda *s: torch.randn(*s, dtype=torch.bfloat16) * 0.8)(B, H, Sq, D)),
        "k": _w((lambda *s: torch.randn(*s, dtype=torch.bfloat16) * 0.8)(B, H, Sk, D)),
        "v": _w((lambda *s: torch.randn(*s, dtype=torch.bfloat16) * 0.05)(B, H, Sk, D)),
    }

def reference(q, k, v):
    scale = q.shape[-1] ** -0.5
    scores = torch.einsum("bhqd,bhkd->bhqk", q, k) * scale
    p = _softmax(scores, axis=-1)
    return torch.einsum("bhqk,bhkd->bhqd", p, v)
```

```cpp
#include <hip/hip_runtime.h>
#include <hip/hip_bf16.h>

typedef short    bf16x8 __attribute__((ext_vector_type(8)));
typedef short    s16x4  __attribute__((ext_vector_type(4)));
typedef float    f32x4  __attribute__((ext_vector_type(4)));
typedef float    f32x2  __attribute__((ext_vector_type(2)));
typedef __bf16   bf16x2 __attribute__((ext_vector_type(2)));
typedef unsigned u32x4  __attribute__((ext_vector_type(4)));
typedef unsigned u32x2  __attribute__((ext_vector_type(2)));
typedef __attribute__((address_space(3))) const char* lds_cptr;
#define DI __device__ __forceinline__
constexpr int S = 384, D = 256, NT = 12;
DI unsigned cvtpk(float lo, float hi) { f32x2 v = {lo, hi}; bf16x2 b = __builtin_convertvector(v, bf16x2); return __builtin_bit_cast(unsigned, b); }
DI s16x4 vtr(lds_cptr p) { return __builtin_bit_cast(s16x4, __builtin_amdgcn_ds_read_tr16_b64_v4i16((__attribute__((address_space(3))) s16x4*)p)); }
#define MFMA(a, b, c) __builtin_amdgcn_mfma_f32_16x16x32_bf16(__builtin_bit_cast(__attribute__((ext_vector_type(8))) __bf16, a), __builtin_bit_cast(__attribute__((ext_vector_type(8))) __bf16, b), c, 0, 0, 0)

__global__ __launch_bounds__(256, 1) void attn_v0(const unsigned short* __restrict__ Q, const unsigned short* __restrict__ K,
                                                  const unsigned short* __restrict__ V, unsigned short* __restrict__ O) {
  extern __shared__ __attribute__((aligned(16))) char lds[];
  const int tid = threadIdx.x, lane = tid & 63, wid = __builtin_amdgcn_readfirstlane(tid >> 6);
  const int l15 = lane & 15, g = lane >> 4;
  const int b = blockIdx.x, xcd = b & 7, slot = b >> 3;
  const int head = (slot >> 1) * 8 + xcd, half = slot & 1;
  const size_t hb = (size_t)head * S * D;
  const unsigned short* Qw = Q + hb + (size_t)(half * 192 + wid * 48) * D;
  const unsigned short* Kh = K + hb; const unsigned short* Vh = V + hb;
  bf16x8 qf[3][8];
#pragma unroll
  for (int qs = 0; qs < 3; ++qs)
#pragma unroll
    for (int s = 0; s < 8; ++s) qf[qs][s] = *(const bf16x8*)(Qw + (size_t)(qs * 16 + l15) * D + s * 32 + g * 8);
  f32x4 o[3][16];
#pragma unroll
  for (int qs = 0; qs < 3; ++qs)
#pragma unroll
    for (int dt = 0; dt < 16; ++dt) o[qs][dt] = f32x4{0.f, 0.f, 0.f, 0.f};
  float m[3] = {-1e30f, -1e30f, -1e30f}, l[3] = {0.f, 0.f, 0.f};
  const float C = 0.0625f * 1.4426950408889634f;
  auto dma = [&](const unsigned short* src, int t, int ldsoff) {
#pragma unroll
    for (int j = 0; j < 4; ++j) {
      const int r = 2 * j + (lane >> 5);
      const char* gp = (const char*)src + (size_t)(t * 32 + 8 * wid + r) * 512 + (((lane & 31) ^ (2 * r)) << 4);
      __builtin_amdgcn_global_load_lds((const __attribute__((address_space(1))) void*)gp,
                                       (__attribute__((address_space(3))) void*)(lds + ldsoff + (4 * wid + j) * 1024), 16, 0, 0);
    }
  };
  dma(Kh, 0, 0); dma(Vh, 0, 32768);
  for (int t = 0; t < NT; ++t) {
    asm volatile("s_waitcnt vmcnt(0)" ::: "memory");
    __syncthreads();
    const int cur = (t & 1) * 16384;
    if (t + 1 < NT) { dma(Kh, t + 1, 16384 - cur); dma(Vh, t + 1, 32768 + 16384 - cur); }
    f32x4 sc[3][2];
#pragma unroll
    for (int qs = 0; qs < 3; ++qs) { sc[qs][0] = f32x4{0.f, 0.f, 0.f, 0.f}; sc[qs][1] = f32x4{0.f, 0.f, 0.f, 0.f}; }
#pragma unroll
    for (int s = 0; s < 8; ++s)
#pragma unroll
      for (int kb = 0; kb < 2; ++kb) {
        const bf16x8 kf = *(const __attribute__((address_space(3))) bf16x8*)((lds_cptr)lds + cur + (16 * kb + l15) * 512 + (((4 * s + g) ^ (2 * (l15 & 7))) << 4));
#pragma unroll
        for (int qs = 0; qs < 3; ++qs) sc[qs][kb] = MFMA(kf, qf[qs][s], sc[qs][kb]);
      }
    u32x4 pf[3];
#pragma unroll
    for (int qs = 0; qs < 3; ++qs) {
      float mx = fmaxf(fmaxf(fmaxf(sc[qs][0][0], sc[qs][0][1]), fmaxf(sc[qs][0][2], sc[qs][0][3])),
                       fmaxf(fmaxf(sc[qs][1][0], sc[qs][1][1]), fmaxf(sc[qs][1][2], sc[qs][1][3])));
      mx = fmaxf(mx, __shfl_xor(mx, 16)); mx = fmaxf(mx, __shfl_xor(mx, 32));
      const float mn = fmaxf(m[qs], mx * C), al = __builtin_amdgcn_exp2f(m[qs] - mn); m[qs] = mn;
      float p[8]; float ps = 0.f;
#pragma unroll
      for (int e = 0; e < 8; ++e) { p[e] = __builtin_amdgcn_exp2f(fmaf(sc[qs][e >> 2][e & 3], C, -mn)); ps += p[e]; }
      l[qs] = l[qs] * al + ps;
      pf[qs] = u32x4{cvtpk(p[0], p[1]), cvtpk(p[2], p[3]), cvtpk(p[4], p[5]), cvtpk(p[6], p[7])};
#pragma unroll
      for (int dt = 0; dt < 16; ++dt) o[qs][dt] *= al;
    }
    const int q4 = (lane >> 2) & 3, p4 = lane & 3;
#pragma unroll
    for (int dt = 0; dt < 16; ++dt) {
      const int key = 4 * g + q4;
      const lds_cptr vp = (lds_cptr)lds + 32768 + cur + key * 512 + (((2 * dt + (p4 >> 1)) ^ (2 * (key & 7))) << 4) + (p4 & 1) * 8;
      const s16x4 lo = vtr(vp), hi = vtr(vp + 16 * 512);
      const bf16x8 vf = __builtin_shufflevector(lo, hi, 0, 1, 2, 3, 4, 5, 6, 7);
#pragma unroll
      for (int qs = 0; qs < 3; ++qs) o[qs][dt] = MFMA(vf, __builtin_bit_cast(bf16x8, pf[qs]), o[qs][dt]);
    }
  }
  unsigned short* Ow = O + hb + (size_t)(half * 192 + wid * 48) * D;
#pragma unroll
  for (int qs = 0; qs < 3; ++qs) {
    float ls = l[qs]; ls += __shfl_xor(ls, 16); ls += __shfl_xor(ls, 32);
    const float il = 1.f / ls;
#pragma unroll
    for (int dt = 0; dt < 16; ++dt) {
      u32x2 w = {cvtpk(o[qs][dt][0] * il, o[qs][dt][1] * il), cvtpk(o[qs][dt][2] * il, o[qs][dt][3] * il)};
      *(u32x2*)(Ow + (size_t)(qs * 16 + l15) * D + dt * 16 + g * 4) = w;
    }
  }
}

extern "C" void kernel_launch(void* const* d_in, const int* in_sizes, int n_in, void* d_out, int out_size, void* d_ws, size_t ws_size, hipStream_t stream) {
  (void)in_sizes; (void)n_in; (void)out_size; (void)d_ws; (void)ws_size;
  hipFuncSetAttribute((const void*)attn_v0, hipFuncAttributeMaxDynamicSharedMemorySize, 65536);
  attn_v0<<<1024, 256, 65536, stream>>>((const unsigned short*)d_in[0], (const unsigned short*)d_in[1], (const unsigned short*)d_in[2], (unsigned short*)d_out);
}
```

```cpp
#include <hip/hip_runtime.h>

__global__ __launch_bounds__(256, 1) void mha_fwd_kernel(const void* q, const void* k, const void* v, void* o) {
  extern __shared__ __attribute__((aligned(16))) char lds[];
  const unsigned wid = __builtin_amdgcn_readfirstlane(threadIdx.x >> 6);
  const unsigned bid = blockIdx.x;
  const unsigned ldsb = (unsigned)(size_t)lds;
  asm volatile(
    "v_mbcnt_lo_u32_b32 v0, -1, 0\n"
    "v_mbcnt_hi_u32_b32 v0, -1, v0\n"
    "s_getpc_b64 s[18:19]\n"
    "s_bfe_u32 s16, %[bid], 196611\n"
    "s_lshl_b32 s16, s16, 2\n"
    "s_add_u32 s16, s16, %[wid]\n"
    "s_lshl_b32 s16, s16, 10\n"
    "v_lshlrev_b32 v11, 4, v0\n"
    "v_add_u32 v11, s16, v11\n"
    "global_load_dwordx4 v[12:15], v11, s[18:19]\n"
    "s_and_b32 s14, %[bid], 7\n"
    "s_lshr_b32 s15, %[bid], 4\n"
    "s_lshl_b32 s15, s15, 3\n"
    "s_add_u32 s14, s14, s15\n"
    "s_mul_i32 s14, s14, 196608\n"
    "s_bfe_u32 s15, %[bid], 0x10003\n"
    "s_mul_i32 s15, s15, 98304\n"
    "s_add_u32 s15, s15, s14\n"
    "s_mov_b64 s[20:21], %[k]\n"
    "s_add_u32 s20, s20, s14\n"
    "s_addc_u32 s21, s21, 0\n"
    "s_mov_b32 s22, 196608\n"
    "s_mov_b32 s23, 0x20000\n"
    "s_mul_i32 s16, %[wid], 24576\n"
    "s_add_u32 s35, s16, %[lds]\n"
    "s_add_u32 s35, s35, 65536\n"
    "s_add_u32 s16, s16, s15\n"
    "s_mov_b64 s[28:29], %[q]\n"
    "s_add_u32 s28, s28, s16\n"
    "s_addc_u32 s29, s29, 0\n"
    "s_mov_b32 s34, 24576\n"
    "s_mov_b32 s30, 24576\n"
    "s_mov_b32 s31, 0x20000\n"
    "s_lshl_b32 s91, %[wid], 12\n"
    "s_add_u32 s91, s91, %[lds]\n"
    "s_mov_b32 s88, 4096\n"
    "s_mov_b32 s89, 12288\n"
    "s_mov_b32 s90, 20480\n"
    "s_mov_b32 s40, 0\n"
    "s_mov_b32 s41, 8192\n"
    "s_mov_b32 s42, 16384\n"
    "s_mov_b32 s43, 24576\n"
    "v_lshrrev_b32 v1, 5, v0\n"
    "v_and_b32 v2, 31, v0\n"
    "v_lshlrev_b32 v3, 9, v1\n"
    "v_lshlrev_b32 v4, 1, v1\n"
    "v_xor_b32 v4, v4, v2\n"
    "v_lshlrev_b32 v4, 4, v4\n"
    "v_xor_b32 v5, 0, v4\n"
    "v_add_u32 v84, v3, v5\n"
    "v_xor_b32 v5, 64, v4\n"
    "v_add_u32 v85, v3, v5\n"
    "v_xor_b32 v5, 128, v4\n"
    "v_add_u32 v86, v3, v5\n"
    "v_xor_b32 v5, 192, v4\n"
    "v_add_u32 v87, v3, v5\n"
    "s_and_b32 s17, %[wid], 1\n"
    "s_lshl_b32 s17, s17, 7\n"
    "s_lshl_b32 s15, %[wid], 11\n"
    "v_xor_b32 v4, s17, v4\n"
    "v_add_u32 v3, s15, v3\n"
    "v_xor_b32 v5, 0, v4\n"
    "v_add_u32 v80, v3, v5\n"
    "v_xor_b32 v5, 64, v4\n"
    "v_add_u32 v81, v3, v5\n"
    "s_add_u32 m0, s91, 0\n"
    "s_nop 0\n"
    "buffer_load_dwordx4 v80, s[20:23], s40 offen lds\n"
    "buffer_load_dwordx4 v81, s[20:23], s40 offen offset:1024 nt lds\n"
    "s_add_u32 m0, s91, 16384\n"
    "s_nop 0\n"
    "buffer_load_dwordx4 v80, s[20:23], s41 offen lds\n"
    "buffer_load_dwordx4 v81, s[20:23], s41 offen offset:1024 nt lds\n"
    "s_add_u32 m0, s91, 32768\n"
    "s_nop 0\n"
    "buffer_load_dwordx4 v80, s[20:23], s42 offen lds\n"
    "buffer_load_dwordx4 v81, s[20:23], s42 offen offset:1024 nt lds\n"
    "s_add_u32 m0, s91, 49152\n"
    "s_nop 0\n"
    "buffer_load_dwordx4 v80, s[20:23], s43 offen lds\n"
    "buffer_load_dwordx4 v81, s[20:23], s43 offen offset:1024 nt lds\n"
    "s_add_u32 m0, s35, 0\n"
    "s_nop 0\n"
    "buffer_load_dwordx4 v84, s[28:31], s40 offen nt lds\n"
    "buffer_load_dwordx4 v85, s[28:31], s40 offen offset:1024 nt lds\n"
    "buffer_load_dwordx4 v86, s[28:31], s40 offen offset:2048 nt lds\n"
    "buffer_load_dwordx4 v87, s[28:31], s40 offen offset:3072 nt lds\n"
    "s_add_u32 m0, s35, 4096\n"
    "s_nop 0\n"
    "buffer_load_dwordx4 v84, s[28:31], s88 offen nt lds\n"
    "buffer_load_dwordx4 v85, s[28:31], s88 offen offset:1024 nt lds\n"
    "buffer_load_dwordx4 v86, s[28:31], s88 offen offset:2048 nt lds\n"
    "buffer_load_dwordx4 v87, s[28:31], s88 offen offset:3072 nt lds\n"
    "s_add_u32 m0, s35, 8192\n"
    "s_nop 0\n"
    "buffer_load_dwordx4 v84, s[28:31], s41 offen nt lds\n"
    "buffer_load_dwordx4 v85, s[28:31], s41 offen offset:1024 nt lds\n"
    "buffer_load_dwordx4 v86, s[28:31], s41 offen offset:2048 nt lds\n"
    "buffer_load_dwordx4 v87, s[28:31], s41 offen offset:3072 nt lds\n"
    "s_add_u32 m0, s35, 12288\n"
    "s_nop 0\n"
    "buffer_load_dwordx4 v84, s[28:31], s89 offen nt lds\n"
    "buffer_load_dwordx4 v85, s[28:31], s89 offen offset:1024 nt lds\n"
    "buffer_load_dwordx4 v86, s[28:31], s89 offen offset:2048 nt lds\n"
    "buffer_load_dwordx4 v87, s[28:31], s89 offen offset:3072 nt lds\n"
    "s_mov_b64 s[24:25], %[v]\n"
    "s_add_u32 s24, s24, s14\n"
    "s_addc_u32 s25, s25, 0\n"
    "s_sub_u32 s24, s24, 4096\n"
    "s_subb_u32 s25, s25, 0\n"
    "s_mov_b32 s26, 200704\n"
    "s_mov_b32 s27, 0x20000\n"
    "s_mov_b64 s[36:37], %[o]\n"
    "s_add_u32 s36, s36, s16\n"
    "s_addc_u32 s37, s37, 0\n"
    "s_sub_u32 s36, s36, 25165824\n"
    "s_subb_u32 s37, s37, 0\n"
    "s_mov_b32 s38, 0\n"
    "s_mov_b32 s39, 0x20000\n"
    "s_mov_b32 s44, 32768\n"
    "s_mov_b32 s45, 40960\n"
    "s_mov_b32 s46, 49152\n"
    "s_mov_b32 s47, 57344\n"
    "s_mov_b32 s48, 65536\n"
    "s_mov_b32 s49, 73728\n"
    "s_mov_b32 s50, 81920\n"
    "s_mov_b32 s51, 90112\n"
    "s_mov_b32 s52, 98304\n"
    "s_mov_b32 s53, 106496\n"
    "s_mov_b32 s54, 114688\n"
    "s_mov_b32 s55, 122880\n"
    "s_mov_b32 s56, 131072\n"
    "s_mov_b32 s57, 139264\n"
    "s_mov_b32 s58, 147456\n"
    "s_mov_b32 s59, 155648\n"
    "s_mov_b32 s60, 163840\n"
    "s_mov_b32 s61, 172032\n"
    "s_mov_b32 s62, 180224\n"
    "s_mov_b32 s63, 188416\n"
    "s_mov_b32 s64, 163840\n"
    "s_mov_b32 s65, 163968\n"
    "s_mov_b32 s66, 164096\n"
    "s_mov_b32 s67, 164224\n"
    "s_mov_b32 s68, 0\n"
    "s_mov_b32 s69, 128\n"
    "s_mov_b32 s70, 256\n"
    "s_mov_b32 s71, 384\n"
    "s_mov_b32 s72, 32768\n"
    "s_mov_b32 s73, 32896\n"
    "s_mov_b32 s74, 33024\n"
    "s_mov_b32 s75, 33152\n"
    "s_mov_b32 s76, 65536\n"
    "s_mov_b32 s77, 65664\n"
    "s_mov_b32 s78, 65792\n"
    "s_mov_b32 s79, 65920\n"
    "s_mov_b32 s80, 98304\n"
    "s_mov_b32 s81, 98432\n"
    "s_mov_b32 s82, 98560\n"
    "s_mov_b32 s83, 98688\n"
    "s_mov_b32 s84, 131072\n"
    "s_mov_b32 s85, 131200\n"
    "s_mov_b32 s86, 131328\n"
    "s_mov_b32 s87, 131456\n"
    "v_lshrrev_b32 v3, 3, v0\n"
    "v_and_b32 v4, 7, v0\n"
    "v_and_b32 v5, 6, v3\n"
    "v_xor_b32 v5, v5, v4\n"
    "v_lshlrev_b32 v5, 4, v5\n"
    "v_lshlrev_b32 v6, 9, v3\n"
    "v_add_u32 v5, v5, v6\n"
    "s_lshl_b32 s15, %[wid], 13\n"
    "s_add_u32 s15, s15, 2048\n"
    "v_add_u32 v82, s15, v5\n"
    "v_add_u32 v83, 3072, v82\n"
    "v_lshlrev_b32 v5, 4, v4\n"
    "v_add_u32 v89, v6, v5\n"
    "v_lshrrev_b32 v5, 4, v0\n"
    "v_lshlrev_b32 v5, 9, v5\n"
    "v_and_b32 v9, 15, v0\n"
    "v_lshlrev_b32 v9, 4, v9\n"
    "v_add_u32 v89, v5, v9\n"
    "v_xor_b32 v5, v3, v4\n"
    "v_lshlrev_b32 v5, 4, v5\n"
    "v_lshlrev_b32 v6, 7, v3\n"
    "v_add3_u32 v88, v5, v6, s35\n"
    "v_lshrrev_b32 v5, 4, v0\n"
    "v_and_b32 v6, 15, v0\n"
    "v_xor_b32 v6, v6, v5\n"
    "v_lshlrev_b32 v6, 4, v6\n"
    "v_lshlrev_b32 v5, 8, v5\n"
    "v_add3_u32 v88, v5, v6, s35\n"
    "v_xor_b32 v90, 64, v88\n"
    "v_and_b32 v6, 15, v0\n"
    "v_lshrrev_b32 v7, 4, v0\n"
    "v_and_b32 v8, 7, v0\n"
    "v_lshrrev_b32 v3, 2, v6\n"
    "v_lshlrev_b32 v9, 12, v3\n"
    "v_and_b32 v4, 3, v6\n"
    "v_lshlrev_b32 v4, 9, v4\n"
    "v_add3_u32 v3, v9, v4, %[lds]\n"
    "v_lshlrev_b32 v4, 1, v8\n"
    "v_xor_b32 v4, v4, v7\n"
    "v_lshlrev_b32 v4, 4, v4\n"
    "v_xor_b32 v5, 0, v4\n"
    "v_add_u32 v72, v3, v5\n"
    "v_xor_b32 v5, 64, v4\n"
    "v_add_u32 v73, v3, v5\n"
    "v_xor_b32 v5, 128, v4\n"
    "v_add_u32 v74, v3, v5\n"
    "v_xor_b32 v5, 192, v4\n"
    "v_add_u32 v75, v3, v5\n"
    "v_lshrrev_b32 v9, 1, v9\n"
    "s_sub_u32 s15, s35, %[lds]\n"
    "v_sub_u32 v103, s15, v9\n"
    "v_lshlrev_b32 v3, 8, v6\n"
    "v_add_u32 v3, s35, v3\n"
    "v_lshrrev_b32 v4, 1, v7\n"
    "v_xor_b32 v4, v4, v8\n"
    "v_lshlrev_b32 v4, 4, v4\n"
    "v_and_b32 v5, 1, v7\n"
    "v_lshlrev_b32 v5, 3, v5\n"
    "v_add3_u32 v112, v3, v4, v5\n"
    "v_xor_b32 v113, 32, v112\n"
    "v_xor_b32 v114, 64, v112\n"
    "v_xor_b32 v115, 96, v112\n"
    "v_bfe_u32 v9, v0, 2, 3\n"
    "v_lshlrev_b32 v3, 7, v9\n"
    "v_lshrrev_b32 v4, 5, v0\n"
    "v_lshlrev_b32 v4, 10, v4\n"
    "v_add3_u32 v3, v3, v4, %[lds]\n"
    "v_and_b32 v5, 3, v0\n"
    "v_lshlrev_b32 v5, 3, v5\n"
    "v_add_u32 v3, v3, v5\n"
    "v_add_u32 v3, 2048, v3\n"
    "v_lshrrev_b32 v4, 1, v9\n"
    "v_lshlrev_b32 v4, 5, v4\n"
    "v_xor_b32 v5, 0, v4\n"
    "v_add_u32 v76, v3, v5\n"
    "v_xor_b32 v5, 32, v4\n"
    "v_add_u32 v77, v3, v5\n"
    "v_xor_b32 v5, 64, v4\n"
    "v_add_u32 v78, v3, v5\n"
    "v_xor_b32 v5, 96, v4\n"
    "v_add_u32 v79, v3, v5\n"
    "v_mov_b32 v3, 0x3f803f80\n"
    "v_accvgpr_write_b32 a160, v3\n"
    "v_accvgpr_write_b32 a161, v3\n"
    "v_accvgpr_write_b32 a162, v3\n"
    "v_accvgpr_write_b32 a163, v3\n"
    "s_mov_b32 s96, 0x3db8aa3b\n"
    "s_mov_b32 s99, 0xbdb8aa3b\n"
    "s_mov_b32 s97, 0x41800000\n"
    "s_mov_b32 s98, 0\n"
    "v_add_u32 v108, v103, v72\n"
    "v_add_u32 v109, v103, v73\n"
    "v_add_u32 v110, v103, v74\n"
    "v_add_u32 v111, v103, v75\n"
    "s_waitcnt vmcnt(16)\n"
    "s_barrier\n"
    "ds_read_b128 a[96:99], v72 offset:0\n"
    "ds_read_b128 a[100:103], v73 offset:0\n"
    "ds_read_b128 a[104:107], v74 offset:0\n"
    "ds_read_b128 a[108:111], v75 offset:0\n"
    "ds_read_b128 a[112:115], v72 offset:256\n"
    "ds_read_b128 a[116:119], v73 offset:256\n"
    "ds_read_b128 a[120:123], v74 offset:256\n"
    "ds_read_b128 a[124:127], v75 offset:256\n"
    "ds_read_b128 a[128:131], v72 offset:16384\n"
    "ds_read_b128 a[132:135], v73 offset:16384\n"
    "ds_read_b128 a[136:139], v74 offset:16384\n"
    "ds_read_b128 a[140:143], v75 offset:16384\n"
    "ds_read_b128 a[144:147], v72 offset:16640\n"
    "ds_read_b128 a[148:151], v73 offset:16640\n"
    "ds_read_b128 a[152:155], v74 offset:16640\n"
    "ds_read_b128 a[156:159], v75 offset:16640\n"
    "ds_read_b128 a[164:167], v72 offset:32768\n"
    "ds_read_b128 a[168:171], v73 offset:32768\n"
    "ds_read_b128 a[172:175], v74 offset:32768\n"
    "ds_read_b128 a[176:179], v75 offset:32768\n"
    "ds_read_b128 a[180:183], v72 offset:33024\n"
    "ds_read_b128 a[184:187], v73 offset:33024\n"
    "ds_read_b128 a[188:191], v74 offset:33024\n"
    "ds_read_b128 a[192:195], v75 offset:33024\n"
    "ds_read_b128 a[196:199], v72 offset:49152\n"
    "ds_read_b128 a[200:203], v73 offset:49152\n"
    "ds_read_b128 a[204:207], v74 offset:49152\n"
    "ds_read_b128 a[208:211], v75 offset:49152\n"
    "ds_read_b128 a[212:215], v72 offset:49408\n"
    "ds_read_b128 a[216:219], v73 offset:49408\n"
    "ds_read_b128 a[220:223], v74 offset:49408\n"
    "ds_read_b128 a[224:227], v75 offset:49408\n"
    "s_waitcnt lgkmcnt(0)\n"
    "s_barrier\n"
    "s_waitcnt vmcnt(8)\n"
    "ds_read_b128 a[0:3], v108 offset:0\n"
    "ds_read_b128 a[4:7], v109 offset:0\n"
    "ds_read_b128 a[8:11], v110 offset:0\n"
    "ds_read_b128 a[12:15], v111 offset:0\n"
    "ds_read_b128 a[16:19], v108 offset:256\n"
    "ds_read_b128 a[20:23], v109 offset:256\n"
    "ds_read_b128 a[24:27], v110 offset:256\n"
    "ds_read_b128 a[28:31], v111 offset:256\n"
    "s_waitcnt lgkmcnt(7)\n"
    "v_mfma_f32_16x16x32_bf16 v[116:119], a[96:99], a[0:3], 0\n"
    "s_add_u32 m0, s35, 16384\n"
    "s_waitcnt lgkmcnt(6)\n"
    "v_mfma_f32_16x16x32_bf16 v[116:119], a[100:103], a[4:7], v[116:119]\n"
    "buffer_load_dwordx4 v84, s[28:31], s42 offen nt lds\n"
    "s_waitcnt lgkmcnt(5)\n"
    "v_mfma_f32_16x16x32_bf16 v[116:119], a[104:107], a[8:11], v[116:119]\n"
    "s_waitcnt lgkmcnt(4)\n"
    "v_mfma_f32_16x16x32_bf16 v[116:119], a[108:111], a[12:15], v[116:119]\n"
    "buffer_load_dwordx4 v85, s[28:31], s42 offen offset:1024 nt lds\n"
    "s_waitcnt lgkmcnt(3)\n"
    "v_mfma_f32_16x16x32_bf16 v[116:119], a[112:115], a[16:19], v[116:119]\n"
    "s_waitcnt lgkmcnt(2)\n"
    "v_mfma_f32_16x16x32_bf16 v[116:119], a[116:119], a[20:23], v[116:119]\n"
    "buffer_load_dwordx4 v86, s[28:31], s42 offen offset:2048 nt lds\n"
    "s_waitcnt lgkmcnt(1)\n"
    "v_mfma_f32_16x16x32_bf16 v[116:119], a[120:123], a[24:27], v[116:119]\n"
    "s_waitcnt lgkmcnt(0)\n"
    "v_mfma_f32_16x16x32_bf16 v[116:119], a[124:127], a[28:31], v[116:119]\n"
    "buffer_load_dwordx4 v87, s[28:31], s42 offen offset:3072 nt lds\n"
    "v_mfma_f32_16x16x32_bf16 v[120:123], a[128:131], a[0:3], 0\n"
    "s_add_u32 m0, s35, 20480\n"
    "v_mfma_f32_16x16x32_bf16 v[120:123], a[132:135], a[4:7], v[120:123]\n"
    "v_mfma_f32_16x16x32_bf16 v[120:123], a[136:139], a[8:11], v[120:123]\n"
    "buffer_load_dwordx4 v84, s[28:31], s90 offen nt lds\n"
    "v_mfma_f32_16x16x32_bf16 v[120:123], a[140:143], a[12:15], v[120:123]\n"
    "v_mfma_f32_16x16x32_bf16 v[120:123], a[144:147], a[16:19], v[120:123]\n"
    "buffer_load_dwordx4 v85, s[28:31], s90 offen offset:1024 nt lds\n"
    "v_mfma_f32_16x16x32_bf16 v[120:123], a[148:151], a[20:23], v[120:123]\n"
    "v_mfma_f32_16x16x32_bf16 v[120:123], a[152:155], a[24:27], v[120:123]\n"
    "buffer_load_dwordx4 v86, s[28:31], s90 offen offset:2048 nt lds\n"
    "v_mfma_f32_16x16x32_bf16 v[120:123], a[156:159], a[28:31], v[120:123]\n"
    "v_mfma_f32_16x16x32_bf16 v[124:127], a[164:167], a[0:3], 0\n"
    "buffer_load_dwordx4 v87, s[28:31], s90 offen offset:3072 nt lds\n"
    "v_mfma_f32_16x16x32_bf16 v[124:127], a[168:171], a[4:7], v[124:127]\n"
    "v_mfma_f32_16x16x32_bf16 v[124:127], a[172:175], a[8:11], v[124:127]\n"
    "v_mfma_f32_16x16x32_bf16 v[124:127], a[176:179], a[12:15], v[124:127]\n"
    "v_mfma_f32_16x16x32_bf16 v[124:127], a[180:183], a[16:19], v[124:127]\n"
    "v_mfma_f32_16x16x32_bf16 v[124:127], a[184:187], a[20:23], v[124:127]\n"
    "v_mfma_f32_16x16x32_bf16 v[124:127], a[188:191], a[24:27], v[124:127]\n"
    "v_mfma_f32_16x16x32_bf16 v[124:127], a[192:195], a[28:31], v[124:127]\n"
    "v_mfma_f32_16x16x32_bf16 v[128:131], a[196:199], a[0:3], 0\n"
    "v_mfma_f32_16x16x32_bf16 v[128:131], a[200:203], a[4:7], v[128:131]\n"
    "v_mfma_f32_16x16x32_bf16 v[128:131], a[204:207], a[8:11], v[128:131]\n"
    "v_mfma_f32_16x16x32_bf16 v[128:131], a[208:211], a[12:15], v[128:131]\n"
    "v_mfma_f32_16x16x32_bf16 v[128:131], a[212:215], a[16:19], v[128:131]\n"
    "v_mfma_f32_16x16x32_bf16 v[128:131], a[216:219], a[20:23], v[128:131]\n"
    "v_mfma_f32_16x16x32_bf16 v[128:131], a[220:223], a[24:27], v[128:131]\n"
    "v_mfma_f32_16x16x32_bf16 v[128:131], a[224:227], a[28:31], v[128:131]\n"
    "s_add_u32 s28, s28, 25165824\n"
    "s_addc_u32 s29, s29, 0\n"
    "s_cmp_lt_u32 s98, 3\n"
    "s_cselect_b32 s30, s34, 0\n"
    "v_max3_f32 v97, v116, v117, v118\n"
    "v_max3_f32 v97, v97, v119, v120\n"
    "v_max3_f32 v97, v97, v121, v122\n"
    "v_max3_f32 v97, v97, v123, v124\n"
    "v_max3_f32 v97, v97, v125, v126\n"
    "v_max3_f32 v97, v97, v127, v128\n"
    "v_max3_f32 v97, v97, v129, v130\n"
    "v_max_f32 v97, v97, v131\n"
    "v_mov_b32 v100, v97\n"
    "s_nop 1\n"
    "v_permlane32_swap_b32 v97, v100\n"
    "v_max_f32 v97, v97, v100\n"
    "v_mov_b32 v100, v97\n"
    "s_nop 1\n"
    "v_permlane16_swap_b32 v97, v100\n"
    "v_max_f32 v97, v97, v100\n"
    "v_mul_f32 v91, s99, v97\n"
    "v_fma_f32 v116, v116, s96, v91\n"
    "v_fma_f32 v117, v117, s96, v91\n"
    "v_fma_f32 v118, v118, s96, v91\n"
    "v_fma_f32 v119, v119, s96, v91\n"
    "v_fma_f32 v120, v120, s96, v91\n"
    "v_fma_f32 v121, v121, s96, v91\n"
    "v_fma_f32 v122, v122, s96, v91\n"
    "v_fma_f32 v123, v123, s96, v91\n"
    "v_fma_f32 v124, v124, s96, v91\n"
    "v_fma_f32 v125, v125, s96, v91\n"
    "v_fma_f32 v126, v126, s96, v91\n"
    "v_fma_f32 v127, v127, s96, v91\n"
    "v_fma_f32 v128, v128, s96, v91\n"
    "v_fma_f32 v129, v129, s96, v91\n"
    "v_fma_f32 v130, v130, s96, v91\n"
    "v_fma_f32 v131, v131, s96, v91\n"
    "v_exp_f32 v116, v116\n"
    "v_exp_f32 v117, v117\n"
    "v_exp_f32 v118, v118\n"
    "v_exp_f32 v119, v119\n"
    "v_exp_f32 v120, v120\n"
    "v_exp_f32 v121, v121\n"
    "v_exp_f32 v122, v122\n"
    "v_exp_f32 v123, v123\n"
    "v_exp_f32 v124, v124\n"
    "v_exp_f32 v125, v125\n"
    "v_exp_f32 v126, v126\n"
    "v_exp_f32 v127, v127\n"
    "v_exp_f32 v128, v128\n"
    "v_exp_f32 v129, v129\n"
    "v_exp_f32 v130, v130\n"
    "v_exp_f32 v131, v131\n"
    "v_cvt_pk_bf16_f32 v24, v116, v117\n"
    "v_cvt_pk_bf16_f32 v25, v118, v119\n"
    "v_cvt_pk_bf16_f32 v26, v120, v121\n"
    "v_cvt_pk_bf16_f32 v27, v122, v123\n"
    "v_cvt_pk_bf16_f32 v28, v124, v125\n"
    "v_cvt_pk_bf16_f32 v29, v126, v127\n"
    "v_cvt_pk_bf16_f32 v30, v128, v129\n"
    "v_cvt_pk_bf16_f32 v31, v130, v131\n"
    "s_waitcnt vmcnt(8)\n"
    "ds_read_b128 a[32:35], v108 offset:8192\n"
    "ds_read_b128 a[36:39], v109 offset:8192\n"
    "ds_read_b128 a[40:43], v110 offset:8192\n"
    "ds_read_b128 a[44:47], v111 offset:8192\n"
    "ds_read_b128 a[48:51], v108 offset:8448\n"
    "ds_read_b128 a[52:55], v109 offset:8448\n"
    "ds_read_b128 a[56:59], v110 offset:8448\n"
    "ds_read_b128 a[60:63], v111 offset:8448\n"
    "s_waitcnt lgkmcnt(7)\n"
    "v_mfma_f32_16x16x32_bf16 v[132:135], a[96:99], a[32:35], 0\n"
    "s_add_u32 m0, s91, 0\n"
    "s_waitcnt lgkmcnt(6)\n"
    "v_mfma_f32_16x16x32_bf16 v[132:135], a[100:103], a[36:39], v[132:135]\n"
    "buffer_load_dwordx4 v80, s[20:23], s44 offen lds\n"
    "s_waitcnt lgkmcnt(5)\n"
    "v_mfma_f32_16x16x32_bf16 v[132:135], a[104:107], a[40:43], v[132:135]\n"
    "s_waitcnt lgkmcnt(4)\n"
    "v_mfma_f32_16x16x32_bf16 v[132:135], a[108:111], a[44:47], v[132:135]\n"
    "buffer_load_dwordx4 v81, s[20:23], s44 offen offset:1024 nt lds\n"
    "s_waitcnt lgkmcnt(3)\n"
    "v_mfma_f32_16x16x32_bf16 v[132:135], a[112:115], a[48:51], v[132:135]\n"
    "s_waitcnt lgkmcnt(2)\n"
    "v_mfma_f32_16x16x32_bf16 v[132:135], a[116:119], a[52:55], v[132:135]\n"
    "buffer_load_dwordx4 v82, s[24:27], s68 offen offset:2048 nt lds\n"
    "s_waitcnt lgkmcnt(1)\n"
    "v_mfma_f32_16x16x32_bf16 v[132:135], a[120:123], a[56:59], v[132:135]\n"
    "s_waitcnt lgkmcnt(0)\n"
    "v_mfma_f32_16x16x32_bf16 v[132:135], a[124:127], a[60:63], v[132:135]\n"
    "buffer_load_dwordx4 v83, s[24:27], s68 offen offset:3072 nt lds\n"
    "v_mfma_f32_16x16x32_bf16 v[136:139], a[128:131], a[32:35], 0\n"
    "s_add_u32 m0, s91, 16384\n"
    "v_mfma_f32_16x16x32_bf16 v[136:139], a[132:135], a[36:39], v[136:139]\n"
    "v_mfma_f32_16x16x32_bf16 v[136:139], a[136:139], a[40:43], v[136:139]\n"
    "buffer_load_dwordx4 v80, s[20:23], s45 offen lds\n"
    "v_mfma_f32_16x16x32_bf16 v[136:139], a[140:143], a[44:47], v[136:139]\n"
    "v_mfma_f32_16x16x32_bf16 v[136:139], a[144:147], a[48:51], v[136:139]\n"
    "buffer_load_dwordx4 v81, s[20:23], s45 offen offset:1024 nt lds\n"
    "v_mfma_f32_16x16x32_bf16 v[136:139], a[148:151], a[52:55], v[136:139]\n"
    "v_mfma_f32_16x16x32_bf16 v[136:139], a[152:155], a[56:59], v[136:139]\n"
    "buffer_load_dwordx4 v82, s[24:27], s69 offen offset:2048 nt lds\n"
    "v_mfma_f32_16x16x32_bf16 v[136:139], a[156:159], a[60:63], v[136:139]\n"
    "v_mfma_f32_16x16x32_bf16 v[140:143], a[164:167], a[32:35], 0\n"
    "buffer_load_dwordx4 v83, s[24:27], s69 offen offset:3072 nt lds\n"
    "v_mfma_f32_16x16x32_bf16 v[140:143], a[168:171], a[36:39], v[140:143]\n"
    "v_mfma_f32_16x16x32_bf16 v[140:143], a[172:175], a[40:43], v[140:143]\n"
    "v_mfma_f32_16x16x32_bf16 v[140:143], a[176:179], a[44:47], v[140:143]\n"
    "v_mfma_f32_16x16x32_bf16 v[140:143], a[180:183], a[48:51], v[140:143]\n"
    "v_mfma_f32_16x16x32_bf16 v[140:143], a[184:187], a[52:55], v[140:143]\n"
    "v_mfma_f32_16x16x32_bf16 v[140:143], a[188:191], a[56:59], v[140:143]\n"
    "v_mfma_f32_16x16x32_bf16 v[140:143], a[192:195], a[60:63], v[140:143]\n"
    "v_mfma_f32_16x16x32_bf16 v[144:147], a[196:199], a[32:35], 0\n"
    "v_mfma_f32_16x16x32_bf16 v[144:147], a[200:203], a[36:39], v[144:147]\n"
    "v_mfma_f32_16x16x32_bf16 v[144:147], a[204:207], a[40:43], v[144:147]\n"
    "v_mfma_f32_16x16x32_bf16 v[144:147], a[208:211], a[44:47], v[144:147]\n"
    "v_mfma_f32_16x16x32_bf16 v[144:147], a[212:215], a[48:51], v[144:147]\n"
    "v_mfma_f32_16x16x32_bf16 v[144:147], a[216:219], a[52:55], v[144:147]\n"
    "v_mfma_f32_16x16x32_bf16 v[144:147], a[220:223], a[56:59], v[144:147]\n"
    "v_mfma_f32_16x16x32_bf16 v[144:147], a[224:227], a[60:63], v[144:147]\n"
    "v_max3_f32 v97, v132, v133, v134\n"
    "v_max3_f32 v97, v97, v135, v136\n"
    "v_max3_f32 v97, v97, v137, v138\n"
    "v_max3_f32 v97, v97, v139, v140\n"
    "v_max3_f32 v97, v97, v141, v142\n"
    "s_nop 2\n"
    "v_max3_f32 v97, v97, v143, v144\n"
    "v_max3_f32 v97, v97, v145, v146\n"
    "v_max_f32 v97, v97, v147\n"
    "v_mov_b32 v100, v97\n"
    "s_nop 1\n"
    "v_permlane32_swap_b32 v97, v100\n"
    "v_max_f32 v97, v97, v100\n"
    "v_mov_b32 v100, v97\n"
    "s_nop 1\n"
    "v_permlane16_swap_b32 v97, v100\n"
    "v_max_f32 v97, v97, v100\n"
    "v_mul_f32 v92, s99, v97\n"
    "v_fma_f32 v132, v132, s96, v92\n"
    "v_fma_f32 v133, v133, s96, v92\n"
    "v_fma_f32 v134, v134, s96, v92\n"
    "v_fma_f32 v135, v135, s96, v92\n"
    "v_fma_f32 v136, v136, s96, v92\n"
    "v_fma_f32 v137, v137, s96, v92\n"
    "v_fma_f32 v138, v138, s96, v92\n"
    "v_fma_f32 v139, v139, s96, v92\n"
    "v_fma_f32 v140, v140, s96, v92\n"
    "v_fma_f32 v141, v141, s96, v92\n"
    "v_fma_f32 v142, v142, s96, v92\n"
    "v_fma_f32 v143, v143, s96, v92\n"
    "v_fma_f32 v144, v144, s96, v92\n"
    "v_fma_f32 v145, v145, s96, v92\n"
    "v_fma_f32 v146, v146, s96, v92\n"
    "v_fma_f32 v147, v147, s96, v92\n"
    "v_exp_f32 v132, v132\n"
    "v_exp_f32 v133, v133\n"
    "v_exp_f32 v134, v134\n"
    "v_exp_f32 v135, v135\n"
    "v_exp_f32 v136, v136\n"
    "v_exp_f32 v137, v137\n"
    "v_exp_f32 v138, v138\n"
    "v_exp_f32 v139, v139\n"
    "v_exp_f32 v140, v140\n"
    "v_exp_f32 v141, v141\n"
    "v_exp_f32 v142, v142\n"
    "v_exp_f32 v143, v143\n"
    "v_exp_f32 v144, v144\n"
    "v_exp_f32 v145, v145\n"
    "v_exp_f32 v146, v146\n"
    "v_exp_f32 v147, v147\n"
    "v_cvt_pk_bf16_f32 v32, v132, v133\n"
    "v_cvt_pk_bf16_f32 v33, v134, v135\n"
    "v_cvt_pk_bf16_f32 v34, v136, v137\n"
    "v_cvt_pk_bf16_f32 v35, v138, v139\n"
    "v_cvt_pk_bf16_f32 v36, v140, v141\n"
    "v_cvt_pk_bf16_f32 v37, v142, v143\n"
    "v_cvt_pk_bf16_f32 v38, v144, v145\n"
    "v_cvt_pk_bf16_f32 v39, v146, v147\n"
    "s_waitcnt vmcnt(8)\n"
    "ds_read_b128 a[64:67], v108 offset:16384\n"
    "ds_read_b128 a[68:71], v109 offset:16384\n"
    "ds_read_b128 a[72:75], v110 offset:16384\n"
    "ds_read_b128 a[76:79], v111 offset:16384\n"
    "ds_read_b128 a[80:83], v108 offset:16640\n"
    "ds_read_b128 a[84:87], v109 offset:16640\n"
    "ds_read_b128 a[88:91], v110 offset:16640\n"
    "ds_read_b128 a[92:95], v111 offset:16640\n"
    "s_waitcnt lgkmcnt(7)\n"
    "v_mfma_f32_16x16x32_bf16 v[148:151], a[96:99], a[64:67], 0\n"
    "s_add_u32 m0, s91, 32768\n"
    "s_waitcnt lgkmcnt(6)\n"
    "v_mfma_f32_16x16x32_bf16 v[148:151], a[100:103], a[68:71], v[148:151]\n"
    "buffer_load_dwordx4 v80, s[20:23], s46 offen lds\n"
    "s_waitcnt lgkmcnt(5)\n"
    "v_mfma_f32_16x16x32_bf16 v[148:151], a[104:107], a[72:75], v[148:151]\n"
    "s_waitcnt lgkmcnt(4)\n"
    "v_mfma_f32_16x16x32_bf16 v[148:151], a[108:111], a[76:79], v[148:151]\n"
    "buffer_load_dwordx4 v81, s[20:23], s46 offen offset:1024 nt lds\n"
    "s_waitcnt lgkmcnt(3)\n"
    "v_mfma_f32_16x16x32_bf16 v[148:151], a[112:115], a[80:83], v[148:151]\n"
    "s_waitcnt lgkmcnt(2)\n"
    "v_mfma_f32_16x16x32_bf16 v[148:151], a[116:119], a[84:87], v[148:151]\n"
    "buffer_load_dwordx4 v82, s[24:27], s70 offen offset:2048 nt lds\n"
    "s_waitcnt lgkmcnt(1)\n"
    "v_mfma_f32_16x16x32_bf16 v[148:151], a[120:123], a[88:91], v[148:151]\n"
    "s_waitcnt lgkmcnt(0)\n"
    "v_mfma_f32_16x16x32_bf16 v[148:151], a[124:127], a[92:95], v[148:151]\n"
    "buffer_load_dwordx4 v83, s[24:27], s70 offen offset:3072 nt lds\n"
    "v_mfma_f32_16x16x32_bf16 v[152:155], a[128:131], a[64:67], 0\n"
    "s_add_u32 m0, s91, 49152\n"
    "v_mfma_f32_16x16x32_bf16 v[152:155], a[132:135], a[68:71], v[152:155]\n"
    "v_mfma_f32_16x16x32_bf16 v[152:155], a[136:139], a[72:75], v[152:155]\n"
    "buffer_load_dwordx4 v80, s[20:23], s47 offen lds\n"
    "v_mfma_f32_16x16x32_bf16 v[152:155], a[140:143], a[76:79], v[152:155]\n"
    "v_mfma_f32_16x16x32_bf16 v[152:155], a[144:147], a[80:83], v[152:155]\n"
    "buffer_load_dwordx4 v81, s[20:23], s47 offen offset:1024 nt lds\n"
    "v_mfma_f32_16x16x32_bf16 v[152:155], a[148:151], a[84:87], v[152:155]\n"
    "v_mfma_f32_16x16x32_bf16 v[152:155], a[152:155], a[88:91], v[152:155]\n"
    "buffer_load_dwordx4 v82, s[24:27], s71 offen offset:2048 nt lds\n"
    "v_mfma_f32_16x16x32_bf16 v[152:155], a[156:159], a[92:95], v[152:155]\n"
    "v_mfma_f32_16x16x32_bf16 v[156:159], a[164:167], a[64:67], 0\n"
    "buffer_load_dwordx4 v83, s[24:27], s71 offen offset:3072 nt lds\n"
    "v_mfma_f32_16x16x32_bf16 v[156:159], a[168:171], a[68:71], v[156:159]\n"
    "v_mfma_f32_16x16x32_bf16 v[156:159], a[172:175], a[72:75], v[156:159]\n"
    "v_mfma_f32_16x16x32_bf16 v[156:159], a[176:179], a[76:79], v[156:159]\n"
    "v_mfma_f32_16x16x32_bf16 v[156:159], a[180:183], a[80:83], v[156:159]\n"
    "v_mfma_f32_16x16x32_bf16 v[156:159], a[184:187], a[84:87], v[156:159]\n"
    "v_mfma_f32_16x16x32_bf16 v[156:159], a[188:191], a[88:91], v[156:159]\n"
    "v_mfma_f32_16x16x32_bf16 v[156:159], a[192:195], a[92:95], v[156:159]\n"
    "v_mfma_f32_16x16x32_bf16 v[160:163], a[196:199], a[64:67], 0\n"
    "v_mfma_f32_16x16x32_bf16 v[160:163], a[200:203], a[68:71], v[160:163]\n"
    "v_mfma_f32_16x16x32_bf16 v[160:163], a[204:207], a[72:75], v[160:163]\n"
    "v_mfma_f32_16x16x32_bf16 v[160:163], a[208:211], a[76:79], v[160:163]\n"
    "v_mfma_f32_16x16x32_bf16 v[160:163], a[212:215], a[80:83], v[160:163]\n"
    "v_mfma_f32_16x16x32_bf16 v[160:163], a[216:219], a[84:87], v[160:163]\n"
    "v_mfma_f32_16x16x32_bf16 v[160:163], a[220:223], a[88:91], v[160:163]\n"
    "v_mfma_f32_16x16x32_bf16 v[160:163], a[224:227], a[92:95], v[160:163]\n"
    "s_waitcnt vmcnt(12)\n"
    "s_barrier\n"
    "ds_read_b128 a[96:99], v72 offset:0\n"
    "ds_read_b128 a[100:103], v73 offset:0\n"
    "ds_read_b128 a[104:107], v74 offset:0\n"
    "ds_read_b128 a[108:111], v75 offset:0\n"
    "ds_read_b128 a[112:115], v72 offset:256\n"
    "ds_read_b128 a[116:119], v73 offset:256\n"
    "ds_read_b128 a[120:123], v74 offset:256\n"
    "ds_read_b128 a[124:127], v75 offset:256\n"
    "v_max3_f32 v97, v148, v149, v150\n"
    "v_max3_f32 v97, v97, v151, v152\n"
    "v_max3_f32 v97, v97, v153, v154\n"
    "v_max3_f32 v97, v97, v155, v156\n"
    "v_max3_f32 v97, v97, v157, v158\n"
    "v_max3_f32 v97, v97, v159, v160\n"
    "v_max3_f32 v97, v97, v161, v162\n"
    "v_max_f32 v97, v97, v163\n"
    "v_mov_b32 v100, v97\n"
    "s_waitcnt lgkmcnt(7)\n"
    "v_mfma_f32_16x16x32_bf16 v[0:3], a[96:99], a[0:3], 0\n"
    "ds_read_b64_tr_b16 a[128:129], v76 offset:0\n"
    "v_permlane32_swap_b32 v97, v100\n"
    "s_waitcnt lgkmcnt(7)\n"
    "v_mfma_f32_16x16x32_bf16 v[0:3], a[100:103], a[4:7], v[0:3]\n"
    "ds_read_b64_tr_b16 a[130:131], v76 offset:4096\n"
    "v_max_f32 v97, v97, v100\n"
    "s_waitcnt lgkmcnt(7)\n"
    "v_mfma_f32_16x16x32_bf16 v[0:3], a[104:107], a[8:11], v[0:3]\n"
    "ds_read_b64_tr_b16 a[132:133], v76 offset:8192\n"
    "v_mov_b32 v100, v97\n"
    "s_waitcnt lgkmcnt(7)\n"
    "v_mfma_f32_16x16x32_bf16 v[0:3], a[108:111], a[12:15], v[0:3]\n"
    "ds_read_b64_tr_b16 a[134:135], v76 offset:12288\n"
    "v_permlane16_swap_b32 v97, v100\n"
    "s_waitcnt lgkmcnt(7)\n"
    "v_mfma_f32_16x16x32_bf16 v[0:3], a[112:115], a[16:19], v[0:3]\n"
    "ds_read_b64_tr_b16 a[136:137], v77 offset:0\n"
    "v_max_f32 v97, v97, v100\n"
    "s_waitcnt lgkmcnt(7)\n"
    "v_mfma_f32_16x16x32_bf16 v[0:3], a[116:119], a[20:23], v[0:3]\n"
    "ds_read_b64_tr_b16 a[138:139], v77 offset:4096\n"
    "v_mul_f32 v93, s99, v97\n"
    "s_waitcnt lgkmcnt(7)\n"
    "v_mfma_f32_16x16x32_bf16 v[0:3], a[120:123], a[24:27], v[0:3]\n"
    "ds_read_b64_tr_b16 a[140:141], v77 offset:8192\n"
    "v_fma_f32 v148, v148, s96, v93\n"
    "s_waitcnt lgkmcnt(7)\n"
    "v_mfma_f32_16x16x32_bf16 v[0:3], a[124:127], a[28:31], v[0:3]\n"
    "ds_read_b64_tr_b16 a[142:143], v77 offset:12288\n"
    "s_add_u32 m0, s35, 0\n"
    "v_mfma_f32_16x16x32_bf16 v[4:7], a[96:99], a[32:35], 0\n"
    "ds_read_b64_tr_b16 a[144:145], v78 offset:0\n"
    "v_fma_f32 v149, v149, s96, v93\n"
    "v_mfma_f32_16x16x32_bf16 v[4:7], a[100:103], a[36:39], v[4:7]\n"
    "buffer_load_dwordx4 v84, s[28:31], s40 offen nt lds\n"
    "v_mfma_f32_16x16x32_bf16 v[4:7], a[104:107], a[40:43], v[4:7]\n"
    "buffer_load_dwordx4 v85, s[28:31], s40 offen offset:1024 nt lds\n"
    "v_mfma_f32_16x16x32_bf16 v[4:7], a[108:111], a[44:47], v[4:7]\n"
    "buffer_load_dwordx4 v86, s[28:31], s40 offen offset:2048 nt lds\n"
    "v_mfma_f32_16x16x32_bf16 v[4:7], a[112:115], a[48:51], v[4:7]\n"
    "buffer_load_dwordx4 v87, s[28:31], s40 offen offset:3072 nt lds\n"
    "v_mfma_f32_16x16x32_bf16 v[4:7], a[116:119], a[52:55], v[4:7]\n"
    "ds_read_b64_tr_b16 a[146:147], v78 offset:4096\n"
    "s_add_u32 m0, s35, 4096\n"
    "v_mfma_f32_16x16x32_bf16 v[4:7], a[120:123], a[56:59], v[4:7]\n"
    "buffer_load_dwordx4 v84, s[28:31], s88 offen nt lds\n"
    "v_mfma_f32_16x16x32_bf16 v[4:7], a[124:127], a[60:63], v[4:7]\n"
    "ds_read_b64_tr_b16 a[148:149], v78 offset:8192\n"
    "ds_read_b64_tr_b16 a[150:151], v78 offset:12288\n"
    "v_mfma_f32_16x16x32_bf16 v[8:11], a[96:99], a[64:67], 0\n"
    "ds_read_b64_tr_b16 a[152:153], v79 offset:0\n"
    "ds_read_b64_tr_b16 a[154:155], v79 offset:4096\n"
    "v_mfma_f32_16x16x32_bf16 v[8:11], a[100:103], a[68:71], v[8:11]\n"
    "ds_read_b64_tr_b16 a[156:157], v79 offset:8192\n"
    "ds_read_b64_tr_b16 a[158:159], v79 offset:12288\n"
    "v_mfma_f32_16x16x32_bf16 v[8:11], a[104:107], a[72:75], v[8:11]\n"
    "v_fma_f32 v150, v150, s96, v93\n"
    "v_fma_f32 v151, v151, s96, v93\n"
    "v_mfma_f32_16x16x32_bf16 v[8:11], a[108:111], a[76:79], v[8:11]\n"
    "v_fma_f32 v152, v152, s96, v93\n"
    "v_fma_f32 v153, v153, s96, v93\n"
    "v_mfma_f32_16x16x32_bf16 v[8:11], a[112:115], a[80:83], v[8:11]\n"
    "v_fma_f32 v154, v154, s96, v93\n"
    "v_fma_f32 v155, v155, s96, v93\n"
    "v_mfma_f32_16x16x32_bf16 v[8:11], a[116:119], a[84:87], v[8:11]\n"
    "v_fma_f32 v156, v156, s96, v93\n"
    "v_fma_f32 v157, v157, s96, v93\n"
    "v_mfma_f32_16x16x32_bf16 v[8:11], a[120:123], a[88:91], v[8:11]\n"
    "v_fma_f32 v158, v158, s96, v93\n"
    "v_fma_f32 v159, v159, s96, v93\n"
    "v_mfma_f32_16x16x32_bf16 v[8:11], a[124:127], a[92:95], v[8:11]\n"
    "v_fma_f32 v160, v160, s96, v93\n"
    "v_fma_f32 v161, v161, s96, v93\n"
    "v_fma_f32 v162, v162, s96, v93\n"
    "v_fma_f32 v163, v163, s96, v93\n"
    "v_exp_f32 v148, v148\n"
    "v_exp_f32 v149, v149\n"
    "v_exp_f32 v150, v150\n"
    "v_exp_f32 v151, v151\n"
    "v_exp_f32 v152, v152\n"
    "v_exp_f32 v153, v153\n"
    "v_exp_f32 v154, v154\n"
    "v_exp_f32 v155, v155\n"
    "v_exp_f32 v156, v156\n"
    "v_exp_f32 v157, v157\n"
    "v_exp_f32 v158, v158\n"
    "v_exp_f32 v159, v159\n"
    "v_exp_f32 v160, v160\n"
    "v_exp_f32 v161, v161\n"
    "v_exp_f32 v162, v162\n"
    "v_exp_f32 v163, v163\n"
    "v_cvt_pk_bf16_f32 v40, v148, v149\n"
    "v_cvt_pk_bf16_f32 v41, v150, v151\n"
    "v_cvt_pk_bf16_f32 v42, v152, v153\n"
    "v_cvt_pk_bf16_f32 v43, v154, v155\n"
    "v_cvt_pk_bf16_f32 v44, v156, v157\n"
    "v_cvt_pk_bf16_f32 v45, v158, v159\n"
    "v_cvt_pk_bf16_f32 v46, v160, v161\n"
    "v_cvt_pk_bf16_f32 v47, v162, v163\n"
    "LOOP0_%=:\n"
    "s_waitcnt vmcnt(13) lgkmcnt(0)\n"
    "s_barrier\n"
    "v_mfma_f32_16x16x32_bf16 a[164:167], a[160:163], v[24:27], 0\n"
    "v_fma_f32 v0, v0, s96, v91\n"
    "s_add_u32 m0, s91, 0\n"
    "v_mfma_f32_16x16x32_bf16 a[164:167], a[160:163], v[28:31], a[164:167]\n"
    "buffer_load_dwordx4 v80, s[20:23], s48 offen lds\n"
    "v_mfma_f32_16x16x32_bf16 v[116:119], a[128:131], v[24:27], 0\n"
    "ds_read_b128 a[96:99], v72 offset:16384\n"
    "v_fma_f32 v1, v1, s96, v91\n"
    "v_mfma_f32_16x16x32_bf16 v[116:119], a[132:135], v[28:31], v[116:119]\n"
    "ds_read_b128 a[100:103], v73 offset:16384\n"
    "v_fma_f32 v2, v2, s96, v91\n"
    "v_mfma_f32_16x16x32_bf16 v[120:123], a[128:131], v[32:35], 0\n"
    "ds_read_b128 a[104:107], v74 offset:16384\n"
    "v_fma_f32 v3, v3, s96, v91\n"
    "v_mfma_f32_16x16x32_bf16 v[120:123], a[132:135], v[36:39], v[120:123]\n"
    "buffer_load_dwordx4 v81, s[20:23], s48 offen offset:1024 nt lds\n"
    "v_mfma_f32_16x16x32_bf16 v[124:127], a[128:131], v[40:43], 0\n"
    "ds_read_b128 a[108:111], v75 offset:16384\n"
    "v_fma_f32 v4, v4, s96, v92\n"
    "v_mfma_f32_16x16x32_bf16 v[124:127], a[132:135], v[44:47], v[124:127]\n"
    "ds_read_b128  a[228:231], v90 offset:5120\n"
    "v_fma_f32 v5, v5, s96, v92\n"
    "v_mfma_f32_16x16x32_bf16 v[128:131], a[136:139], v[24:27], 0\n"
    "ds_read_b128  a[232:235], v88 offset:6144\n"
    "v_fma_f32 v6, v6, s96, v92\n"
    "v_mfma_f32_16x16x32_bf16 v[128:131], a[140:143], v[28:31], v[128:131]\n"
    "buffer_load_dwordx4 v82, s[24:27], s72 offen offset:2048 nt lds\n"
    "v_mfma_f32_16x16x32_bf16 v[132:135], a[136:139], v[32:35], 0\n"
    "ds_read_b128 a[112:115], v72 offset:16640\n"
    "v_fma_f32 v7, v7, s96, v92\n"
    "v_mfma_f32_16x16x32_bf16 v[132:135], a[140:143], v[36:39], v[132:135]\n"
    "ds_read_b128 a[116:119], v73 offset:16640\n"
    "v_fma_f32 v8, v8, s96, v93\n"
    "v_mfma_f32_16x16x32_bf16 v[136:139], a[136:139], v[40:43], 0\n"
    "ds_read_b128 a[120:123], v74 offset:16640\n"
    "v_fma_f32 v9, v9, s96, v93\n"
    "v_mfma_f32_16x16x32_bf16 v[136:139], a[140:143], v[44:47], v[136:139]\n"
    "buffer_load_dwordx4 v83, s[24:27], s72 offen offset:3072 nt lds\n"
    "v_mfma_f32_16x16x32_bf16 v[140:143], a[144:147], v[24:27], 0\n"
    "ds_read_b128 a[124:127], v75 offset:16640\n"
    "v_fma_f32 v10, v10, s96, v93\n"
    "v_mfma_f32_16x16x32_bf16 v[140:143], a[148:151], v[28:31], v[140:143]\n"
    "v_fma_f32 v11, v11, s96, v93\n"
    "v_max3_f32 v97, v0, v1, v2\n"
    "v_mfma_f32_16x16x32_bf16 v[144:147], a[144:147], v[32:35], 0\n"
    "v_max3_f32 v97, v97, v3, v4\n"
    "v_max3_f32 v97, v97, v5, v6\n"
    "v_mfma_f32_16x16x32_bf16 v[144:147], a[148:151], v[36:39], v[144:147]\n"
    "s_waitcnt lgkmcnt(5)\n"
    "buffer_store_dwordx4 a[228:231], v89, s[36:39], s41 offen offset:2048 nt\n"
    "v_mfma_f32_16x16x32_bf16 v[148:151], a[144:147], v[40:43], 0\n"
    "v_max3_f32 v97, v97, v7, v8\n"
    "v_max3_f32 v97, v97, v9, v10\n"
    "v_mfma_f32_16x16x32_bf16 v[148:151], a[148:151], v[44:47], v[148:151]\n"
    "v_max_f32 v97, v97, v11\n"
    "s_add_u32 m0, s35, 4096\n"
    "v_mfma_f32_16x16x32_bf16 a[176:179], a[152:155], v[24:27], 0\n"
    "v_cmp_lt_f32 vcc, s97, v97\n"
    "v_mfma_f32_16x16x32_bf16 a[176:179], a[156:159], v[28:31], a[176:179]\n"
    "buffer_load_dwordx4 v85, s[28:31], s88 offen offset:1024 nt lds\n"
    "v_mfma_f32_16x16x32_bf16 a[180:183], a[152:155], v[32:35], 0\n"
    "ds_read_b64_tr_b16 a[128:129], v76 offset:16384\n"
    "ds_read_b64_tr_b16 a[130:131], v76 offset:20480\n"
    "v_mfma_f32_16x16x32_bf16 a[180:183], a[156:159], v[36:39], a[180:183]\n"
    "s_cbranch_vccnz STUBb4c0_%=\n"
    "RETb4c0_%=:\n"
    "v_mfma_f32_16x16x32_bf16 a[184:187], a[152:155], v[40:43], 0\n"
    "ds_read_b64_tr_b16 a[132:133], v76 offset:24576\n"
    "ds_read_b64_tr_b16 a[134:135], v76 offset:28672\n"
    "v_mfma_f32_16x16x32_bf16 a[184:187], a[156:159], v[44:47], a[184:187]\n"
    "s_waitcnt lgkmcnt(8)\n"
    "buffer_store_dwordx4 a[232:235], v89, s[36:39], s89 offen nt\n"
    "s_waitcnt lgkmcnt(4)\n"
    "v_mfma_f32_16x16x32_bf16 v[12:15], a[96:99], a[0:3], 0\n"
    "v_exp_f32 v0, v0\n"
    "v_mfma_f32_16x16x32_bf16 v[12:15], a[100:103], a[4:7], v[12:15]\n"
    "ds_read_b64_tr_b16 a[136:137], v77 offset:16384\n"
    "ds_read_b64_tr_b16 a[138:139], v77 offset:20480\n"
    "v_mfma_f32_16x16x32_bf16 v[12:15], a[104:107], a[8:11], v[12:15]\n"
    "v_exp_f32 v1, v1\n"
    "v_mfma_f32_16x16x32_bf16 v[12:15], a[108:111], a[12:15], v[12:15]\n"
    "buffer_load_dwordx4 v86, s[28:31], s88 offen offset:2048 nt lds\n"
    "v_mfma_f32_16x16x32_bf16 v[12:15], a[112:115], a[16:19], v[12:15]\n"
    "ds_read_b64_tr_b16 a[140:141], v77 offset:24576\n"
    "v_cvt_pk_bf16_f32 v48, v0, v1\n"
    "v_mfma_f32_16x16x32_bf16 v[12:15], a[116:119], a[20:23], v[12:15]\n"
    "ds_read_b64_tr_b16 a[142:143], v77 offset:28672\n"
    "ds_read_b64_tr_b16 a[144:145], v78 offset:16384\n"
    "v_mfma_f32_16x16x32_bf16 v[12:15], a[120:123], a[24:27], v[12:15]\n"
    "v_exp_f32 v2, v2\n"
    "v_mfma_f32_16x16x32_bf16 v[12:15], a[124:127], a[28:31], v[12:15]\n"
    "ds_read_b64_tr_b16 a[146:147], v78 offset:20480\n"
    "ds_read_b64_tr_b16 a[148:149], v78 offset:24576\n"
    "v_mfma_f32_16x16x32_bf16 v[16:19], a[96:99], a[32:35], 0\n"
    "v_exp_f32 v3, v3\n"
    "v_mfma_f32_16x16x32_bf16 v[16:19], a[100:103], a[36:39], v[16:19]\n"
    "ds_read_b64_tr_b16 a[150:151], v78 offset:28672\n"
    "v_cvt_pk_bf16_f32 v49, v2, v3\n"
    "v_mfma_f32_16x16x32_bf16 v[16:19], a[104:107], a[40:43], v[16:19]\n"
    "v_exp_f32 v4, v4\n"
    "v_mfma_f32_16x16x32_bf16 v[16:19], a[108:111], a[44:47], v[16:19]\n"
    "ds_read_b64_tr_b16 a[152:153], v79 offset:16384\n"
    "ds_read_b64_tr_b16 a[154:155], v79 offset:20480\n"
    "v_mfma_f32_16x16x32_bf16 v[16:19], a[112:115], a[48:51], v[16:19]\n"
    "v_exp_f32 v5, v5\n"
    "v_mfma_f32_16x16x32_bf16 v[16:19], a[116:119], a[52:55], v[16:19]\n"
    "ds_read_b64_tr_b16 a[156:157], v79 offset:24576\n"
    "v_cvt_pk_bf16_f32 v56, v4, v5\n"
    "v_mfma_f32_16x16x32_bf16 v[16:19], a[120:123], a[56:59], v[16:19]\n"
    "ds_read_b64_tr_b16 a[158:159], v79 offset:28672\n"
    "v_mfma_f32_16x16x32_bf16 v[16:19], a[124:127], a[60:63], v[16:19]\n"
    "v_exp_f32 v6, v6\n"
    "v_mfma_f32_16x16x32_bf16 v[20:23], a[96:99], a[64:67], 0\n"
    "v_exp_f32 v7, v7\n"
    "v_mfma_f32_16x16x32_bf16 v[20:23], a[100:103], a[68:71], v[20:23]\n"
    "v_exp_f32 v8, v8\n"
    "v_mfma_f32_16x16x32_bf16 v[20:23], a[104:107], a[72:75], v[20:23]\n"
    "v_exp_f32 v9, v9\n"
    "v_mfma_f32_16x16x32_bf16 v[20:23], a[108:111], a[76:79], v[20:23]\n"
    "v_exp_f32 v10, v10\n"
    "v_mfma_f32_16x16x32_bf16 v[20:23], a[112:115], a[80:83], v[20:23]\n"
    "v_exp_f32 v11, v11\n"
    "v_mfma_f32_16x16x32_bf16 v[20:23], a[116:119], a[84:87], v[20:23]\n"
    "v_cvt_pk_bf16_f32 v57, v6, v7\n"
    "v_cvt_pk_bf16_f32 v64, v8, v9\n"
    "v_mfma_f32_16x16x32_bf16 v[20:23], a[120:123], a[88:91], v[20:23]\n"
    "v_cvt_pk_bf16_f32 v65, v10, v11\n"
    "v_mfma_f32_16x16x32_bf16 v[20:23], a[124:127], a[92:95], v[20:23]\n"
    "s_waitcnt vmcnt(17) lgkmcnt(0)\n"
    "s_barrier\n"
    "v_mfma_f32_16x16x32_bf16 a[168:171], a[160:163], v[32:35], 0\n"
    "v_fma_f32 v12, v12, s96, v91\n"
    "s_add_u32 m0, s91, 16384\n"
    "v_mfma_f32_16x16x32_bf16 a[168:171], a[160:163], v[36:39], a[168:171]\n"
    "buffer_load_dwordx4 v80, s[20:23], s49 offen lds\n"
    "v_mfma_f32_16x16x32_bf16 v[152:155], a[128:131], v[24:27], 0\n"
    "ds_read_b128 a[96:99], v72 offset:32768\n"
    "v_fma_f32 v13, v13, s96, v91\n"
    "v_mfma_f32_16x16x32_bf16 v[152:155], a[132:135], v[28:31], v[152:155]\n"
    "ds_read_b128 a[100:103], v73 offset:32768\n"
    "v_fma_f32 v14, v14, s96, v91\n"
    "v_mfma_f32_16x16x32_bf16 v[156:159], a[128:131], v[32:35], 0\n"
    "ds_read_b128 a[104:107], v74 offset:32768\n"
    "v_fma_f32 v15, v15, s96, v91\n"
    "v_mfma_f32_16x16x32_bf16 v[156:159], a[132:135], v[36:39], v[156:159]\n"
    "buffer_load_dwordx4 v81, s[20:23], s49 offen offset:1024 nt lds\n"
    "v_mfma_f32_16x16x32_bf16 v[160:163], a[128:131], v[40:43], 0\n"
    "ds_read_b128 a[108:111], v75 offset:32768\n"
    "v_fma_f32 v16, v16, s96, v92\n"
    "v_mfma_f32_16x16x32_bf16 v[160:163], a[132:135], v[44:47], v[160:163]\n"
    "ds_read_b128  a[228:231], v90 offset:7168\n"
    "v_fma_f32 v17, v17, s96, v92\n"
    "v_mfma_f32_16x16x32_bf16 v[164:167], a[136:139], v[24:27], 0\n"
    "ds_read_b128 a[112:115], v72 offset:33024\n"
    "v_fma_f32 v18, v18, s96, v92\n"
    "v_mfma_f32_16x16x32_bf16 v[164:167], a[140:143], v[28:31], v[164:167]\n"
    "buffer_load_dwordx4 v82, s[24:27], s73 offen offset:2048 nt lds\n"
    "v_mfma_f32_16x16x32_bf16 v[168:171], a[136:139], v[32:35], 0\n"
    "ds_read_b128 a[116:119], v73 offset:33024\n"
    "v_fma_f32 v19, v19, s96, v92\n"
    "v_mfma_f32_16x16x32_bf16 v[168:171], a[140:143], v[36:39], v[168:171]\n"
    "ds_read_b128 a[120:123], v74 offset:33024\n"
    "v_fma_f32 v20, v20, s96, v93\n"
    "v_mfma_f32_16x16x32_bf16 v[172:175], a[136:139], v[40:43], 0\n"
    "v_fma_f32 v21, v21, s96, v93\n"
    "v_fma_f32 v22, v22, s96, v93\n"
    "v_mfma_f32_16x16x32_bf16 v[172:175], a[140:143], v[44:47], v[172:175]\n"
    "buffer_load_dwordx4 v83, s[24:27], s73 offen offset:3072 nt lds\n"
    "v_mfma_f32_16x16x32_bf16 v[176:179], a[144:147], v[24:27], 0\n"
    "ds_read_b128 a[124:127], v75 offset:33024\n"
    "v_fma_f32 v23, v23, s96, v93\n"
    "v_mfma_f32_16x16x32_bf16 v[176:179], a[148:151], v[28:31], v[176:179]\n"
    "v_max3_f32 v97, v12, v13, v14\n"
    "v_max3_f32 v97, v97, v15, v16\n"
    "v_mfma_f32_16x16x32_bf16 v[180:183], a[144:147], v[32:35], 0\n"
    "v_max3_f32 v97, v97, v17, v18\n"
    "v_max3_f32 v97, v97, v19, v20\n"
    "v_mfma_f32_16x16x32_bf16 v[180:183], a[148:151], v[36:39], v[180:183]\n"
    "s_waitcnt lgkmcnt(4)\n"
    "buffer_store_dwordx4 a[228:231], v89, s[36:39], s89 offen offset:2048 nt\n"
    "v_mfma_f32_16x16x32_bf16 v[184:187], a[144:147], v[40:43], 0\n"
    "v_max3_f32 v97, v97, v21, v22\n"
    "v_max_f32 v97, v97, v23\n"
    "v_mfma_f32_16x16x32_bf16 v[184:187], a[148:151], v[44:47], v[184:187]\n"
    "v_cmp_lt_f32 vcc, s97, v97\n"
    "s_add_u32 m0, s35, 4096\n"
    "v_mfma_f32_16x16x32_bf16 a[188:191], a[152:155], v[24:27], 0\n"
    "v_mfma_f32_16x16x32_bf16 a[188:191], a[156:159], v[28:31], a[188:191]\n"
    "buffer_load_dwordx4 v87, s[28:31], s88 offen offset:3072 nt lds\n"
    "v_mfma_f32_16x16x32_bf16 a[192:195], a[152:155], v[32:35], 0\n"
    "ds_read_b64_tr_b16 a[128:129], v76 offset:32768\n"
    "s_cbranch_vccnz STUBb5c0_%=\n"
    "RETb5c0_%=:\n"
    "v_mfma_f32_16x16x32_bf16 a[192:195], a[156:159], v[36:39], a[192:195]\n"
    "ds_read_b64_tr_b16 a[130:131], v76 offset:36864\n"
    "ds_read_b64_tr_b16 a[132:133], v76 offset:40960\n"
    "v_mfma_f32_16x16x32_bf16 a[196:199], a[152:155], v[40:43], 0\n"
    "v_exp_f32 v12, v12\n"
    "v_mfma_f32_16x16x32_bf16 a[196:199], a[156:159], v[44:47], a[196:199]\n"
    "ds_read_b64_tr_b16 a[134:135], v76 offset:45056\n"
    "s_waitcnt lgkmcnt(4)\n"
    "v_mfma_f32_16x16x32_bf16 v[0:3], a[96:99], a[0:3], 0\n"
    "v_exp_f32 v13, v13\n"
    "v_mfma_f32_16x16x32_bf16 v[0:3], a[100:103], a[4:7], v[0:3]\n"
    "ds_read_b64_tr_b16 a[136:137], v77 offset:32768\n"
    "v_cvt_pk_bf16_f32 v50, v12, v13\n"
    "v_mfma_f32_16x16x32_bf16 v[0:3], a[104:107], a[8:11], v[0:3]\n"
    "ds_read_b64_tr_b16 a[138:139], v77 offset:36864\n"
    "ds_read_b64_tr_b16 a[140:141], v77 offset:40960\n"
    "v_mfma_f32_16x16x32_bf16 v[0:3], a[108:111], a[12:15], v[0:3]\n"
    "v_exp_f32 v14, v14\n"
    "v_mfma_f32_16x16x32_bf16 v[0:3], a[112:115], a[16:19], v[0:3]\n"
    "ds_read_b64_tr_b16 a[142:143], v77 offset:45056\n"
    "v_mfma_f32_16x16x32_bf16 v[0:3], a[116:119], a[20:23], v[0:3]\n"
    "v_exp_f32 v15, v15\n"
    "v_mfma_f32_16x16x32_bf16 v[0:3], a[120:123], a[24:27], v[0:3]\n"
    "ds_read_b64_tr_b16 a[144:145], v78 offset:32768\n"
    "v_cvt_pk_bf16_f32 v51, v14, v15\n"
    "v_mfma_f32_16x16x32_bf16 v[0:3], a[124:127], a[28:31], v[0:3]\n"
    "ds_read_b64_tr_b16 a[146:147], v78 offset:36864\n"
    "ds_read_b64_tr_b16 a[148:149], v78 offset:40960\n"
    "v_mfma_f32_16x16x32_bf16 v[4:7], a[96:99], a[32:35], 0\n"
    "v_exp_f32 v16, v16\n"
    "v_mfma_f32_16x16x32_bf16 v[4:7], a[100:103], a[36:39], v[4:7]\n"
    "ds_read_b64_tr_b16 a[150:151], v78 offset:45056\n"
    "v_mfma_f32_16x16x32_bf16 v[4:7], a[104:107], a[40:43], v[4:7]\n"
    "v_exp_f32 v17, v17\n"
    "v_mfma_f32_16x16x32_bf16 v[4:7], a[108:111], a[44:47], v[4:7]\n"
    "ds_read_b64_tr_b16 a[152:153], v79 offset:32768\n"
    "v_cvt_pk_bf16_f32 v58, v16, v17\n"
    "v_mfma_f32_16x16x32_bf16 v[4:7], a[112:115], a[48:51], v[4:7]\n"
    "ds_read_b64_tr_b16 a[154:155], v79 offset:36864\n"
    "ds_read_b64_tr_b16 a[156:157], v79 offset:40960\n"
    "v_mfma_f32_16x16x32_bf16 v[4:7], a[116:119], a[52:55], v[4:7]\n"
    "v_exp_f32 v18, v18\n"
    "v_mfma_f32_16x16x32_bf16 v[4:7], a[120:123], a[56:59], v[4:7]\n"
    "ds_read_b64_tr_b16 a[158:159], v79 offset:45056\n"
    "v_mfma_f32_16x16x32_bf16 v[4:7], a[124:127], a[60:63], v[4:7]\n"
    "v_exp_f32 v19, v19\n"
    "v_mfma_f32_16x16x32_bf16 v[8:11], a[96:99], a[64:67], 0\n"
    "v_exp_f32 v20, v20\n"
    "v_mfma_f32_16x16x32_bf16 v[8:11], a[100:103], a[68:71], v[8:11]\n"
    "v_exp_f32 v21, v21\n"
    "v_mfma_f32_16x16x32_bf16 v[8:11], a[104:107], a[72:75], v[8:11]\n"
    "v_exp_f32 v22, v22\n"
    "v_mfma_f32_16x16x32_bf16 v[8:11], a[108:111], a[76:79], v[8:11]\n"
    "v_exp_f32 v23, v23\n"
    "v_mfma_f32_16x16x32_bf16 v[8:11], a[112:115], a[80:83], v[8:11]\n"
    "v_cvt_pk_bf16_f32 v59, v18, v19\n"
    "v_cvt_pk_bf16_f32 v66, v20, v21\n"
    "v_mfma_f32_16x16x32_bf16 v[8:11], a[116:119], a[84:87], v[8:11]\n"
    "v_cvt_pk_bf16_f32 v67, v22, v23\n"
    "v_mfma_f32_16x16x32_bf16 v[8:11], a[120:123], a[88:91], v[8:11]\n"
    "v_mfma_f32_16x16x32_bf16 v[8:11], a[124:127], a[92:95], v[8:11]\n"
    "s_waitcnt vmcnt(18) lgkmcnt(0)\n"
    "s_barrier\n"
    "v_mfma_f32_16x16x32_bf16 a[172:175], a[160:163], v[40:43], 0\n"
    "v_fma_f32 v0, v0, s96, v91\n"
    "s_add_u32 m0, s91, 32768\n"
    "v_mfma_f32_16x16x32_bf16 a[172:175], a[160:163], v[44:47], a[172:175]\n"
    "buffer_load_dwordx4 v80, s[20:23], s50 offen lds\n"
    "v_mfma_f32_16x16x32_bf16 v[188:191], a[128:131], v[24:27], 0\n"
    "ds_read_b128 a[96:99], v72 offset:49152\n"
    "v_fma_f32 v1, v1, s96, v91\n"
    "v_mfma_f32_16x16x32_bf16 v[188:191], a[132:135], v[28:31], v[188:191]\n"
    "ds_read_b128 a[100:103], v73 offset:49152\n"
    "v_fma_f32 v2, v2, s96, v91\n"
    "v_mfma_f32_16x16x32_bf16 v[192:195], a[128:131], v[32:35], 0\n"
    "ds_read_b128 a[104:107], v74 offset:49152\n"
    "v_fma_f32 v3, v3, s96, v91\n"
    "v_mfma_f32_16x16x32_bf16 v[192:195], a[132:135], v[36:39], v[192:195]\n"
    "buffer_load_dwordx4 v81, s[20:23], s50 offen offset:1024 nt lds\n"
    "v_mfma_f32_16x16x32_bf16 v[196:199], a[128:131], v[40:43], 0\n"
    "ds_read_b128 a[108:111], v75 offset:49152\n"
    "v_fma_f32 v4, v4, s96, v92\n"
    "v_mfma_f32_16x16x32_bf16 v[196:199], a[132:135], v[44:47], v[196:199]\n"
    "ds_read_b128  a[228:231], v88 offset:8192\n"
    "v_fma_f32 v5, v5, s96, v92\n"
    "v_mfma_f32_16x16x32_bf16 v[200:203], a[136:139], v[24:27], 0\n"
    "ds_read_b128 a[112:115], v72 offset:49408\n"
    "v_fma_f32 v6, v6, s96, v92\n"
    "v_mfma_f32_16x16x32_bf16 v[200:203], a[140:143], v[28:31], v[200:203]\n"
    "buffer_load_dwordx4 v82, s[24:27], s74 offen offset:2048 nt lds\n"
    "v_mfma_f32_16x16x32_bf16 v[204:207], a[136:139], v[32:35], 0\n"
    "ds_read_b128 a[116:119], v73 offset:49408\n"
    "v_fma_f32 v7, v7, s96, v92\n"
    "v_mfma_f32_16x16x32_bf16 v[204:207], a[140:143], v[36:39], v[204:207]\n"
    "ds_read_b128 a[120:123], v74 offset:49408\n"
    "v_fma_f32 v8, v8, s96, v93\n"
    "v_mfma_f32_16x16x32_bf16 v[208:211], a[136:139], v[40:43], 0\n"
    "v_fma_f32 v9, v9, s96, v93\n"
    "v_fma_f32 v10, v10, s96, v93\n"
    "v_mfma_f32_16x16x32_bf16 v[208:211], a[140:143], v[44:47], v[208:211]\n"
    "buffer_load_dwordx4 v83, s[24:27], s74 offen offset:3072 nt lds\n"
    "v_mfma_f32_16x16x32_bf16 v[212:215], a[144:147], v[24:27], 0\n"
    "ds_read_b128 a[124:127], v75 offset:49408\n"
    "v_fma_f32 v11, v11, s96, v93\n"
    "v_mfma_f32_16x16x32_bf16 v[212:215], a[148:151], v[28:31], v[212:215]\n"
    "v_max3_f32 v97, v0, v1, v2\n"
    "v_max3_f32 v97, v97, v3, v4\n"
    "v_mfma_f32_16x16x32_bf16 v[216:219], a[144:147], v[32:35], 0\n"
    "v_max3_f32 v97, v97, v5, v6\n"
    "v_max3_f32 v97, v97, v7, v8\n"
    "v_mfma_f32_16x16x32_bf16 v[216:219], a[148:151], v[36:39], v[216:219]\n"
    "s_waitcnt lgkmcnt(4)\n"
    "buffer_store_dwordx4 a[228:231], v89, s[36:39], s42 offen nt\n"
    "v_mfma_f32_16x16x32_bf16 v[220:223], a[144:147], v[40:43], 0\n"
    "v_max3_f32 v97, v97, v9, v10\n"
    "v_max_f32 v97, v97, v11\n"
    "v_mfma_f32_16x16x32_bf16 v[220:223], a[148:151], v[44:47], v[220:223]\n"
    "v_cmp_lt_f32 vcc, s97, v97\n"
    "s_add_u32 m0, s35, 8192\n"
    "v_mfma_f32_16x16x32_bf16 a[200:203], a[152:155], v[24:27], 0\n"
    "v_mfma_f32_16x16x32_bf16 a[200:203], a[156:159], v[28:31], a[200:203]\n"
    "buffer_load_dwordx4 v84, s[28:31], s41 offen nt lds\n"
    "v_mfma_f32_16x16x32_bf16 a[204:207], a[152:155], v[32:35], 0\n"
    "ds_read_b64_tr_b16 a[128:129], v76 offset:49152\n"
    "s_cbranch_vccnz STUBb6c0_%=\n"
    "RETb6c0_%=:\n"
    "v_mfma_f32_16x16x32_bf16 a[204:207], a[156:159], v[36:39], a[204:207]\n"
    "ds_read_b64_tr_b16 a[130:131], v76 offset:53248\n"
    "ds_read_b64_tr_b16 a[132:133], v76 offset:57344\n"
    "v_mfma_f32_16x16x32_bf16 a[208:211], a[152:155], v[40:43], 0\n"
    "v_exp_f32 v0, v0\n"
    "v_mfma_f32_16x16x32_bf16 a[208:211], a[156:159], v[44:47], a[208:211]\n"
    "ds_read_b64_tr_b16 a[134:135], v76 offset:61440\n"
    "s_waitcnt lgkmcnt(4)\n"
    "v_mfma_f32_16x16x32_bf16 v[12:15], a[96:99], a[0:3], 0\n"
    "v_exp_f32 v1, v1\n"
    "v_mfma_f32_16x16x32_bf16 v[12:15], a[100:103], a[4:7], v[12:15]\n"
    "ds_read_b64_tr_b16 a[136:137], v77 offset:49152\n"
    "v_cvt_pk_bf16_f32 v52, v0, v1\n"
    "v_mfma_f32_16x16x32_bf16 v[12:15], a[104:107], a[8:11], v[12:15]\n"
    "ds_read_b64_tr_b16 a[138:139], v77 offset:53248\n"
    "ds_read_b64_tr_b16 a[140:141], v77 offset:57344\n"
    "v_mfma_f32_16x16x32_bf16 v[12:15], a[108:111], a[12:15], v[12:15]\n"
    "v_exp_f32 v2, v2\n"
    "v_mfma_f32_16x16x32_bf16 v[12:15], a[112:115], a[16:19], v[12:15]\n"
    "ds_read_b64_tr_b16 a[142:143], v77 offset:61440\n"
    "v_mfma_f32_16x16x32_bf16 v[12:15], a[116:119], a[20:23], v[12:15]\n"
    "v_exp_f32 v3, v3\n"
    "v_mfma_f32_16x16x32_bf16 v[12:15], a[120:123], a[24:27], v[12:15]\n"
    "ds_read_b64_tr_b16 a[144:145], v78 offset:49152\n"
    "v_cvt_pk_bf16_f32 v53, v2, v3\n"
    "v_mfma_f32_16x16x32_bf16 v[12:15], a[124:127], a[28:31], v[12:15]\n"
    "ds_read_b64_tr_b16 a[146:147], v78 offset:53248\n"
    "ds_read_b64_tr_b16 a[148:149], v78 offset:57344\n"
    "v_mfma_f32_16x16x32_bf16 v[16:19], a[96:99], a[32:35], 0\n"
    "v_exp_f32 v4, v4\n"
    "v_mfma_f32_16x16x32_bf16 v[16:19], a[100:103], a[36:39], v[16:19]\n"
    "ds_read_b64_tr_b16 a[150:151], v78 offset:61440\n"
    "v_mfma_f32_16x16x32_bf16 v[16:19], a[104:107], a[40:43], v[16:19]\n"
    "v_exp_f32 v5, v5\n"
    "v_mfma_f32_16x16x32_bf16 v[16:19], a[108:111], a[44:47], v[16:19]\n"
    "ds_read_b64_tr_b16 a[152:153], v79 offset:49152\n"
    "v_cvt_pk_bf16_f32 v60, v4, v5\n"
    "v_mfma_f32_16x16x32_bf16 v[16:19], a[112:115], a[48:51], v[16:19]\n"
    "ds_read_b64_tr_b16 a[154:155], v79 offset:53248\n"
    "ds_read_b64_tr_b16 a[156:157], v79 offset:57344\n"
    "v_mfma_f32_16x16x32_bf16 v[16:19], a[116:119], a[52:55], v[16:19]\n"
    "v_exp_f32 v6, v6\n"
    "v_mfma_f32_16x16x32_bf16 v[16:19], a[120:123], a[56:59], v[16:19]\n"
    "ds_read_b64_tr_b16 a[158:159], v79 offset:61440\n"
    "v_mfma_f32_16x16x32_bf16 v[16:19], a[124:127], a[60:63], v[16:19]\n"
    "v_exp_f32 v7, v7\n"
    "v_mfma_f32_16x16x32_bf16 v[20:23], a[96:99], a[64:67], 0\n"
    "v_exp_f32 v8, v8\n"
    "v_mfma_f32_16x16x32_bf16 v[20:23], a[100:103], a[68:71], v[20:23]\n"
    "v_exp_f32 v9, v9\n"
    "v_mfma_f32_16x16x32_bf16 v[20:23], a[104:107], a[72:75], v[20:23]\n"
    "v_exp_f32 v10, v10\n"
    "v_mfma_f32_16x16x32_bf16 v[20:23], a[108:111], a[76:79], v[20:23]\n"
    "v_exp_f32 v11, v11\n"
    "v_mfma_f32_16x16x32_bf16 v[20:23], a[112:115], a[80:83], v[20:23]\n"
    "v_cvt_pk_bf16_f32 v61, v6, v7\n"
    "v_cvt_pk_bf16_f32 v68, v8, v9\n"
    "v_mfma_f32_16x16x32_bf16 v[20:23], a[116:119], a[84:87], v[20:23]\n"
    "v_cvt_pk_bf16_f32 v69, v10, v11\n"
    "v_mfma_f32_16x16x32_bf16 v[20:23], a[120:123], a[88:91], v[20:23]\n"
    "v_mfma_f32_16x16x32_bf16 v[20:23], a[124:127], a[92:95], v[20:23]\n"
    "s_waitcnt vmcnt(16) lgkmcnt(0)\n"
    "s_barrier\n"
    "v_mfma_f32_16x16x32_bf16 v[224:227], a[128:131], v[24:27], 0\n"
    "ds_read_b128 a[96:99], v72 offset:0\n"
    "s_add_u32 m0, s91, 49152\n"
    "v_mfma_f32_16x16x32_bf16 v[224:227], a[132:135], v[28:31], v[224:227]\n"
    "buffer_load_dwordx4 v80, s[20:23], s51 offen lds\n"
    "v_mfma_f32_16x16x32_bf16 v[228:231], a[128:131], v[32:35], 0\n"
    "ds_read_b128 a[100:103], v73 offset:0\n"
    "v_fma_f32 v12, v12, s96, v91\n"
    "v_mfma_f32_16x16x32_bf16 v[228:231], a[132:135], v[36:39], v[228:231]\n"
    "ds_read_b128 a[104:107], v74 offset:0\n"
    "v_fma_f32 v13, v13, s96, v91\n"
    "v_mfma_f32_16x16x32_bf16 v[232:235], a[128:131], v[40:43], 0\n"
    "v_fma_f32 v14, v14, s96, v91\n"
    "v_fma_f32 v15, v15, s96, v91\n"
    "v_mfma_f32_16x16x32_bf16 v[232:235], a[132:135], v[44:47], v[232:235]\n"
    "buffer_load_dwordx4 v81, s[20:23], s51 offen offset:1024 nt lds\n"
    "v_mfma_f32_16x16x32_bf16 v[236:239], a[136:139], v[24:27], 0\n"
    "ds_read_b128 a[108:111], v75 offset:0\n"
    "v_fma_f32 v16, v16, s96, v92\n"
    "v_mfma_f32_16x16x32_bf16 v[236:239], a[140:143], v[28:31], v[236:239]\n"
    "ds_read_b128  a[228:231], v90 offset:9216\n"
    "v_fma_f32 v17, v17, s96, v92\n"
    "v_mfma_f32_16x16x32_bf16 v[240:243], a[136:139], v[32:35], 0\n"
    "ds_read_b128 a[112:115], v72 offset:256\n"
    "v_fma_f32 v18, v18, s96, v92\n"
    "v_mfma_f32_16x16x32_bf16 v[240:243], a[140:143], v[36:39], v[240:243]\n"
    "buffer_load_dwordx4 v82, s[24:27], s75 offen offset:2048 nt lds\n"
    "v_mfma_f32_16x16x32_bf16 v[244:247], a[136:139], v[40:43], 0\n"
    "ds_read_b128 a[116:119], v73 offset:256\n"
    "v_fma_f32 v19, v19, s96, v92\n"
    "v_mfma_f32_16x16x32_bf16 v[244:247], a[140:143], v[44:47], v[244:247]\n"
    "ds_read_b128 a[120:123], v74 offset:256\n"
    "v_fma_f32 v20, v20, s96, v93\n"
    "v_mfma_f32_16x16x32_bf16 v[248:251], a[144:147], v[24:27], 0\n"
    "ds_read_b128 a[124:127], v75 offset:256\n"
    "v_fma_f32 v21, v21, s96, v93\n"
    "v_mfma_f32_16x16x32_bf16 v[248:251], a[148:151], v[28:31], v[248:251]\n"
    "buffer_load_dwordx4 v83, s[24:27], s75 offen offset:3072 nt lds\n"
    "v_mfma_f32_16x16x32_bf16 v[252:255], a[144:147], v[32:35], 0\n"
    "v_fma_f32 v22, v22, s96, v93\n"
    "v_fma_f32 v23, v23, s96, v93\n"
    "v_mfma_f32_16x16x32_bf16 v[252:255], a[148:151], v[36:39], v[252:255]\n"
    "v_max3_f32 v97, v12, v13, v14\n"
    "v_max3_f32 v97, v97, v15, v16\n"
    "v_mfma_f32_16x16x32_bf16 a[224:227], a[144:147], v[40:43], 0\n"
    "v_max3_f32 v97, v97, v17, v18\n"
    "v_max3_f32 v97, v97, v19, v20\n"
    "v_mfma_f32_16x16x32_bf16 a[224:227], a[148:151], v[44:47], a[224:227]\n"
    "s_waitcnt lgkmcnt(4)\n"
    "buffer_store_dwordx4 a[228:231], v89, s[36:39], s42 offen offset:2048 nt\n"
    "v_mfma_f32_16x16x32_bf16 a[212:215], a[152:155], v[24:27], 0\n"
    "v_max3_f32 v97, v97, v21, v22\n"
    "v_max_f32 v97, v97, v23\n"
    "v_mfma_f32_16x16x32_bf16 a[212:215], a[156:159], v[28:31], a[212:215]\n"
    "v_cmp_lt_f32 vcc, s97, v97\n"
    "s_add_u32 m0, s35, 8192\n"
    "v_mfma_f32_16x16x32_bf16 a[216:219], a[152:155], v[32:35], 0\n"
    "ds_read_b64_tr_b16 a[128:129], v76 offset:0\n"
    "ds_read_b64_tr_b16 a[130:131], v76 offset:4096\n"
    "v_mfma_f32_16x16x32_bf16 a[216:219], a[156:159], v[36:39], a[216:219]\n"
    "buffer_load_dwordx4 v85, s[28:31], s41 offen offset:1024 nt lds\n"
    "v_mfma_f32_16x16x32_bf16 a[220:223], a[152:155], v[40:43], 0\n"
    "ds_read_b64_tr_b16 a[132:133], v76 offset:8192\n"
    "s_cbranch_vccnz STUBb7c0_%=\n"
    "RETb7c0_%=:\n"
    "v_mfma_f32_16x16x32_bf16 a[220:223], a[156:159], v[44:47], a[220:223]\n"
    "ds_read_b64_tr_b16 a[134:135], v76 offset:12288\n"
    "s_waitcnt lgkmcnt(4)\n"
    "v_mfma_f32_16x16x32_bf16 v[0:3], a[96:99], a[0:3], 0\n"
    "v_exp_f32 v12, v12\n"
    "v_mfma_f32_16x16x32_bf16 v[0:3], a[100:103], a[4:7], v[0:3]\n"
    "ds_read_b64_tr_b16 a[136:137], v77 offset:0\n"
    "ds_read_b64_tr_b16 a[138:139], v77 offset:4096\n"
    "v_mfma_f32_16x16x32_bf16 v[0:3], a[104:107], a[8:11], v[0:3]\n"
    "v_exp_f32 v13, v13\n"
    "v_mfma_f32_16x16x32_bf16 v[0:3], a[108:111], a[12:15], v[0:3]\n"
    "ds_read_b64_tr_b16 a[140:141], v77 offset:8192\n"
    "v_cvt_pk_bf16_f32 v54, v12, v13\n"
    "v_mfma_f32_16x16x32_bf16 v[0:3], a[112:115], a[16:19], v[0:3]\n"
    "ds_read_b64_tr_b16 a[142:143], v77 offset:12288\n"
    "v_mfma_f32_16x16x32_bf16 v[0:3], a[116:119], a[20:23], v[0:3]\n"
    "v_exp_f32 v14, v14\n"
    "v_mfma_f32_16x16x32_bf16 v[0:3], a[120:123], a[24:27], v[0:3]\n"
    "ds_read_b64_tr_b16 a[144:145], v78 offset:0\n"
    "ds_read_b64_tr_b16 a[146:147], v78 offset:4096\n"
    "v_mfma_f32_16x16x32_bf16 v[0:3], a[124:127], a[28:31], v[0:3]\n"
    "v_exp_f32 v15, v15\n"
    "v_mfma_f32_16x16x32_bf16 v[4:7], a[96:99], a[32:35], 0\n"
    "ds_read_b64_tr_b16 a[148:149], v78 offset:8192\n"
    "v_cvt_pk_bf16_f32 v55, v14, v15\n"
    "v_mfma_f32_16x16x32_bf16 v[4:7], a[100:103], a[36:39], v[4:7]\n"
    "ds_read_b64_tr_b16 a[150:151], v78 offset:12288\n"
    "v_mfma_f32_16x16x32_bf16 v[4:7], a[104:107], a[40:43], v[4:7]\n"
    "v_exp_f32 v16, v16\n"
    "v_mfma_f32_16x16x32_bf16 v[4:7], a[108:111], a[44:47], v[4:7]\n"
    "ds_read_b64_tr_b16 a[152:153], v79 offset:0\n"
    "ds_read_b64_tr_b16 a[154:155], v79 offset:4096\n"
    "v_mfma_f32_16x16x32_bf16 v[4:7], a[112:115], a[48:51], v[4:7]\n"
    "v_exp_f32 v17, v17\n"
    "v_mfma_f32_16x16x32_bf16 v[4:7], a[116:119], a[52:55], v[4:7]\n"
    "ds_read_b64_tr_b16 a[156:157], v79 offset:8192\n"
    "v_cvt_pk_bf16_f32 v62, v16, v17\n"
    "v_mfma_f32_16x16x32_bf16 v[4:7], a[120:123], a[56:59], v[4:7]\n"
    "ds_read_b64_tr_b16 a[158:159], v79 offset:12288\n"
    "v_mfma_f32_16x16x32_bf16 v[4:7], a[124:127], a[60:63], v[4:7]\n"
    "v_exp_f32 v18, v18\n"
    "v_mfma_f32_16x16x32_bf16 v[8:11], a[96:99], a[64:67], 0\n"
    "v_exp_f32 v19, v19\n"
    "v_mfma_f32_16x16x32_bf16 v[8:11], a[100:103], a[68:71], v[8:11]\n"
    "v_exp_f32 v20, v20\n"
    "v_mfma_f32_16x16x32_bf16 v[8:11], a[104:107], a[72:75], v[8:11]\n"
    "v_exp_f32 v21, v21\n"
    "v_mfma_f32_16x16x32_bf16 v[8:11], a[108:111], a[76:79], v[8:11]\n"
    "v_exp_f32 v22, v22\n"
    "v_mfma_f32_16x16x32_bf16 v[8:11], a[112:115], a[80:83], v[8:11]\n"
    "v_exp_f32 v23, v23\n"
    "v_mfma_f32_16x16x32_bf16 v[8:11], a[116:119], a[84:87], v[8:11]\n"
    "v_cvt_pk_bf16_f32 v63, v18, v19\n"
    "v_cvt_pk_bf16_f32 v70, v20, v21\n"
    "v_mfma_f32_16x16x32_bf16 v[8:11], a[120:123], a[88:91], v[8:11]\n"
    "v_cvt_pk_bf16_f32 v71, v22, v23\n"
    "v_mfma_f32_16x16x32_bf16 v[8:11], a[124:127], a[92:95], v[8:11]\n"
    "s_waitcnt vmcnt(14) lgkmcnt(0)\n"
    "s_barrier\n"
    "v_mfma_f32_16x16x32_bf16 a[164:167], a[160:163], v[48:51], a[164:167]\n"
    "v_fma_f32 v0, v0, s96, v91\n"
    "s_add_u32 m0, s91, 0\n"
    "v_mfma_f32_16x16x32_bf16 a[164:167], a[160:163], v[52:55], a[164:167]\n"
    "buffer_load_dwordx4 v80, s[20:23], s52 offen lds\n"
    "v_mfma_f32_16x16x32_bf16 v[116:119], a[128:131], v[48:51], v[116:119]\n"
    "ds_read_b128 a[96:99], v72 offset:16384\n"
    "v_fma_f32 v1, v1, s96, v91\n"
    "v_mfma_f32_16x16x32_bf16 v[116:119], a[132:135], v[52:55], v[116:119]\n"
    "ds_read_b128 a[100:103], v73 offset:16384\n"
    "v_fma_f32 v2, v2, s96, v91\n"
    "v_mfma_f32_16x16x32_bf16 v[120:123], a[128:131], v[56:59], v[120:123]\n"
    "ds_read_b128 a[104:107], v74 offset:16384\n"
    "v_fma_f32 v3, v3, s96, v91\n"
    "v_mfma_f32_16x16x32_bf16 v[120:123], a[132:135], v[60:63], v[120:123]\n"
    "buffer_load_dwordx4 v81, s[20:23], s52 offen offset:1024 nt lds\n"
    "v_mfma_f32_16x16x32_bf16 v[124:127], a[128:131], v[64:67], v[124:127]\n"
    "ds_read_b128 a[108:111], v75 offset:16384\n"
    "v_fma_f32 v4, v4, s96, v92\n"
    "v_mfma_f32_16x16x32_bf16 v[124:127], a[132:135], v[68:71], v[124:127]\n"
    "ds_read_b128  a[228:231], v88 offset:10240\n"
    "v_fma_f32 v5, v5, s96, v92\n"
    "v_mfma_f32_16x16x32_bf16 v[128:131], a[136:139], v[48:51], v[128:131]\n"
    "ds_read_b128 a[112:115], v72 offset:16640\n"
    "v_fma_f32 v6, v6, s96, v92\n"
    "v_mfma_f32_16x16x32_bf16 v[128:131], a[140:143], v[52:55], v[128:131]\n"
    "buffer_load_dwordx4 v82, s[24:27], s76 offen offset:2048 nt lds\n"
    "v_mfma_f32_16x16x32_bf16 v[132:135], a[136:139], v[56:59], v[132:135]\n"
    "ds_read_b128 a[116:119], v73 offset:16640\n"
    "v_fma_f32 v7, v7, s96, v92\n"
    "v_mfma_f32_16x16x32_bf16 v[132:135], a[140:143], v[60:63], v[132:135]\n"
    "ds_read_b128 a[120:123], v74 offset:16640\n"
    "v_fma_f32 v8, v8, s96, v93\n"
    "v_mfma_f32_16x16x32_bf16 v[136:139], a[136:139], v[64:67], v[136:139]\n"
    "v_fma_f32 v9, v9, s96, v93\n"
    "v_fma_f32 v10, v10, s96, v93\n"
    "v_mfma_f32_16x16x32_bf16 v[136:139], a[140:143], v[68:71], v[136:139]\n"
    "buffer_load_dwordx4 v83, s[24:27], s76 offen offset:3072 nt lds\n"
    "v_mfma_f32_16x16x32_bf16 v[140:143], a[144:147], v[48:51], v[140:143]\n"
    "ds_read_b128 a[124:127], v75 offset:16640\n"
    "v_fma_f32 v11, v11, s96, v93\n"
    "v_mfma_f32_16x16x32_bf16 v[140:143], a[148:151], v[52:55], v[140:143]\n"
    "v_max3_f32 v97, v0, v1, v2\n"
    "v_max3_f32 v97, v97, v3, v4\n"
    "v_mfma_f32_16x16x32_bf16 v[144:147], a[144:147], v[56:59], v[144:147]\n"
    "v_max3_f32 v97, v97, v5, v6\n"
    "v_max3_f32 v97, v97, v7, v8\n"
    "v_mfma_f32_16x16x32_bf16 v[144:147], a[148:151], v[60:63], v[144:147]\n"
    "s_waitcnt lgkmcnt(4)\n"
    "buffer_store_dwordx4 a[228:231], v89, s[36:39], s90 offen nt\n"
    "v_mfma_f32_16x16x32_bf16 v[148:151], a[144:147], v[64:67], v[148:151]\n"
    "v_max3_f32 v97, v97, v9, v10\n"
    "v_max_f32 v97, v97, v11\n"
    "v_mfma_f32_16x16x32_bf16 v[148:151], a[148:151], v[68:71], v[148:151]\n"
    "v_cmp_lt_f32 vcc, s97, v97\n"
    "s_add_u32 m0, s35, 8192\n"
    "v_mfma_f32_16x16x32_bf16 a[176:179], a[152:155], v[48:51], a[176:179]\n"
    "v_mfma_f32_16x16x32_bf16 a[176:179], a[156:159], v[52:55], a[176:179]\n"
    "buffer_load_dwordx4 v86, s[28:31], s41 offen offset:2048 nt lds\n"
    "v_mfma_f32_16x16x32_bf16 a[180:183], a[152:155], v[56:59], a[180:183]\n"
    "ds_read_b64_tr_b16 a[128:129], v76 offset:16384\n"
    "s_cbranch_vccnz STUBb8c0_%=\n"
    "RETb8c0_%=:\n"
    "v_mfma_f32_16x16x32_bf16 a[180:183], a[156:159], v[60:63], a[180:183]\n"
    "ds_read_b64_tr_b16 a[130:131], v76 offset:20480\n"
    "ds_read_b64_tr_b16 a[132:133], v76 offset:24576\n"
    "v_mfma_f32_16x16x32_bf16 a[184:187], a[152:155], v[64:67], a[184:187]\n"
    "v_exp_f32 v0, v0\n"
    "v_mfma_f32_16x16x32_bf16 a[184:187], a[156:159], v[68:71], a[184:187]\n"
    "ds_read_b64_tr_b16 a[134:135], v76 offset:28672\n"
    "s_waitcnt lgkmcnt(4)\n"
    "v_mfma_f32_16x16x32_bf16 v[12:15], a[96:99], a[0:3], 0\n"
    "v_exp_f32 v1, v1\n"
    "v_mfma_f32_16x16x32_bf16 v[12:15], a[100:103], a[4:7], v[12:15]\n"
    "ds_read_b64_tr_b16 a[136:137], v77 offset:16384\n"
    "v_cvt_pk_bf16_f32 v24, v0, v1\n"
    "v_mfma_f32_16x16x32_bf16 v[12:15], a[104:107], a[8:11], v[12:15]\n"
    "ds_read_b64_tr_b16 a[138:139], v77 offset:20480\n"
    "ds_read_b64_tr_b16 a[140:141], v77 offset:24576\n"
    "v_mfma_f32_16x16x32_bf16 v[12:15], a[108:111], a[12:15], v[12:15]\n"
    "v_exp_f32 v2, v2\n"
    "v_mfma_f32_16x16x32_bf16 v[12:15], a[112:115], a[16:19], v[12:15]\n"
    "ds_read_b64_tr_b16 a[142:143], v77 offset:28672\n"
    "v_mfma_f32_16x16x32_bf16 v[12:15], a[116:119], a[20:23], v[12:15]\n"
    "v_exp_f32 v3, v3\n"
    "v_mfma_f32_16x16x32_bf16 v[12:15], a[120:123], a[24:27], v[12:15]\n"
    "ds_read_b64_tr_b16 a[144:145], v78 offset:16384\n"
    "v_cvt_pk_bf16_f32 v25, v2, v3\n"
    "v_mfma_f32_16x16x32_bf16 v[12:15], a[124:127], a[28:31], v[12:15]\n"
    "ds_read_b64_tr_b16 a[146:147], v78 offset:20480\n"
    "ds_read_b64_tr_b16 a[148:149], v78 offset:24576\n"
    "v_mfma_f32_16x16x32_bf16 v[16:19], a[96:99], a[32:35], 0\n"
    "v_exp_f32 v4, v4\n"
    "v_mfma_f32_16x16x32_bf16 v[16:19], a[100:103], a[36:39], v[16:19]\n"
    "ds_read_b64_tr_b16 a[150:151], v78 offset:28672\n"
    "v_mfma_f32_16x16x32_bf16 v[16:19], a[104:107], a[40:43], v[16:19]\n"
    "v_exp_f32 v5, v5\n"
    "v_mfma_f32_16x16x32_bf16 v[16:19], a[108:111], a[44:47], v[16:19]\n"
    "ds_read_b64_tr_b16 a[152:153], v79 offset:16384\n"
    "v_cvt_pk_bf16_f32 v32, v4, v5\n"
    "v_mfma_f32_16x16x32_bf16 v[16:19], a[112:115], a[48:51], v[16:19]\n"
    "ds_read_b64_tr_b16 a[154:155], v79 offset:20480\n"
    "ds_read_b64_tr_b16 a[156:157], v79 offset:24576\n"
    "v_mfma_f32_16x16x32_bf16 v[16:19], a[116:119], a[52:55], v[16:19]\n"
    "v_exp_f32 v6, v6\n"
    "v_mfma_f32_16x16x32_bf16 v[16:19], a[120:123], a[56:59], v[16:19]\n"
    "ds_read_b64_tr_b16 a[158:159], v79 offset:28672\n"
    "v_mfma_f32_16x16x32_bf16 v[16:19], a[124:127], a[60:63], v[16:19]\n"
    "v_exp_f32 v7, v7\n"
    "v_mfma_f32_16x16x32_bf16 v[20:23], a[96:99], a[64:67], 0\n"
    "v_exp_f32 v8, v8\n"
    "v_mfma_f32_16x16x32_bf16 v[20:23], a[100:103], a[68:71], v[20:23]\n"
    "v_exp_f32 v9, v9\n"
    "v_mfma_f32_16x16x32_bf16 v[20:23], a[104:107], a[72:75], v[20:23]\n"
    "v_exp_f32 v10, v10\n"
    "v_mfma_f32_16x16x32_bf16 v[20:23], a[108:111], a[76:79], v[20:23]\n"
    "v_exp_f32 v11, v11\n"
    "v_mfma_f32_16x16x32_bf16 v[20:23], a[112:115], a[80:83], v[20:23]\n"
    "v_cvt_pk_bf16_f32 v33, v6, v7\n"
    "v_cvt_pk_bf16_f32 v40, v8, v9\n"
    "v_mfma_f32_16x16x32_bf16 v[20:23], a[116:119], a[84:87], v[20:23]\n"
    "v_cvt_pk_bf16_f32 v41, v10, v11\n"
    "v_mfma_f32_16x16x32_bf16 v[20:23], a[120:123], a[88:91], v[20:23]\n"
    "v_mfma_f32_16x16x32_bf16 v[20:23], a[124:127], a[92:95], v[20:23]\n"
    "s_waitcnt vmcnt(14) lgkmcnt(0)\n"
    "s_barrier\n"
    "v_mfma_f32_16x16x32_bf16 a[168:171], a[160:163], v[56:59], a[168:171]\n"
    "v_fma_f32 v12, v12, s96, v91\n"
    "s_add_u32 m0, s91, 16384\n"
    "v_mfma_f32_16x16x32_bf16 a[168:171], a[160:163], v[60:63], a[168:171]\n"
    "buffer_load_dwordx4 v80, s[20:23], s53 offen lds\n"
    "v_mfma_f32_16x16x32_bf16 v[152:155], a[128:131], v[48:51], v[152:155]\n"
    "ds_read_b128 a[96:99], v72 offset:32768\n"
    "v_fma_f32 v13, v13, s96, v91\n"
    "v_mfma_f32_16x16x32_bf16 v[152:155], a[132:135], v[52:55], v[152:155]\n"
    "ds_read_b128 a[100:103], v73 offset:32768\n"
    "v_fma_f32 v14, v14, s96, v91\n"
    "v_mfma_f32_16x16x32_bf16 v[156:159], a[128:131], v[56:59], v[156:159]\n"
    "ds_read_b128 a[104:107], v74 offset:32768\n"
    "v_fma_f32 v15, v15, s96, v91\n"
    "v_mfma_f32_16x16x32_bf16 v[156:159], a[132:135], v[60:63], v[156:159]\n"
    "buffer_load_dwordx4 v81, s[20:23], s53 offen offset:1024 nt lds\n"
    "v_mfma_f32_16x16x32_bf16 v[160:163], a[128:131], v[64:67], v[160:163]\n"
    "ds_read_b128 a[108:111], v75 offset:32768\n"
    "v_fma_f32 v16, v16, s96, v92\n"
    "v_mfma_f32_16x16x32_bf16 v[160:163], a[132:135], v[68:71], v[160:163]\n"
    "ds_read_b128  a[228:231], v90 offset:11264\n"
    "v_fma_f32 v17, v17, s96, v92\n"
    "v_mfma_f32_16x16x32_bf16 v[164:167], a[136:139], v[48:51], v[164:167]\n"
    "ds_read_b128 a[112:115], v72 offset:33024\n"
    "v_fma_f32 v18, v18, s96, v92\n"
    "v_mfma_f32_16x16x32_bf16 v[164:167], a[140:143], v[52:55], v[164:167]\n"
    "buffer_load_dwordx4 v82, s[24:27], s77 offen offset:2048 nt lds\n"
    "v_mfma_f32_16x16x32_bf16 v[168:171], a[136:139], v[56:59], v[168:171]\n"
    "ds_read_b128 a[116:119], v73 offset:33024\n"
    "v_fma_f32 v19, v19, s96, v92\n"
    "v_mfma_f32_16x16x32_bf16 v[168:171], a[140:143], v[60:63], v[168:171]\n"
    "ds_read_b128 a[120:123], v74 offset:33024\n"
    "v_fma_f32 v20, v20, s96, v93\n"
    "v_mfma_f32_16x16x32_bf16 v[172:175], a[136:139], v[64:67], v[172:175]\n"
    "v_fma_f32 v21, v21, s96, v93\n"
    "v_fma_f32 v22, v22, s96, v93\n"
    "v_mfma_f32_16x16x32_bf16 v[172:175], a[140:143], v[68:71], v[172:175]\n"
    "buffer_load_dwordx4 v83, s[24:27], s77 offen offset:3072 nt lds\n"
    "v_mfma_f32_16x16x32_bf16 v[176:179], a[144:147], v[48:51], v[176:179]\n"
    "ds_read_b128 a[124:127], v75 offset:33024\n"
    "v_fma_f32 v23, v23, s96, v93\n"
    "v_mfma_f32_16x16x32_bf16 v[176:179], a[148:151], v[52:55], v[176:179]\n"
    "v_max3_f32 v97, v12, v13, v14\n"
    "v_max3_f32 v97, v97, v15, v16\n"
    "v_mfma_f32_16x16x32_bf16 v[180:183], a[144:147], v[56:59], v[180:183]\n"
    "v_max3_f32 v97, v97, v17, v18\n"
    "v_max3_f32 v97, v97, v19, v20\n"
    "v_mfma_f32_16x16x32_bf16 v[180:183], a[148:151], v[60:63], v[180:183]\n"
    "s_waitcnt lgkmcnt(4)\n"
    "buffer_store_dwordx4 a[228:231], v89, s[36:39], s90 offen offset:2048 nt\n"
    "v_mfma_f32_16x16x32_bf16 v[184:187], a[144:147], v[64:67], v[184:187]\n"
    "v_max3_f32 v97, v97, v21, v22\n"
    "v_max_f32 v97, v97, v23\n"
    "v_mfma_f32_16x16x32_bf16 v[184:187], a[148:151], v[68:71], v[184:187]\n"
    "v_cmp_lt_f32 vcc, s97, v97\n"
    "s_add_u32 m0, s35, 8192\n"
    "v_mfma_f32_16x16x32_bf16 a[188:191], a[152:155], v[48:51], a[188:191]\n"
    "v_mfma_f32_16x16x32_bf16 a[188:191], a[156:159], v[52:55], a[188:191]\n"
    "buffer_load_dwordx4 v87, s[28:31], s41 offen offset:3072 nt lds\n"
    "v_mfma_f32_16x16x32_bf16 a[192:195], a[152:155], v[56:59], a[192:195]\n"
    "ds_read_b64_tr_b16 a[128:129], v76 offset:32768\n"
    "s_cbranch_vccnz STUBb9c0_%=\n"
    "RETb9c0_%=:\n"
    "v_mfma_f32_16x16x32_bf16 a[192:195], a[156:159], v[60:63], a[192:195]\n"
    "ds_read_b64_tr_b16 a[130:131], v76 offset:36864\n"
    "ds_read_b64_tr_b16 a[132:133], v76 offset:40960\n"
    "v_mfma_f32_16x16x32_bf16 a[196:199], a[152:155], v[64:67], a[196:199]\n"
    "v_exp_f32 v12, v12\n"
    "v_mfma_f32_16x16x32_bf16 a[196:199], a[156:159], v[68:71], a[196:199]\n"
    "ds_read_b64_tr_b16 a[134:135], v76 offset:45056\n"
    "s_waitcnt lgkmcnt(4)\n"
    "v_mfma_f32_16x16x32_bf16 v[0:3], a[96:99], a[0:3], 0\n"
    "v_exp_f32 v13, v13\n"
    "v_mfma_f32_16x16x32_bf16 v[0:3], a[100:103], a[4:7], v[0:3]\n"
    "ds_read_b64_tr_b16 a[136:137], v77 offset:32768\n"
    "v_cvt_pk_bf16_f32 v26, v12, v13\n"
    "v_mfma_f32_16x16x32_bf16 v[0:3], a[104:107], a[8:11], v[0:3]\n"
    "ds_read_b64_tr_b16 a[138:139], v77 offset:36864\n"
    "ds_read_b64_tr_b16 a[140:141], v77 offset:40960\n"
    "v_mfma_f32_16x16x32_bf16 v[0:3], a[108:111], a[12:15], v[0:3]\n"
    "v_exp_f32 v14, v14\n"
    "v_mfma_f32_16x16x32_bf16 v[0:3], a[112:115], a[16:19], v[0:3]\n"
    "ds_read_b64_tr_b16 a[142:143], v77 offset:45056\n"
    "v_mfma_f32_16x16x32_bf16 v[0:3], a[116:119], a[20:23], v[0:3]\n"
    "v_exp_f32 v15, v15\n"
    "v_mfma_f32_16x16x32_bf16 v[0:3], a[120:123], a[24:27], v[0:3]\n"
    "ds_read_b64_tr_b16 a[144:145], v78 offset:32768\n"
    "v_cvt_pk_bf16_f32 v27, v14, v15\n"
    "v_mfma_f32_16x16x32_bf16 v[0:3], a[124:127], a[28:31], v[0:3]\n"
    "ds_read_b64_tr_b16 a[146:147], v78 offset:36864\n"
    "ds_read_b64_tr_b16 a[148:149], v78 offset:40960\n"
    "v_mfma_f32_16x16x32_bf16 v[4:7], a[96:99], a[32:35], 0\n"
    "v_exp_f32 v16, v16\n"
    "v_mfma_f32_16x16x32_bf16 v[4:7], a[100:103], a[36:39], v[4:7]\n"
    "ds_read_b64_tr_b16 a[150:151], v78 offset:45056\n"
    "v_mfma_f32_16x16x32_bf16 v[4:7], a[104:107], a[40:43], v[4:7]\n"
    "v_exp_f32 v17, v17\n"
    "v_mfma_f32_16x16x32_bf16 v[4:7], a[108:111], a[44:47], v[4:7]\n"
    "ds_read_b64_tr_b16 a[152:153], v79 offset:32768\n"
    "v_cvt_pk_bf16_f32 v34, v16, v17\n"
    "v_mfma_f32_16x16x32_bf16 v[4:7], a[112:115], a[48:51], v[4:7]\n"
    "ds_read_b64_tr_b16 a[154:155], v79 offset:36864\n"
    "ds_read_b64_tr_b16 a[156:157], v79 offset:40960\n"
    "v_mfma_f32_16x16x32_bf16 v[4:7], a[116:119], a[52:55], v[4:7]\n"
    "v_exp_f32 v18, v18\n"
    "v_mfma_f32_16x16x32_bf16 v[4:7], a[120:123], a[56:59], v[4:7]\n"
    "ds_read_b64_tr_b16 a[158:159], v79 offset:45056\n"
    "v_mfma_f32_16x16x32_bf16 v[4:7], a[124:127], a[60:63], v[4:7]\n"
    "v_exp_f32 v19, v19\n"
    "v_mfma_f32_16x16x32_bf16 v[8:11], a[96:99], a[64:67], 0\n"
    "v_exp_f32 v20, v20\n"
    "v_mfma_f32_16x16x32_bf16 v[8:11], a[100:103], a[68:71], v[8:11]\n"
    "v_exp_f32 v21, v21\n"
    "v_mfma_f32_16x16x32_bf16 v[8:11], a[104:107], a[72:75], v[8:11]\n"
    "v_exp_f32 v22, v22\n"
    "v_mfma_f32_16x16x32_bf16 v[8:11], a[108:111], a[76:79], v[8:11]\n"
    "v_exp_f32 v23, v23\n"
    "v_mfma_f32_16x16x32_bf16 v[8:11], a[112:115], a[80:83], v[8:11]\n"
    "v_cvt_pk_bf16_f32 v35, v18, v19\n"
    "v_cvt_pk_bf16_f32 v42, v20, v21\n"
    "v_mfma_f32_16x16x32_bf16 v[8:11], a[116:119], a[84:87], v[8:11]\n"
    "v_cvt_pk_bf16_f32 v43, v22, v23\n"
    "v_mfma_f32_16x16x32_bf16 v[8:11], a[120:123], a[88:91], v[8:11]\n"
    "v_mfma_f32_16x16x32_bf16 v[8:11], a[124:127], a[92:95], v[8:11]\n"
    "s_waitcnt vmcnt(14) lgkmcnt(0)\n"
    "s_barrier\n"
    "v_mfma_f32_16x16x32_bf16 a[172:175], a[160:163], v[64:67], a[172:175]\n"
    "v_fma_f32 v0, v0, s96, v91\n"
    "s_add_u32 m0, s91, 32768\n"
    "v_mfma_f32_16x16x32_bf16 a[172:175], a[160:163], v[68:71], a[172:175]\n"
    "buffer_load_dwordx4 v80, s[20:23], s54 offen lds\n"
    "v_mfma_f32_16x16x32_bf16 v[188:191], a[128:131], v[48:51], v[188:191]\n"
    "ds_read_b128 a[96:99], v72 offset:49152\n"
    "v_fma_f32 v1, v1, s96, v91\n"
    "v_mfma_f32_16x16x32_bf16 v[188:191], a[132:135], v[52:55], v[188:191]\n"
    "ds_read_b128 a[100:103], v73 offset:49152\n"
    "v_fma_f32 v2, v2, s96, v91\n"
    "v_mfma_f32_16x16x32_bf16 v[192:195], a[128:131], v[56:59], v[192:195]\n"
    "ds_read_b128 a[104:107], v74 offset:49152\n"
    "v_fma_f32 v3, v3, s96, v91\n"
    "v_mfma_f32_16x16x32_bf16 v[192:195], a[132:135], v[60:63], v[192:195]\n"
    "buffer_load_dwordx4 v81, s[20:23], s54 offen offset:1024 nt lds\n"
    "v_mfma_f32_16x16x32_bf16 v[196:199], a[128:131], v[64:67], v[196:199]\n"
    "ds_read_b128 a[108:111], v75 offset:49152\n"
    "v_fma_f32 v4, v4, s96, v92\n"
    "v_mfma_f32_16x16x32_bf16 v[196:199], a[132:135], v[68:71], v[196:199]\n"
    "ds_read_b128  a[228:231], v88 offset:12288\n"
    "v_fma_f32 v5, v5, s96, v92\n"
    "v_mfma_f32_16x16x32_bf16 v[200:203], a[136:139], v[48:51], v[200:203]\n"
    "ds_read_b128 a[112:115], v72 offset:49408\n"
    "v_fma_f32 v6, v6, s96, v92\n"
    "v_mfma_f32_16x16x32_bf16 v[200:203], a[140:143], v[52:55], v[200:203]\n"
    "buffer_load_dwordx4 v82, s[24:27], s78 offen offset:2048 nt lds\n"
    "v_mfma_f32_16x16x32_bf16 v[204:207], a[136:139], v[56:59], v[204:207]\n"
    "ds_read_b128 a[116:119], v73 offset:49408\n"
    "v_fma_f32 v7, v7, s96, v92\n"
    "v_mfma_f32_16x16x32_bf16 v[204:207], a[140:143], v[60:63], v[204:207]\n"
    "ds_read_b128 a[120:123], v74 offset:49408\n"
    "v_fma_f32 v8, v8, s96, v93\n"
    "v_mfma_f32_16x16x32_bf16 v[208:211], a[136:139], v[64:67], v[208:211]\n"
    "v_fma_f32 v9, v9, s96, v93\n"
    "v_fma_f32 v10, v10, s96, v93\n"
    "v_mfma_f32_16x16x32_bf16 v[208:211], a[140:143], v[68:71], v[208:211]\n"
    "buffer_load_dwordx4 v83, s[24:27], s78 offen offset:3072 nt lds\n"
    "v_mfma_f32_16x16x32_bf16 v[212:215], a[144:147], v[48:51], v[212:215]\n"
    "ds_read_b128 a[124:127], v75 offset:49408\n"
    "v_fma_f32 v11, v11, s96, v93\n"
    "v_mfma_f32_16x16x32_bf16 v[212:215], a[148:151], v[52:55], v[212:215]\n"
    "v_max3_f32 v97, v0, v1, v2\n"
    "v_max3_f32 v97, v97, v3, v4\n"
    "v_mfma_f32_16x16x32_bf16 v[216:219], a[144:147], v[56:59], v[216:219]\n"
    "v_max3_f32 v97, v97, v5, v6\n"
    "v_max3_f32 v97, v97, v7, v8\n"
    "v_mfma_f32_16x16x32_bf16 v[216:219], a[148:151], v[60:63], v[216:219]\n"
    "s_waitcnt lgkmcnt(4)\n"
    "buffer_store_dwordx4 a[228:231], v89, s[36:39], s40 offen offset:256 nt\n"
    "v_mfma_f32_16x16x32_bf16 v[220:223], a[144:147], v[64:67], v[220:223]\n"
    "v_max3_f32 v97, v97, v9, v10\n"
    "v_max_f32 v97, v97, v11\n"
    "v_mfma_f32_16x16x32_bf16 v[220:223], a[148:151], v[68:71], v[220:223]\n"
    "v_cmp_lt_f32 vcc, s97, v97\n"
    "s_add_u32 m0, s35, 12288\n"
    "v_mfma_f32_16x16x32_bf16 a[200:203], a[152:155], v[48:51], a[200:203]\n"
    "v_mfma_f32_16x16x32_bf16 a[200:203], a[156:159], v[52:55], a[200:203]\n"
    "buffer_load_dwordx4 v84, s[28:31], s89 offen nt lds\n"
    "v_mfma_f32_16x16x32_bf16 a[204:207], a[152:155], v[56:59], a[204:207]\n"
    "ds_read_b64_tr_b16 a[128:129], v76 offset:49152\n"
    "s_cbranch_vccnz STUBb10c0_%=\n"
    "RETb10c0_%=:\n"
    "v_mfma_f32_16x16x32_bf16 a[204:207], a[156:159], v[60:63], a[204:207]\n"
    "ds_read_b64_tr_b16 a[130:131], v76 offset:53248\n"
    "ds_read_b64_tr_b16 a[132:133], v76 offset:57344\n"
    "v_mfma_f32_16x16x32_bf16 a[208:211], a[152:155], v[64:67], a[208:211]\n"
    "v_exp_f32 v0, v0\n"
    "v_mfma_f32_16x16x32_bf16 a[208:211], a[156:159], v[68:71], a[208:211]\n"
    "ds_read_b64_tr_b16 a[134:135], v76 offset:61440\n"
    "s_waitcnt lgkmcnt(4)\n"
    "v_mfma_f32_16x16x32_bf16 v[12:15], a[96:99], a[0:3], 0\n"
    "v_exp_f32 v1, v1\n"
    "v_mfma_f32_16x16x32_bf16 v[12:15], a[100:103], a[4:7], v[12:15]\n"
    "ds_read_b64_tr_b16 a[136:137], v77 offset:49152\n"
    "v_cvt_pk_bf16_f32 v28, v0, v1\n"
    "v_mfma_f32_16x16x32_bf16 v[12:15], a[104:107], a[8:11], v[12:15]\n"
    "ds_read_b64_tr_b16 a[138:139], v77 offset:53248\n"
    "ds_read_b64_tr_b16 a[140:141], v77 offset:57344\n"
    "v_mfma_f32_16x16x32_bf16 v[12:15], a[108:111], a[12:15], v[12:15]\n"
    "v_exp_f32 v2, v2\n"
    "v_mfma_f32_16x16x32_bf16 v[12:15], a[112:115], a[16:19], v[12:15]\n"
    "ds_read_b64_tr_b16 a[142:143], v77 offset:61440\n"
    "v_mfma_f32_16x16x32_bf16 v[12:15], a[116:119], a[20:23], v[12:15]\n"
    "v_exp_f32 v3, v3\n"
    "v_mfma_f32_16x16x32_bf16 v[12:15], a[120:123], a[24:27], v[12:15]\n"
    "ds_read_b64_tr_b16 a[144:145], v78 offset:49152\n"
    "v_cvt_pk_bf16_f32 v29, v2, v3\n"
    "v_mfma_f32_16x16x32_bf16 v[12:15], a[124:127], a[28:31], v[12:15]\n"
    "ds_read_b64_tr_b16 a[146:147], v78 offset:53248\n"
    "ds_read_b64_tr_b16 a[148:149], v78 offset:57344\n"
    "v_mfma_f32_16x16x32_bf16 v[16:19], a[96:99], a[32:35], 0\n"
    "v_exp_f32 v4, v4\n"
    "v_mfma_f32_16x16x32_bf16 v[16:19], a[100:103], a[36:39], v[16:19]\n"
    "ds_read_b64_tr_b16 a[150:151], v78 offset:61440\n"
    "v_mfma_f32_16x16x32_bf16 v[16:19], a[104:107], a[40:43], v[16:19]\n"
    "v_exp_f32 v5, v5\n"
    "v_mfma_f32_16x16x32_bf16 v[16:19], a[108:111], a[44:47], v[16:19]\n"
    "ds_read_b64_tr_b16 a[152:153], v79 offset:49152\n"
    "v_cvt_pk_bf16_f32 v36, v4, v5\n"
    "v_mfma_f32_16x16x32_bf16 v[16:19], a[112:115], a[48:51], v[16:19]\n"
    "ds_read_b64_tr_b16 a[154:155], v79 offset:53248\n"
    "ds_read_b64_tr_b16 a[156:157], v79 offset:57344\n"
    "v_mfma_f32_16x16x32_bf16 v[16:19], a[116:119], a[52:55], v[16:19]\n"
    "v_exp_f32 v6, v6\n"
    "v_mfma_f32_16x16x32_bf16 v[16:19], a[120:123], a[56:59], v[16:19]\n"
    "ds_read_b64_tr_b16 a[158:159], v79 offset:61440\n"
    "v_mfma_f32_16x16x32_bf16 v[16:19], a[124:127], a[60:63], v[16:19]\n"
    "v_exp_f32 v7, v7\n"
    "v_mfma_f32_16x16x32_bf16 v[20:23], a[96:99], a[64:67], 0\n"
    "v_exp_f32 v8, v8\n"
    "v_mfma_f32_16x16x32_bf16 v[20:23], a[100:103], a[68:71], v[20:23]\n"
    "v_exp_f32 v9, v9\n"
    "v_mfma_f32_16x16x32_bf16 v[20:23], a[104:107], a[72:75], v[20:23]\n"
    "v_exp_f32 v10, v10\n"
    "v_mfma_f32_16x16x32_bf16 v[20:23], a[108:111], a[76:79], v[20:23]\n"
    "v_exp_f32 v11, v11\n"
    "v_mfma_f32_16x16x32_bf16 v[20:23], a[112:115], a[80:83], v[20:23]\n"
    "v_cvt_pk_bf16_f32 v37, v6, v7\n"
    "v_cvt_pk_bf16_f32 v44, v8, v9\n"
    "v_mfma_f32_16x16x32_bf16 v[20:23], a[116:119], a[84:87], v[20:23]\n"
    "v_cvt_pk_bf16_f32 v45, v10, v11\n"
    "v_mfma_f32_16x16x32_bf16 v[20:23], a[120:123], a[88:91], v[20:23]\n"
    "v_mfma_f32_16x16x32_bf16 v[20:23], a[124:127], a[92:95], v[20:23]\n"
    "s_waitcnt vmcnt(14) lgkmcnt(0)\n"
    "s_barrier\n"
    "v_mfma_f32_16x16x32_bf16 v[224:227], a[128:131], v[48:51], v[224:227]\n"
    "ds_read_b128 a[96:99], v72 offset:0\n"
    "s_add_u32 m0, s91, 49152\n"
    "v_mfma_f32_16x16x32_bf16 v[224:227], a[132:135], v[52:55], v[224:227]\n"
    "buffer_load_dwordx4 v80, s[20:23], s55 offen lds\n"
    "v_mfma_f32_16x16x32_bf16 v[228:231], a[128:131], v[56:59], v[228:231]\n"
    "ds_read_b128 a[100:103], v73 offset:0\n"
    "v_fma_f32 v12, v12, s96, v91\n"
    "v_mfma_f32_16x16x32_bf16 v[228:231], a[132:135], v[60:63], v[228:231]\n"
    "ds_read_b128 a[104:107], v74 offset:0\n"
    "v_fma_f32 v13, v13, s96, v91\n"
    "v_mfma_f32_16x16x32_bf16 v[232:235], a[128:131], v[64:67], v[232:235]\n"
    "v_fma_f32 v14, v14, s96, v91\n"
    "v_fma_f32 v15, v15, s96, v91\n"
    "v_mfma_f32_16x16x32_bf16 v[232:235], a[132:135], v[68:71], v[232:235]\n"
    "buffer_load_dwordx4 v81, s[20:23], s55 offen offset:1024 nt lds\n"
    "v_mfma_f32_16x16x32_bf16 v[236:239], a[136:139], v[48:51], v[236:239]\n"
    "ds_read_b128 a[108:111], v75 offset:0\n"
    "v_fma_f32 v16, v16, s96, v92\n"
    "v_mfma_f32_16x16x32_bf16 v[236:239], a[140:143], v[52:55], v[236:239]\n"
    "ds_read_b128  a[228:231], v90 offset:13312\n"
    "v_fma_f32 v17, v17, s96, v92\n"
    "v_mfma_f32_16x16x32_bf16 v[240:243], a[136:139], v[56:59], v[240:243]\n"
    "ds_read_b128 a[112:115], v72 offset:256\n"
    "v_fma_f32 v18, v18, s96, v92\n"
    "v_mfma_f32_16x16x32_bf16 v[240:243], a[140:143], v[60:63], v[240:243]\n"
    "buffer_load_dwordx4 v82, s[24:27], s79 offen offset:2048 nt lds\n"
    "v_mfma_f32_16x16x32_bf16 v[244:247], a[136:139], v[64:67], v[244:247]\n"
    "ds_read_b128 a[116:119], v73 offset:256\n"
    "v_fma_f32 v19, v19, s96, v92\n"
    "v_mfma_f32_16x16x32_bf16 v[244:247], a[140:143], v[68:71], v[244:247]\n"
    "ds_read_b128 a[120:123], v74 offset:256\n"
    "v_fma_f32 v20, v20, s96, v93\n"
    "v_mfma_f32_16x16x32_bf16 v[248:251], a[144:147], v[48:51], v[248:251]\n"
    "ds_read_b128 a[124:127], v75 offset:256\n"
    "v_fma_f32 v21, v21, s96, v93\n"
    "v_mfma_f32_16x16x32_bf16 v[248:251], a[148:151], v[52:55], v[248:251]\n"
    "buffer_load_dwordx4 v83, s[24:27], s79 offen offset:3072 nt lds\n"
    "v_mfma_f32_16x16x32_bf16 v[252:255], a[144:147], v[56:59], v[252:255]\n"
    "v_fma_f32 v22, v22, s96, v93\n"
    "v_fma_f32 v23, v23, s96, v93\n"
    "v_mfma_f32_16x16x32_bf16 v[252:255], a[148:151], v[60:63], v[252:255]\n"
    "v_max3_f32 v97, v12, v13, v14\n"
    "v_max3_f32 v97, v97, v15, v16\n"
    "v_mfma_f32_16x16x32_bf16 a[224:227], a[144:147], v[64:67], a[224:227]\n"
    "v_max3_f32 v97, v97, v17, v18\n"
    "v_max3_f32 v97, v97, v19, v20\n"
    "v_mfma_f32_16x16x32_bf16 a[224:227], a[148:151], v[68:71], a[224:227]\n"
    "s_waitcnt lgkmcnt(4)\n"
    "buffer_store_dwordx4 a[228:231], v89, s[36:39], s40 offen offset:2304 nt\n"
    "v_mfma_f32_16x16x32_bf16 a[212:215], a[152:155], v[48:51], a[212:215]\n"
    "v_max3_f32 v97, v97, v21, v22\n"
    "v_max_f32 v97, v97, v23\n"
    "v_mfma_f32_16x16x32_bf16 a[212:215], a[156:159], v[52:55], a[212:215]\n"
    "v_cmp_lt_f32 vcc, s97, v97\n"
    "s_add_u32 m0, s35, 12288\n"
    "v_mfma_f32_16x16x32_bf16 a[216:219], a[152:155], v[56:59], a[216:219]\n"
    "ds_read_b64_tr_b16 a[128:129], v76 offset:0\n"
    "ds_read_b64_tr_b16 a[130:131], v76 offset:4096\n"
    "v_mfma_f32_16x16x32_bf16 a[216:219], a[156:159], v[60:63], a[216:219]\n"
    "buffer_load_dwordx4 v85, s[28:31], s89 offen offset:1024 nt lds\n"
    "v_mfma_f32_16x16x32_bf16 a[220:223], a[152:155], v[64:67], a[220:223]\n"
    "ds_read_b64_tr_b16 a[132:133], v76 offset:8192\n"
    "s_cbranch_vccnz STUBb11c0_%=\n"
    "RETb11c0_%=:\n"
    "v_mfma_f32_16x16x32_bf16 a[220:223], a[156:159], v[68:71], a[220:223]\n"
    "ds_read_b64_tr_b16 a[134:135], v76 offset:12288\n"
    "s_waitcnt lgkmcnt(4)\n"
    "v_mfma_f32_16x16x32_bf16 v[0:3], a[96:99], a[0:3], 0\n"
    "v_exp_f32 v12, v12\n"
    "v_mfma_f32_16x16x32_bf16 v[0:3], a[100:103], a[4:7], v[0:3]\n"
    "ds_read_b64_tr_b16 a[136:137], v77 offset:0\n"
    "ds_read_b64_tr_b16 a[138:139], v77 offset:4096\n"
    "v_mfma_f32_16x16x32_bf16 v[0:3], a[104:107], a[8:11], v[0:3]\n"
    "v_exp_f32 v13, v13\n"
    "v_mfma_f32_16x16x32_bf16 v[0:3], a[108:111], a[12:15], v[0:3]\n"
    "ds_read_b64_tr_b16 a[140:141], v77 offset:8192\n"
    "v_cvt_pk_bf16_f32 v30, v12, v13\n"
    "v_mfma_f32_16x16x32_bf16 v[0:3], a[112:115], a[16:19], v[0:3]\n"
    "ds_read_b64_tr_b16 a[142:143], v77 offset:12288\n"
    "v_mfma_f32_16x16x32_bf16 v[0:3], a[116:119], a[20:23], v[0:3]\n"
    "v_exp_f32 v14, v14\n"
    "v_mfma_f32_16x16x32_bf16 v[0:3], a[120:123], a[24:27], v[0:3]\n"
    "ds_read_b64_tr_b16 a[144:145], v78 offset:0\n"
    "ds_read_b64_tr_b16 a[146:147], v78 offset:4096\n"
    "v_mfma_f32_16x16x32_bf16 v[0:3], a[124:127], a[28:31], v[0:3]\n"
    "v_exp_f32 v15, v15\n"
    "v_mfma_f32_16x16x32_bf16 v[4:7], a[96:99], a[32:35], 0\n"
    "ds_read_b64_tr_b16 a[148:149], v78 offset:8192\n"
    "v_cvt_pk_bf16_f32 v31, v14, v15\n"
    "v_mfma_f32_16x16x32_bf16 v[4:7], a[100:103], a[36:39], v[4:7]\n"
    "ds_read_b64_tr_b16 a[150:151], v78 offset:12288\n"
    "v_mfma_f32_16x16x32_bf16 v[4:7], a[104:107], a[40:43], v[4:7]\n"
    "v_exp_f32 v16, v16\n"
    "v_mfma_f32_16x16x32_bf16 v[4:7], a[108:111], a[44:47], v[4:7]\n"
    "ds_read_b64_tr_b16 a[152:153], v79 offset:0\n"
    "ds_read_b64_tr_b16 a[154:155], v79 offset:4096\n"
    "v_mfma_f32_16x16x32_bf16 v[4:7], a[112:115], a[48:51], v[4:7]\n"
    "v_exp_f32 v17, v17\n"
    "v_mfma_f32_16x16x32_bf16 v[4:7], a[116:119], a[52:55], v[4:7]\n"
    "ds_read_b64_tr_b16 a[156:157], v79 offset:8192\n"
    "v_cvt_pk_bf16_f32 v38, v16, v17\n"
    "v_mfma_f32_16x16x32_bf16 v[4:7], a[120:123], a[56:59], v[4:7]\n"
    "ds_read_b64_tr_b16 a[158:159], v79 offset:12288\n"
    "v_mfma_f32_16x16x32_bf16 v[4:7], a[124:127], a[60:63], v[4:7]\n"
    "v_exp_f32 v18, v18\n"
    "v_mfma_f32_16x16x32_bf16 v[8:11], a[96:99], a[64:67], 0\n"
    "v_exp_f32 v19, v19\n"
    "v_mfma_f32_16x16x32_bf16 v[8:11], a[100:103], a[68:71], v[8:11]\n"
    "v_exp_f32 v20, v20\n"
    "v_mfma_f32_16x16x32_bf16 v[8:11], a[104:107], a[72:75], v[8:11]\n"
    "v_exp_f32 v21, v21\n"
    "v_mfma_f32_16x16x32_bf16 v[8:11], a[108:111], a[76:79], v[8:11]\n"
    "v_exp_f32 v22, v22\n"
    "v_mfma_f32_16x16x32_bf16 v[8:11], a[112:115], a[80:83], v[8:11]\n"
    "v_exp_f32 v23, v23\n"
    "v_mfma_f32_16x16x32_bf16 v[8:11], a[116:119], a[84:87], v[8:11]\n"
    "v_cvt_pk_bf16_f32 v39, v18, v19\n"
    "v_cvt_pk_bf16_f32 v46, v20, v21\n"
    "v_mfma_f32_16x16x32_bf16 v[8:11], a[120:123], a[88:91], v[8:11]\n"
    "v_cvt_pk_bf16_f32 v47, v22, v23\n"
    "v_mfma_f32_16x16x32_bf16 v[8:11], a[124:127], a[92:95], v[8:11]\n"
    "s_waitcnt vmcnt(14) lgkmcnt(0)\n"
    "s_barrier\n"
    "v_mfma_f32_16x16x32_bf16 a[164:167], a[160:163], v[24:27], a[164:167]\n"
    "v_fma_f32 v0, v0, s96, v91\n"
    "s_add_u32 m0, s91, 0\n"
    "v_mfma_f32_16x16x32_bf16 a[164:167], a[160:163], v[28:31], a[164:167]\n"
    "buffer_load_dwordx4 v80, s[20:23], s56 offen lds\n"
    "v_mfma_f32_16x16x32_bf16 v[116:119], a[128:131], v[24:27], v[116:119]\n"
    "ds_read_b128 a[96:99], v72 offset:16384\n"
    "v_fma_f32 v1, v1, s96, v91\n"
    "v_mfma_f32_16x16x32_bf16 v[116:119], a[132:135], v[28:31], v[116:119]\n"
    "ds_read_b128 a[100:103], v73 offset:16384\n"
    "v_fma_f32 v2, v2, s96, v91\n"
    "v_mfma_f32_16x16x32_bf16 v[120:123], a[128:131], v[32:35], v[120:123]\n"
    "ds_read_b128 a[104:107], v74 offset:16384\n"
    "v_fma_f32 v3, v3, s96, v91\n"
    "v_mfma_f32_16x16x32_bf16 v[120:123], a[132:135], v[36:39], v[120:123]\n"
    "buffer_load_dwordx4 v81, s[20:23], s56 offen offset:1024 nt lds\n"
    "v_mfma_f32_16x16x32_bf16 v[124:127], a[128:131], v[40:43], v[124:127]\n"
    "ds_read_b128 a[108:111], v75 offset:16384\n"
    "v_fma_f32 v4, v4, s96, v92\n"
    "v_mfma_f32_16x16x32_bf16 v[124:127], a[132:135], v[44:47], v[124:127]\n"
    "ds_read_b128  a[228:231], v88 offset:14336\n"
    "v_fma_f32 v5, v5, s96, v92\n"
    "v_mfma_f32_16x16x32_bf16 v[128:131], a[136:139], v[24:27], v[128:131]\n"
    "ds_read_b128 a[112:115], v72 offset:16640\n"
    "v_fma_f32 v6, v6, s96, v92\n"
    "v_mfma_f32_16x16x32_bf16 v[128:131], a[140:143], v[28:31], v[128:131]\n"
    "buffer_load_dwordx4 v82, s[24:27], s80 offen offset:2048 nt lds\n"
    "v_mfma_f32_16x16x32_bf16 v[132:135], a[136:139], v[32:35], v[132:135]\n"
    "ds_read_b128 a[116:119], v73 offset:16640\n"
    "v_fma_f32 v7, v7, s96, v92\n"
    "v_mfma_f32_16x16x32_bf16 v[132:135], a[140:143], v[36:39], v[132:135]\n"
    "ds_read_b128 a[120:123], v74 offset:16640\n"
    "v_fma_f32 v8, v8, s96, v93\n"
    "v_mfma_f32_16x16x32_bf16 v[136:139], a[136:139], v[40:43], v[136:139]\n"
    "v_fma_f32 v9, v9, s96, v93\n"
    "v_fma_f32 v10, v10, s96, v93\n"
    "v_mfma_f32_16x16x32_bf16 v[136:139], a[140:143], v[44:47], v[136:139]\n"
    "buffer_load_dwordx4 v83, s[24:27], s80 offen offset:3072 nt lds\n"
    "v_mfma_f32_16x16x32_bf16 v[140:143], a[144:147], v[24:27], v[140:143]\n"
    "ds_read_b128 a[124:127], v75 offset:16640\n"
    "v_fma_f32 v11, v11, s96, v93\n"
    "v_mfma_f32_16x16x32_bf16 v[140:143], a[148:151], v[28:31], v[140:143]\n"
    "v_max3_f32 v97, v0, v1, v2\n"
    "v_max3_f32 v97, v97, v3, v4\n"
    "v_mfma_f32_16x16x32_bf16 v[144:147], a[144:147], v[32:35], v[144:147]\n"
    "v_max3_f32 v97, v97, v5, v6\n"
    "v_max3_f32 v97, v97, v7, v8\n"
    "v_mfma_f32_16x16x32_bf16 v[144:147], a[148:151], v[36:39], v[144:147]\n"
    "s_waitcnt lgkmcnt(4)\n"
    "buffer_store_dwordx4 a[228:231], v89, s[36:39], s88 offen offset:256 nt\n"
    "v_mfma_f32_16x16x32_bf16 v[148:151], a[144:147], v[40:43], v[148:151]\n"
    "v_max3_f32 v97, v97, v9, v10\n"
    "v_max_f32 v97, v97, v11\n"
    "v_mfma_f32_16x16x32_bf16 v[148:151], a[148:151], v[44:47], v[148:151]\n"
    "v_cmp_lt_f32 vcc, s97, v97\n"
    "s_add_u32 m0, s35, 12288\n"
    "v_mfma_f32_16x16x32_bf16 a[176:179], a[152:155], v[24:27], a[176:179]\n"
    "v_mfma_f32_16x16x32_bf16 a[176:179], a[156:159], v[28:31], a[176:179]\n"
    "buffer_load_dwordx4 v86, s[28:31], s89 offen offset:2048 nt lds\n"
    "v_mfma_f32_16x16x32_bf16 a[180:183], a[152:155], v[32:35], a[180:183]\n"
    "ds_read_b64_tr_b16 a[128:129], v76 offset:16384\n"
    "s_cbranch_vccnz STUBb12c0_%=\n"
    "RETb12c0_%=:\n"
    "v_mfma_f32_16x16x32_bf16 a[180:183], a[156:159], v[36:39], a[180:183]\n"
    "ds_read_b64_tr_b16 a[130:131], v76 offset:20480\n"
    "ds_read_b64_tr_b16 a[132:133], v76 offset:24576\n"
    "v_mfma_f32_16x16x32_bf16 a[184:187], a[152:155], v[40:43], a[184:187]\n"
    "v_exp_f32 v0, v0\n"
    "v_mfma_f32_16x16x32_bf16 a[184:187], a[156:159], v[44:47], a[184:187]\n"
    "ds_read_b64_tr_b16 a[134:135], v76 offset:28672\n"
    "s_waitcnt lgkmcnt(4)\n"
    "v_mfma_f32_16x16x32_bf16 v[12:15], a[96:99], a[0:3], 0\n"
    "v_exp_f32 v1, v1\n"
    "v_mfma_f32_16x16x32_bf16 v[12:15], a[100:103], a[4:7], v[12:15]\n"
    "ds_read_b64_tr_b16 a[136:137], v77 offset:16384\n"
    "v_cvt_pk_bf16_f32 v48, v0, v1\n"
    "v_mfma_f32_16x16x32_bf16 v[12:15], a[104:107], a[8:11], v[12:15]\n"
    "ds_read_b64_tr_b16 a[138:139], v77 offset:20480\n"
    "ds_read_b64_tr_b16 a[140:141], v77 offset:24576\n"
    "v_mfma_f32_16x16x32_bf16 v[12:15], a[108:111], a[12:15], v[12:15]\n"
    "v_exp_f32 v2, v2\n"
    "v_mfma_f32_16x16x32_bf16 v[12:15], a[112:115], a[16:19], v[12:15]\n"
    "ds_read_b64_tr_b16 a[142:143], v77 offset:28672\n"
    "v_mfma_f32_16x16x32_bf16 v[12:15], a[116:119], a[20:23], v[12:15]\n"
    "v_exp_f32 v3, v3\n"
    "v_mfma_f32_16x16x32_bf16 v[12:15], a[120:123], a[24:27], v[12:15]\n"
    "ds_read_b64_tr_b16 a[144:145], v78 offset:16384\n"
    "v_cvt_pk_bf16_f32 v49, v2, v3\n"
    "v_mfma_f32_16x16x32_bf16 v[12:15], a[124:127], a[28:31], v[12:15]\n"
    "ds_read_b64_tr_b16 a[146:147], v78 offset:20480\n"
    "ds_read_b64_tr_b16 a[148:149], v78 offset:24576\n"
    "v_mfma_f32_16x16x32_bf16 v[16:19], a[96:99], a[32:35], 0\n"
    "v_exp_f32 v4, v4\n"
    "v_mfma_f32_16x16x32_bf16 v[16:19], a[100:103], a[36:39], v[16:19]\n"
    "ds_read_b64_tr_b16 a[150:151], v78 offset:28672\n"
    "v_mfma_f32_16x16x32_bf16 v[16:19], a[104:107], a[40:43], v[16:19]\n"
    "v_exp_f32 v5, v5\n"
    "v_mfma_f32_16x16x32_bf16 v[16:19], a[108:111], a[44:47], v[16:19]\n"
    "ds_read_b64_tr_b16 a[152:153], v79 offset:16384\n"
    "v_cvt_pk_bf16_f32 v56, v4, v5\n"
    "v_mfma_f32_16x16x32_bf16 v[16:19], a[112:115], a[48:51], v[16:19]\n"
    "ds_read_b64_tr_b16 a[154:155], v79 offset:20480\n"
    "ds_read_b64_tr_b16 a[156:157], v79 offset:24576\n"
    "v_mfma_f32_16x16x32_bf16 v[16:19], a[116:119], a[52:55], v[16:19]\n"
    "v_exp_f32 v6, v6\n"
    "v_mfma_f32_16x16x32_bf16 v[16:19], a[120:123], a[56:59], v[16:19]\n"
    "ds_read_b64_tr_b16 a[158:159], v79 offset:28672\n"
    "v_mfma_f32_16x16x32_bf16 v[16:19], a[124:127], a[60:63], v[16:19]\n"
    "v_exp_f32 v7, v7\n"
    "v_mfma_f32_16x16x32_bf16 v[20:23], a[96:99], a[64:67], 0\n"
    "v_exp_f32 v8, v8\n"
    "v_mfma_f32_16x16x32_bf16 v[20:23], a[100:103], a[68:71], v[20:23]\n"
    "v_exp_f32 v9, v9\n"
    "v_mfma_f32_16x16x32_bf16 v[20:23], a[104:107], a[72:75], v[20:23]\n"
    "v_exp_f32 v10, v10\n"
    "v_mfma_f32_16x16x32_bf16 v[20:23], a[108:111], a[76:79], v[20:23]\n"
    "v_exp_f32 v11, v11\n"
    "v_mfma_f32_16x16x32_bf16 v[20:23], a[112:115], a[80:83], v[20:23]\n"
    "v_cvt_pk_bf16_f32 v57, v6, v7\n"
    "v_cvt_pk_bf16_f32 v64, v8, v9\n"
    "v_mfma_f32_16x16x32_bf16 v[20:23], a[116:119], a[84:87], v[20:23]\n"
    "v_cvt_pk_bf16_f32 v65, v10, v11\n"
    "v_mfma_f32_16x16x32_bf16 v[20:23], a[120:123], a[88:91], v[20:23]\n"
    "v_mfma_f32_16x16x32_bf16 v[20:23], a[124:127], a[92:95], v[20:23]\n"
    "s_waitcnt vmcnt(14) lgkmcnt(0)\n"
    "s_barrier\n"
    "v_mfma_f32_16x16x32_bf16 a[168:171], a[160:163], v[32:35], a[168:171]\n"
    "v_fma_f32 v12, v12, s96, v91\n"
    "s_add_u32 m0, s91, 16384\n"
    "v_mfma_f32_16x16x32_bf16 a[168:171], a[160:163], v[36:39], a[168:171]\n"
    "buffer_load_dwordx4 v80, s[20:23], s57 offen lds\n"
    "v_mfma_f32_16x16x32_bf16 v[152:155], a[128:131], v[24:27], v[152:155]\n"
    "ds_read_b128 a[96:99], v72 offset:32768\n"
    "v_fma_f32 v13, v13, s96, v91\n"
    "v_mfma_f32_16x16x32_bf16 v[152:155], a[132:135], v[28:31], v[152:155]\n"
    "ds_read_b128 a[100:103], v73 offset:32768\n"
    "v_fma_f32 v14, v14, s96, v91\n"
    "v_mfma_f32_16x16x32_bf16 v[156:159], a[128:131], v[32:35], v[156:159]\n"
    "ds_read_b128 a[104:107], v74 offset:32768\n"
    "v_fma_f32 v15, v15, s96, v91\n"
    "v_mfma_f32_16x16x32_bf16 v[156:159], a[132:135], v[36:39], v[156:159]\n"
    "buffer_load_dwordx4 v81, s[20:23], s57 offen offset:1024 nt lds\n"
    "v_mfma_f32_16x16x32_bf16 v[160:163], a[128:131], v[40:43], v[160:163]\n"
    "ds_read_b128 a[108:111], v75 offset:32768\n"
    "v_fma_f32 v16, v16, s96, v92\n"
    "v_mfma_f32_16x16x32_bf16 v[160:163], a[132:135], v[44:47], v[160:163]\n"
    "ds_read_b128  a[228:231], v90 offset:15360\n"
    "v_fma_f32 v17, v17, s96, v92\n"
    "v_mfma_f32_16x16x32_bf16 v[164:167], a[136:139], v[24:27], v[164:167]\n"
    "ds_read_b128 a[112:115], v72 offset:33024\n"
    "v_fma_f32 v18, v18, s96, v92\n"
    "v_mfma_f32_16x16x32_bf16 v[164:167], a[140:143], v[28:31], v[164:167]\n"
    "buffer_load_dwordx4 v82, s[24:27], s81 offen offset:2048 nt lds\n"
    "v_mfma_f32_16x16x32_bf16 v[168:171], a[136:139], v[32:35], v[168:171]\n"
    "ds_read_b128 a[116:119], v73 offset:33024\n"
    "v_fma_f32 v19, v19, s96, v92\n"
    "v_mfma_f32_16x16x32_bf16 v[168:171], a[140:143], v[36:39], v[168:171]\n"
    "ds_read_b128 a[120:123], v74 offset:33024\n"
    "v_fma_f32 v20, v20, s96, v93\n"
    "v_mfma_f32_16x16x32_bf16 v[172:175], a[136:139], v[40:43], v[172:175]\n"
    "v_fma_f32 v21, v21, s96, v93\n"
    "v_fma_f32 v22, v22, s96, v93\n"
    "v_mfma_f32_16x16x32_bf16 v[172:175], a[140:143], v[44:47], v[172:175]\n"
    "buffer_load_dwordx4 v83, s[24:27], s81 offen offset:3072 nt lds\n"
    "v_mfma_f32_16x16x32_bf16 v[176:179], a[144:147], v[24:27], v[176:179]\n"
    "ds_read_b128 a[124:127], v75 offset:33024\n"
    "v_fma_f32 v23, v23, s96, v93\n"
    "v_mfma_f32_16x16x32_bf16 v[176:179], a[148:151], v[28:31], v[176:179]\n"
    "v_max3_f32 v97, v12, v13, v14\n"
    "v_max3_f32 v97, v97, v15, v16\n"
    "v_mfma_f32_16x16x32_bf16 v[180:183], a[144:147], v[32:35], v[180:183]\n"
    "v_max3_f32 v97, v97, v17, v18\n"
    "v_max3_f32 v97, v97, v19, v20\n"
    "v_mfma_f32_16x16x32_bf16 v[180:183], a[148:151], v[36:39], v[180:183]\n"
    "s_waitcnt lgkmcnt(4)\n"
    "buffer_store_dwordx4 a[228:231], v89, s[36:39], s88 offen offset:2304 nt\n"
    "v_mfma_f32_16x16x32_bf16 v[184:187], a[144:147], v[40:43], v[184:187]\n"
    "v_max3_f32 v97, v97, v21, v22\n"
    "v_max_f32 v97, v97, v23\n"
    "v_mfma_f32_16x16x32_bf16 v[184:187], a[148:151], v[44:47], v[184:187]\n"
    "v_cmp_lt_f32 vcc, s97, v97\n"
    "s_add_u32 m0, s35, 12288\n"
    "v_mfma_f32_16x16x32_bf16 a[188:191], a[152:155], v[24:27], a[188:191]\n"
    "v_mfma_f32_16x16x32_bf16 a[188:191], a[156:159], v[28:31], a[188:191]\n"
    "buffer_load_dwordx4 v87, s[28:31], s89 offen offset:3072 nt lds\n"
    "v_mfma_f32_16x16x32_bf16 a[192:195], a[152:155], v[32:35], a[192:195]\n"
    "ds_read_b64_tr_b16 a[128:129], v76 offset:32768\n"
    "s_cbranch_vccnz STUBb13c0_%=\n"
    "RETb13c0_%=:\n"
    "v_mfma_f32_16x16x32_bf16 a[192:195], a[156:159], v[36:39], a[192:195]\n"
    "ds_read_b64_tr_b16 a[130:131], v76 offset:36864\n"
    "ds_read_b64_tr_b16 a[132:133], v76 offset:40960\n"
    "v_mfma_f32_16x16x32_bf16 a[196:199], a[152:155], v[40:43], a[196:199]\n"
    "v_exp_f32 v12, v12\n"
    "v_mfma_f32_16x16x32_bf16 a[196:199], a[156:159], v[44:47], a[196:199]\n"
    "ds_read_b64_tr_b16 a[134:135], v76 offset:45056\n"
    "s_waitcnt lgkmcnt(4)\n"
    "v_mfma_f32_16x16x32_bf16 v[0:3], a[96:99], a[0:3], 0\n"
    "v_exp_f32 v13, v13\n"
    "v_mfma_f32_16x16x32_bf16 v[0:3], a[100:103], a[4:7], v[0:3]\n"
    "ds_read_b64_tr_b16 a[136:137], v77 offset:32768\n"
    "v_cvt_pk_bf16_f32 v50, v12, v13\n"
    "v_mfma_f32_16x16x32_bf16 v[0:3], a[104:107], a[8:11], v[0:3]\n"
    "ds_read_b64_tr_b16 a[138:139], v77 offset:36864\n"
    "ds_read_b64_tr_b16 a[140:141], v77 offset:40960\n"
    "v_mfma_f32_16x16x32_bf16 v[0:3], a[108:111], a[12:15], v[0:3]\n"
    "v_exp_f32 v14, v14\n"
    "v_mfma_f32_16x16x32_bf16 v[0:3], a[112:115], a[16:19], v[0:3]\n"
    "ds_read_b64_tr_b16 a[142:143], v77 offset:45056\n"
    "v_mfma_f32_16x16x32_bf16 v[0:3], a[116:119], a[20:23], v[0:3]\n"
    "v_exp_f32 v15, v15\n"
    "v_mfma_f32_16x16x32_bf16 v[0:3], a[120:123], a[24:27], v[0:3]\n"
    "ds_read_b64_tr_b16 a[144:145], v78 offset:32768\n"
    "v_cvt_pk_bf16_f32 v51, v14, v15\n"
    "v_mfma_f32_16x16x32_bf16 v[0:3], a[124:127], a[28:31], v[0:3]\n"
    "ds_read_b64_tr_b16 a[146:147], v78 offset:36864\n"
    "ds_read_b64_tr_b16 a[148:149], v78 offset:40960\n"
    "v_mfma_f32_16x16x32_bf16 v[4:7], a[96:99], a[32:35], 0\n"
    "v_exp_f32 v16, v16\n"
    "v_mfma_f32_16x16x32_bf16 v[4:7], a[100:103], a[36:39], v[4:7]\n"
    "ds_read_b64_tr_b16 a[150:151], v78 offset:45056\n"
    "v_mfma_f32_16x16x32_bf16 v[4:7], a[104:107], a[40:43], v[4:7]\n"
    "v_exp_f32 v17, v17\n"
    "v_mfma_f32_16x16x32_bf16 v[4:7], a[108:111], a[44:47], v[4:7]\n"
    "ds_read_b64_tr_b16 a[152:153], v79 offset:32768\n"
    "v_cvt_pk_bf16_f32 v58, v16, v17\n"
    "v_mfma_f32_16x16x32_bf16 v[4:7], a[112:115], a[48:51], v[4:7]\n"
    "ds_read_b64_tr_b16 a[154:155], v79 offset:36864\n"
    "ds_read_b64_tr_b16 a[156:157], v79 offset:40960\n"
    "v_mfma_f32_16x16x32_bf16 v[4:7], a[116:119], a[52:55], v[4:7]\n"
    "v_exp_f32 v18, v18\n"
    "v_mfma_f32_16x16x32_bf16 v[4:7], a[120:123], a[56:59], v[4:7]\n"
    "ds_read_b64_tr_b16 a[158:159], v79 offset:45056\n"
    "v_mfma_f32_16x16x32_bf16 v[4:7], a[124:127], a[60:63], v[4:7]\n"
    "v_exp_f32 v19, v19\n"
    "v_mfma_f32_16x16x32_bf16 v[8:11], a[96:99], a[64:67], 0\n"
    "v_exp_f32 v20, v20\n"
    "v_mfma_f32_16x16x32_bf16 v[8:11], a[100:103], a[68:71], v[8:11]\n"
    "v_exp_f32 v21, v21\n"
    "v_mfma_f32_16x16x32_bf16 v[8:11], a[104:107], a[72:75], v[8:11]\n"
    "v_exp_f32 v22, v22\n"
    "v_mfma_f32_16x16x32_bf16 v[8:11], a[108:111], a[76:79], v[8:11]\n"
    "v_exp_f32 v23, v23\n"
    "v_mfma_f32_16x16x32_bf16 v[8:11], a[112:115], a[80:83], v[8:11]\n"
    "v_cvt_pk_bf16_f32 v59, v18, v19\n"
    "v_cvt_pk_bf16_f32 v66, v20, v21\n"
    "v_mfma_f32_16x16x32_bf16 v[8:11], a[116:119], a[84:87], v[8:11]\n"
    "v_cvt_pk_bf16_f32 v67, v22, v23\n"
    "v_mfma_f32_16x16x32_bf16 v[8:11], a[120:123], a[88:91], v[8:11]\n"
    "v_mfma_f32_16x16x32_bf16 v[8:11], a[124:127], a[92:95], v[8:11]\n"
    "s_waitcnt vmcnt(14) lgkmcnt(0)\n"
    "s_barrier\n"
    "v_mfma_f32_16x16x32_bf16 a[172:175], a[160:163], v[40:43], a[172:175]\n"
    "v_fma_f32 v0, v0, s96, v91\n"
    "s_add_u32 m0, s91, 32768\n"
    "v_mfma_f32_16x16x32_bf16 a[172:175], a[160:163], v[44:47], a[172:175]\n"
    "buffer_load_dwordx4 v80, s[20:23], s58 offen lds\n"
    "v_mfma_f32_16x16x32_bf16 v[188:191], a[128:131], v[24:27], v[188:191]\n"
    "ds_read_b128 a[96:99], v72 offset:49152\n"
    "v_fma_f32 v1, v1, s96, v91\n"
    "v_mfma_f32_16x16x32_bf16 v[188:191], a[132:135], v[28:31], v[188:191]\n"
    "ds_read_b128 a[100:103], v73 offset:49152\n"
    "v_fma_f32 v2, v2, s96, v91\n"
    "v_mfma_f32_16x16x32_bf16 v[192:195], a[128:131], v[32:35], v[192:195]\n"
    "ds_read_b128 a[104:107], v74 offset:49152\n"
    "v_fma_f32 v3, v3, s96, v91\n"
    "v_mfma_f32_16x16x32_bf16 v[192:195], a[132:135], v[36:39], v[192:195]\n"
    "buffer_load_dwordx4 v81, s[20:23], s58 offen offset:1024 nt lds\n"
    "v_mfma_f32_16x16x32_bf16 v[196:199], a[128:131], v[40:43], v[196:199]\n"
    "ds_read_b128 a[108:111], v75 offset:49152\n"
    "v_fma_f32 v4, v4, s96, v92\n"
    "v_mfma_f32_16x16x32_bf16 v[196:199], a[132:135], v[44:47], v[196:199]\n"
    "ds_read_b128  a[228:231], v88 offset:16384\n"
    "v_fma_f32 v5, v5, s96, v92\n"
    "v_mfma_f32_16x16x32_bf16 v[200:203], a[136:139], v[24:27], v[200:203]\n"
    "ds_read_b128 a[112:115], v72 offset:49408\n"
    "v_fma_f32 v6, v6, s96, v92\n"
    "v_mfma_f32_16x16x32_bf16 v[200:203], a[140:143], v[28:31], v[200:203]\n"
    "buffer_load_dwordx4 v82, s[24:27], s82 offen offset:2048 nt lds\n"
    "v_mfma_f32_16x16x32_bf16 v[204:207], a[136:139], v[32:35], v[204:207]\n"
    "ds_read_b128 a[116:119], v73 offset:49408\n"
    "v_fma_f32 v7, v7, s96, v92\n"
    "v_mfma_f32_16x16x32_bf16 v[204:207], a[140:143], v[36:39], v[204:207]\n"
    "ds_read_b128 a[120:123], v74 offset:49408\n"
    "v_fma_f32 v8, v8, s96, v93\n"
    "v_mfma_f32_16x16x32_bf16 v[208:211], a[136:139], v[40:43], v[208:211]\n"
    "v_fma_f32 v9, v9, s96, v93\n"
    "v_fma_f32 v10, v10, s96, v93\n"
    "v_mfma_f32_16x16x32_bf16 v[208:211], a[140:143], v[44:47], v[208:211]\n"
    "buffer_load_dwordx4 v83, s[24:27], s82 offen offset:3072 nt lds\n"
    "v_mfma_f32_16x16x32_bf16 v[212:215], a[144:147], v[24:27], v[212:215]\n"
    "ds_read_b128 a[124:127], v75 offset:49408\n"
    "v_fma_f32 v11, v11, s96, v93\n"
    "v_mfma_f32_16x16x32_bf16 v[212:215], a[148:151], v[28:31], v[212:215]\n"
    "v_max3_f32 v97, v0, v1, v2\n"
    "v_max3_f32 v97, v97, v3, v4\n"
    "v_mfma_f32_16x16x32_bf16 v[216:219], a[144:147], v[32:35], v[216:219]\n"
    "v_max3_f32 v97, v97, v5, v6\n"
    "v_max3_f32 v97, v97, v7, v8\n"
    "v_mfma_f32_16x16x32_bf16 v[216:219], a[148:151], v[36:39], v[216:219]\n"
    "s_waitcnt lgkmcnt(4)\n"
    "buffer_store_dwordx4 a[228:231], v89, s[36:39], s41 offen offset:256 nt\n"
    "v_mfma_f32_16x16x32_bf16 v[220:223], a[144:147], v[40:43], v[220:223]\n"
    "v_max3_f32 v97, v97, v9, v10\n"
    "v_max_f32 v97, v97, v11\n"
    "v_mfma_f32_16x16x32_bf16 v[220:223], a[148:151], v[44:47], v[220:223]\n"
    "v_cmp_lt_f32 vcc, s97, v97\n"
    "s_add_u32 m0, s35, 16384\n"
    "v_mfma_f32_16x16x32_bf16 a[200:203], a[152:155], v[24:27], a[200:203]\n"
    "v_mfma_f32_16x16x32_bf16 a[200:203], a[156:159], v[28:31], a[200:203]\n"
    "buffer_load_dwordx4 v84, s[28:31], s42 offen nt lds\n"
    "v_mfma_f32_16x16x32_bf16 a[204:207], a[152:155], v[32:35], a[204:207]\n"
    "ds_read_b64_tr_b16 a[128:129], v76 offset:49152\n"
    "s_cbranch_vccnz STUBb14c0_%=\n"
    "RETb14c0_%=:\n"
    "v_mfma_f32_16x16x32_bf16 a[204:207], a[156:159], v[36:39], a[204:207]\n"
    "ds_read_b64_tr_b16 a[130:131], v76 offset:53248\n"
    "ds_read_b64_tr_b16 a[132:133], v76 offset:57344\n"
    "v_mfma_f32_16x16x32_bf16 a[208:211], a[152:155], v[40:43], a[208:211]\n"
    "v_exp_f32 v0, v0\n"
    "v_mfma_f32_16x16x32_bf16 a[208:211], a[156:159], v[44:47], a[208:211]\n"
    "ds_read_b64_tr_b16 a[134:135], v76 offset:61440\n"
    "s_waitcnt lgkmcnt(4)\n"
    "v_mfma_f32_16x16x32_bf16 v[12:15], a[96:99], a[0:3], 0\n"
    "v_exp_f32 v1, v1\n"
    "v_mfma_f32_16x16x32_bf16 v[12:15], a[100:103], a[4:7], v[12:15]\n"
    "ds_read_b64_tr_b16 a[136:137], v77 offset:49152\n"
    "v_cvt_pk_bf16_f32 v52, v0, v1\n"
    "v_mfma_f32_16x16x32_bf16 v[12:15], a[104:107], a[8:11], v[12:15]\n"
    "ds_read_b64_tr_b16 a[138:139], v77 offset:53248\n"
    "ds_read_b64_tr_b16 a[140:141], v77 offset:57344\n"
    "v_mfma_f32_16x16x32_bf16 v[12:15], a[108:111], a[12:15], v[12:15]\n"
    "v_exp_f32 v2, v2\n"
    "v_mfma_f32_16x16x32_bf16 v[12:15], a[112:115], a[16:19], v[12:15]\n"
    "ds_read_b64_tr_b16 a[142:143], v77 offset:61440\n"
    "v_mfma_f32_16x16x32_bf16 v[12:15], a[116:119], a[20:23], v[12:15]\n"
    "v_exp_f32 v3, v3\n"
    "v_mfma_f32_16x16x32_bf16 v[12:15], a[120:123], a[24:27], v[12:15]\n"
    "ds_read_b64_tr_b16 a[144:145], v78 offset:49152\n"
    "v_cvt_pk_bf16_f32 v53, v2, v3\n"
    "v_mfma_f32_16x16x32_bf16 v[12:15], a[124:127], a[28:31], v[12:15]\n"
    "ds_read_b64_tr_b16 a[146:147], v78 offset:53248\n"
    "ds_read_b64_tr_b16 a[148:149], v78 offset:57344\n"
    "v_mfma_f32_16x16x32_bf16 v[16:19], a[96:99], a[32:35], 0\n"
    "v_exp_f32 v4, v4\n"
    "v_mfma_f32_16x16x32_bf16 v[16:19], a[100:103], a[36:39], v[16:19]\n"
    "ds_read_b64_tr_b16 a[150:151], v78 offset:61440\n"
    "v_mfma_f32_16x16x32_bf16 v[16:19], a[104:107], a[40:43], v[16:19]\n"
    "v_exp_f32 v5, v5\n"
    "v_mfma_f32_16x16x32_bf16 v[16:19], a[108:111], a[44:47], v[16:19]\n"
    "ds_read_b64_tr_b16 a[152:153], v79 offset:49152\n"
    "v_cvt_pk_bf16_f32 v60, v4, v5\n"
    "v_mfma_f32_16x16x32_bf16 v[16:19], a[112:115], a[48:51], v[16:19]\n"
    "ds_read_b64_tr_b16 a[154:155], v79 offset:53248\n"
    "ds_read_b64_tr_b16 a[156:157], v79 offset:57344\n"
    "v_mfma_f32_16x16x32_bf16 v[16:19], a[116:119], a[52:55], v[16:19]\n"
    "v_exp_f32 v6, v6\n"
    "v_mfma_f32_16x16x32_bf16 v[16:19], a[120:123], a[56:59], v[16:19]\n"
    "ds_read_b64_tr_b16 a[158:159], v79 offset:61440\n"
    "v_mfma_f32_16x16x32_bf16 v[16:19], a[124:127], a[60:63], v[16:19]\n"
    "v_exp_f32 v7, v7\n"
    "v_mfma_f32_16x16x32_bf16 v[20:23], a[96:99], a[64:67], 0\n"
    "v_exp_f32 v8, v8\n"
    "v_mfma_f32_16x16x32_bf16 v[20:23], a[100:103], a[68:71], v[20:23]\n"
    "v_exp_f32 v9, v9\n"
    "v_mfma_f32_16x16x32_bf16 v[20:23], a[104:107], a[72:75], v[20:23]\n"
    "v_exp_f32 v10, v10\n"
    "v_mfma_f32_16x16x32_bf16 v[20:23], a[108:111], a[76:79], v[20:23]\n"
    "v_exp_f32 v11, v11\n"
    "v_mfma_f32_16x16x32_bf16 v[20:23], a[112:115], a[80:83], v[20:23]\n"
    "v_cvt_pk_bf16_f32 v61, v6, v7\n"
    "v_cvt_pk_bf16_f32 v68, v8, v9\n"
    "v_mfma_f32_16x16x32_bf16 v[20:23], a[116:119], a[84:87], v[20:23]\n"
    "v_cvt_pk_bf16_f32 v69, v10, v11\n"
    "v_mfma_f32_16x16x32_bf16 v[20:23], a[120:123], a[88:91], v[20:23]\n"
    "v_mfma_f32_16x16x32_bf16 v[20:23], a[124:127], a[92:95], v[20:23]\n"
    "s_waitcnt vmcnt(14) lgkmcnt(0)\n"
    "s_barrier\n"
    "v_mfma_f32_16x16x32_bf16 v[224:227], a[128:131], v[24:27], v[224:227]\n"
    "ds_read_b128 a[96:99], v72 offset:0\n"
    "s_add_u32 m0, s91, 49152\n"
    "v_mfma_f32_16x16x32_bf16 v[224:227], a[132:135], v[28:31], v[224:227]\n"
    "buffer_load_dwordx4 v80, s[20:23], s59 offen lds\n"
    "v_mfma_f32_16x16x32_bf16 v[228:231], a[128:131], v[32:35], v[228:231]\n"
    "ds_read_b128 a[100:103], v73 offset:0\n"
    "v_fma_f32 v12, v12, s96, v91\n"
    "v_mfma_f32_16x16x32_bf16 v[228:231], a[132:135], v[36:39], v[228:231]\n"
    "ds_read_b128 a[104:107], v74 offset:0\n"
    "v_fma_f32 v13, v13, s96, v91\n"
    "v_mfma_f32_16x16x32_bf16 v[232:235], a[128:131], v[40:43], v[232:235]\n"
    "v_fma_f32 v14, v14, s96, v91\n"
    "v_fma_f32 v15, v15, s96, v91\n"
    "v_mfma_f32_16x16x32_bf16 v[232:235], a[132:135], v[44:47], v[232:235]\n"
    "buffer_load_dwordx4 v81, s[20:23], s59 offen offset:1024 nt lds\n"
    "v_mfma_f32_16x16x32_bf16 v[236:239], a[136:139], v[24:27], v[236:239]\n"
    "ds_read_b128 a[108:111], v75 offset:0\n"
    "v_fma_f32 v16, v16, s96, v92\n"
    "v_mfma_f32_16x16x32_bf16 v[236:239], a[140:143], v[28:31], v[236:239]\n"
    "ds_read_b128  a[228:231], v90 offset:17408\n"
    "v_fma_f32 v17, v17, s96, v92\n"
    "v_mfma_f32_16x16x32_bf16 v[240:243], a[136:139], v[32:35], v[240:243]\n"
    "ds_read_b128 a[112:115], v72 offset:256\n"
    "v_fma_f32 v18, v18, s96, v92\n"
    "v_mfma_f32_16x16x32_bf16 v[240:243], a[140:143], v[36:39], v[240:243]\n"
    "buffer_load_dwordx4 v82, s[24:27], s83 offen offset:2048 nt lds\n"
    "v_mfma_f32_16x16x32_bf16 v[244:247], a[136:139], v[40:43], v[244:247]\n"
    "ds_read_b128 a[116:119], v73 offset:256\n"
    "v_fma_f32 v19, v19, s96, v92\n"
    "v_mfma_f32_16x16x32_bf16 v[244:247], a[140:143], v[44:47], v[244:247]\n"
    "ds_read_b128 a[120:123], v74 offset:256\n"
    "v_fma_f32 v20, v20, s96, v93\n"
    "v_mfma_f32_16x16x32_bf16 v[248:251], a[144:147], v[24:27], v[248:251]\n"
    "ds_read_b128 a[124:127], v75 offset:256\n"
    "v_fma_f32 v21, v21, s96, v93\n"
    "v_mfma_f32_16x16x32_bf16 v[248:251], a[148:151], v[28:31], v[248:251]\n"
    "buffer_load_dwordx4 v83, s[24:27], s83 offen offset:3072 nt lds\n"
    "v_mfma_f32_16x16x32_bf16 v[252:255], a[144:147], v[32:35], v[252:255]\n"
    "v_fma_f32 v22, v22, s96, v93\n"
    "v_fma_f32 v23, v23, s96, v93\n"
    "v_mfma_f32_16x16x32_bf16 v[252:255], a[148:151], v[36:39], v[252:255]\n"
    "v_max3_f32 v97, v12, v13, v14\n"
    "v_max3_f32 v97, v97, v15, v16\n"
    "v_mfma_f32_16x16x32_bf16 a[224:227], a[144:147], v[40:43], a[224:227]\n"
    "v_max3_f32 v97, v97, v17, v18\n"
    "v_max3_f32 v97, v97, v19, v20\n"
    "v_mfma_f32_16x16x32_bf16 a[224:227], a[148:151], v[44:47], a[224:227]\n"
    "s_waitcnt lgkmcnt(4)\n"
    "buffer_store_dwordx4 a[228:231], v89, s[36:39], s41 offen offset:2304 nt\n"
    "v_mfma_f32_16x16x32_bf16 a[212:215], a[152:155], v[24:27], a[212:215]\n"
    "v_max3_f32 v97, v97, v21, v22\n"
    "v_max_f32 v97, v97, v23\n"
    "v_mfma_f32_16x16x32_bf16 a[212:215], a[156:159], v[28:31], a[212:215]\n"
    "v_cmp_lt_f32 vcc, s97, v97\n"
    "s_add_u32 m0, s35, 16384\n"
    "v_mfma_f32_16x16x32_bf16 a[216:219], a[152:155], v[32:35], a[216:219]\n"
    "ds_read_b64_tr_b16 a[128:129], v76 offset:0\n"
    "ds_read_b64_tr_b16 a[130:131], v76 offset:4096\n"
    "v_mfma_f32_16x16x32_bf16 a[216:219], a[156:159], v[36:39], a[216:219]\n"
    "buffer_load_dwordx4 v85, s[28:31], s42 offen offset:1024 nt lds\n"
    "v_mfma_f32_16x16x32_bf16 a[220:223], a[152:155], v[40:43], a[220:223]\n"
    "ds_read_b64_tr_b16 a[132:133], v76 offset:8192\n"
    "s_cbranch_vccnz STUBb15c0_%=\n"
    "RETb15c0_%=:\n"
    "v_mfma_f32_16x16x32_bf16 a[220:223], a[156:159], v[44:47], a[220:223]\n"
    "ds_read_b64_tr_b16 a[134:135], v76 offset:12288\n"
    "s_waitcnt lgkmcnt(4)\n"
    "v_mfma_f32_16x16x32_bf16 v[0:3], a[96:99], a[0:3], 0\n"
    "v_exp_f32 v12, v12\n"
    "v_mfma_f32_16x16x32_bf16 v[0:3], a[100:103], a[4:7], v[0:3]\n"
    "ds_read_b64_tr_b16 a[136:137], v77 offset:0\n"
    "ds_read_b64_tr_b16 a[138:139], v77 offset:4096\n"
    "v_mfma_f32_16x16x32_bf16 v[0:3], a[104:107], a[8:11], v[0:3]\n"
    "v_exp_f32 v13, v13\n"
    "v_mfma_f32_16x16x32_bf16 v[0:3], a[108:111], a[12:15], v[0:3]\n"
    "ds_read_b64_tr_b16 a[140:141], v77 offset:8192\n"
    "v_cvt_pk_bf16_f32 v54, v12, v13\n"
    "v_mfma_f32_16x16x32_bf16 v[0:3], a[112:115], a[16:19], v[0:3]\n"
    "ds_read_b64_tr_b16 a[142:143], v77 offset:12288\n"
    "v_mfma_f32_16x16x32_bf16 v[0:3], a[116:119], a[20:23], v[0:3]\n"
    "v_exp_f32 v14, v14\n"
    "v_mfma_f32_16x16x32_bf16 v[0:3], a[120:123], a[24:27], v[0:3]\n"
    "ds_read_b64_tr_b16 a[144:145], v78 offset:0\n"
    "ds_read_b64_tr_b16 a[146:147], v78 offset:4096\n"
    "v_mfma_f32_16x16x32_bf16 v[0:3], a[124:127], a[28:31], v[0:3]\n"
    "v_exp_f32 v15, v15\n"
    "v_mfma_f32_16x16x32_bf16 v[4:7], a[96:99], a[32:35], 0\n"
    "ds_read_b64_tr_b16 a[148:149], v78 offset:8192\n"
    "v_cvt_pk_bf16_f32 v55, v14, v15\n"
    "v_mfma_f32_16x16x32_bf16 v[4:7], a[100:103], a[36:39], v[4:7]\n"
    "ds_read_b64_tr_b16 a[150:151], v78 offset:12288\n"
    "v_mfma_f32_16x16x32_bf16 v[4:7], a[104:107], a[40:43], v[4:7]\n"
    "v_exp_f32 v16, v16\n"
    "v_mfma_f32_16x16x32_bf16 v[4:7], a[108:111], a[44:47], v[4:7]\n"
    "ds_read_b64_tr_b16 a[152:153], v79 offset:0\n"
    "ds_read_b64_tr_b16 a[154:155], v79 offset:4096\n"
    "v_mfma_f32_16x16x32_bf16 v[4:7], a[112:115], a[48:51], v[4:7]\n"
    "v_exp_f32 v17, v17\n"
    "v_mfma_f32_16x16x32_bf16 v[4:7], a[116:119], a[52:55], v[4:7]\n"
    "ds_read_b64_tr_b16 a[156:157], v79 offset:8192\n"
    "v_cvt_pk_bf16_f32 v62, v16, v17\n"
    "v_mfma_f32_16x16x32_bf16 v[4:7], a[120:123], a[56:59], v[4:7]\n"
    "ds_read_b64_tr_b16 a[158:159], v79 offset:12288\n"
    "v_mfma_f32_16x16x32_bf16 v[4:7], a[124:127], a[60:63], v[4:7]\n"
    "v_exp_f32 v18, v18\n"
    "v_mfma_f32_16x16x32_bf16 v[8:11], a[96:99], a[64:67], 0\n"
    "v_exp_f32 v19, v19\n"
    "v_mfma_f32_16x16x32_bf16 v[8:11], a[100:103], a[68:71], v[8:11]\n"
    "v_exp_f32 v20, v20\n"
    "v_mfma_f32_16x16x32_bf16 v[8:11], a[104:107], a[72:75], v[8:11]\n"
    "v_exp_f32 v21, v21\n"
    "v_mfma_f32_16x16x32_bf16 v[8:11], a[108:111], a[76:79], v[8:11]\n"
    "v_exp_f32 v22, v22\n"
    "v_mfma_f32_16x16x32_bf16 v[8:11], a[112:115], a[80:83], v[8:11]\n"
    "v_exp_f32 v23, v23\n"
    "v_mfma_f32_16x16x32_bf16 v[8:11], a[116:119], a[84:87], v[8:11]\n"
    "v_cvt_pk_bf16_f32 v63, v18, v19\n"
    "v_cvt_pk_bf16_f32 v70, v20, v21\n"
    "v_mfma_f32_16x16x32_bf16 v[8:11], a[120:123], a[88:91], v[8:11]\n"
    "v_cvt_pk_bf16_f32 v71, v22, v23\n"
    "v_mfma_f32_16x16x32_bf16 v[8:11], a[124:127], a[92:95], v[8:11]\n"
    "s_waitcnt vmcnt(14) lgkmcnt(0)\n"
    "s_barrier\n"
    "v_mfma_f32_16x16x32_bf16 a[164:167], a[160:163], v[48:51], a[164:167]\n"
    "v_fma_f32 v0, v0, s96, v91\n"
    "s_add_u32 m0, s91, 0\n"
    "v_mfma_f32_16x16x32_bf16 a[164:167], a[160:163], v[52:55], a[164:167]\n"
    "buffer_load_dwordx4 v80, s[20:23], s60 offen lds\n"
    "v_mfma_f32_16x16x32_bf16 v[116:119], a[128:131], v[48:51], v[116:119]\n"
    "ds_read_b128 a[96:99], v72 offset:16384\n"
    "v_fma_f32 v1, v1, s96, v91\n"
    "v_mfma_f32_16x16x32_bf16 v[116:119], a[132:135], v[52:55], v[116:119]\n"
    "ds_read_b128 a[100:103], v73 offset:16384\n"
    "v_fma_f32 v2, v2, s96, v91\n"
    "v_mfma_f32_16x16x32_bf16 v[120:123], a[128:131], v[56:59], v[120:123]\n"
    "ds_read_b128 a[104:107], v74 offset:16384\n"
    "v_fma_f32 v3, v3, s96, v91\n"
    "v_mfma_f32_16x16x32_bf16 v[120:123], a[132:135], v[60:63], v[120:123]\n"
    "buffer_load_dwordx4 v81, s[20:23], s60 offen offset:1024 nt lds\n"
    "v_mfma_f32_16x16x32_bf16 v[124:127], a[128:131], v[64:67], v[124:127]\n"
    "ds_read_b128 a[108:111], v75 offset:16384\n"
    "v_fma_f32 v4, v4, s96, v92\n"
    "v_mfma_f32_16x16x32_bf16 v[124:127], a[132:135], v[68:71], v[124:127]\n"
    "ds_read_b128  a[228:231], v88 offset:18432\n"
    "v_fma_f32 v5, v5, s96, v92\n"
    "v_mfma_f32_16x16x32_bf16 v[128:131], a[136:139], v[48:51], v[128:131]\n"
    "ds_read_b128 a[112:115], v72 offset:16640\n"
    "v_fma_f32 v6, v6, s96, v92\n"
    "v_mfma_f32_16x16x32_bf16 v[128:131], a[140:143], v[52:55], v[128:131]\n"
    "buffer_load_dwordx4 v82, s[24:27], s84 offen offset:2048 nt lds\n"
    "v_mfma_f32_16x16x32_bf16 v[132:135], a[136:139], v[56:59], v[132:135]\n"
    "ds_read_b128 a[116:119], v73 offset:16640\n"
    "v_fma_f32 v7, v7, s96, v92\n"
    "v_mfma_f32_16x16x32_bf16 v[132:135], a[140:143], v[60:63], v[132:135]\n"
    "ds_read_b128 a[120:123], v74 offset:16640\n"
    "v_fma_f32 v8, v8, s96, v93\n"
    "v_mfma_f32_16x16x32_bf16 v[136:139], a[136:139], v[64:67], v[136:139]\n"
    "v_fma_f32 v9, v9, s96, v93\n"
    "v_fma_f32 v10, v10, s96, v93\n"
    "v_mfma_f32_16x16x32_bf16 v[136:139], a[140:143], v[68:71], v[136:139]\n"
    "buffer_load_dwordx4 v83, s[24:27], s84 offen offset:3072 nt lds\n"
    "v_mfma_f32_16x16x32_bf16 v[140:143], a[144:147], v[48:51], v[140:143]\n"
    "ds_read_b128 a[124:127], v75 offset:16640\n"
    "v_fma_f32 v11, v11, s96, v93\n"
    "v_mfma_f32_16x16x32_bf16 v[140:143], a[148:151], v[52:55], v[140:143]\n"
    "v_max3_f32 v97, v0, v1, v2\n"
    "v_max3_f32 v97, v97, v3, v4\n"
    "v_mfma_f32_16x16x32_bf16 v[144:147], a[144:147], v[56:59], v[144:147]\n"
    "v_max3_f32 v97, v97, v5, v6\n"
    "v_max3_f32 v97, v97, v7, v8\n"
    "v_mfma_f32_16x16x32_bf16 v[144:147], a[148:151], v[60:63], v[144:147]\n"
    "s_waitcnt lgkmcnt(4)\n"
    "buffer_store_dwordx4 a[228:231], v89, s[36:39], s89 offen offset:256 nt\n"
    "v_mfma_f32_16x16x32_bf16 v[148:151], a[144:147], v[64:67], v[148:151]\n"
    "v_max3_f32 v97, v97, v9, v10\n"
    "v_max_f32 v97, v97, v11\n"
    "v_mfma_f32_16x16x32_bf16 v[148:151], a[148:151], v[68:71], v[148:151]\n"
    "v_cmp_lt_f32 vcc, s97, v97\n"
    "s_add_u32 m0, s35, 16384\n"
    "v_mfma_f32_16x16x32_bf16 a[176:179], a[152:155], v[48:51], a[176:179]\n"
    "v_mfma_f32_16x16x32_bf16 a[176:179], a[156:159], v[52:55], a[176:179]\n"
    "buffer_load_dwordx4 v86, s[28:31], s42 offen offset:2048 nt lds\n"
    "v_mfma_f32_16x16x32_bf16 a[180:183], a[152:155], v[56:59], a[180:183]\n"
    "ds_read_b64_tr_b16 a[128:129], v76 offset:16384\n"
    "s_cbranch_vccnz STUBb16c0_%=\n"
    "RETb16c0_%=:\n"
    "v_mfma_f32_16x16x32_bf16 a[180:183], a[156:159], v[60:63], a[180:183]\n"
    "ds_read_b64_tr_b16 a[130:131], v76 offset:20480\n"
    "ds_read_b64_tr_b16 a[132:133], v76 offset:24576\n"
    "v_mfma_f32_16x16x32_bf16 a[184:187], a[152:155], v[64:67], a[184:187]\n"
    "v_exp_f32 v0, v0\n"
    "v_mfma_f32_16x16x32_bf16 a[184:187], a[156:159], v[68:71], a[184:187]\n"
    "ds_read_b64_tr_b16 a[134:135], v76 offset:28672\n"
    "s_waitcnt lgkmcnt(4)\n"
    "v_mfma_f32_16x16x32_bf16 v[12:15], a[96:99], a[0:3], 0\n"
    "v_exp_f32 v1, v1\n"
    "v_mfma_f32_16x16x32_bf16 v[12:15], a[100:103], a[4:7], v[12:15]\n"
    "ds_read_b64_tr_b16 a[136:137], v77 offset:16384\n"
    "v_cvt_pk_bf16_f32 v24, v0, v1\n"
    "v_mfma_f32_16x16x32_bf16 v[12:15], a[104:107], a[8:11], v[12:15]\n"
    "ds_read_b64_tr_b16 a[138:139], v77 offset:20480\n"
    "ds_read_b64_tr_b16 a[140:141], v77 offset:24576\n"
    "v_mfma_f32_16x16x32_bf16 v[12:15], a[108:111], a[12:15], v[12:15]\n"
    "v_exp_f32 v2, v2\n"
    "v_mfma_f32_16x16x32_bf16 v[12:15], a[112:115], a[16:19], v[12:15]\n"
    "ds_read_b64_tr_b16 a[142:143], v77 offset:28672\n"
    "v_mfma_f32_16x16x32_bf16 v[12:15], a[116:119], a[20:23], v[12:15]\n"
    "v_exp_f32 v3, v3\n"
    "v_mfma_f32_16x16x32_bf16 v[12:15], a[120:123], a[24:27], v[12:15]\n"
    "ds_read_b64_tr_b16 a[144:145], v78 offset:16384\n"
    "v_cvt_pk_bf16_f32 v25, v2, v3\n"
    "v_mfma_f32_16x16x32_bf16 v[12:15], a[124:127], a[28:31], v[12:15]\n"
    "ds_read_b64_tr_b16 a[146:147], v78 offset:20480\n"
    "ds_read_b64_tr_b16 a[148:149], v78 offset:24576\n"
    "v_mfma_f32_16x16x32_bf16 v[16:19], a[96:99], a[32:35], 0\n"
    "v_exp_f32 v4, v4\n"
    "v_mfma_f32_16x16x32_bf16 v[16:19], a[100:103], a[36:39], v[16:19]\n"
    "ds_read_b64_tr_b16 a[150:151], v78 offset:28672\n"
    "v_mfma_f32_16x16x32_bf16 v[16:19], a[104:107], a[40:43], v[16:19]\n"
    "v_exp_f32 v5, v5\n"
    "v_mfma_f32_16x16x32_bf16 v[16:19], a[108:111], a[44:47], v[16:19]\n"
    "ds_read_b64_tr_b16 a[152:153], v79 offset:16384\n"
    "v_cvt_pk_bf16_f32 v32, v4, v5\n"
    "v_mfma_f32_16x16x32_bf16 v[16:19], a[112:115], a[48:51], v[16:19]\n"
    "ds_read_b64_tr_b16 a[154:155], v79 offset:20480\n"
    "ds_read_b64_tr_b16 a[156:157], v79 offset:24576\n"
    "v_mfma_f32_16x16x32_bf16 v[16:19], a[116:119], a[52:55], v[16:19]\n"
    "v_exp_f32 v6, v6\n"
    "v_mfma_f32_16x16x32_bf16 v[16:19], a[120:123], a[56:59], v[16:19]\n"
    "ds_read_b64_tr_b16 a[158:159], v79 offset:28672\n"
    "v_mfma_f32_16x16x32_bf16 v[16:19], a[124:127], a[60:63], v[16:19]\n"
    "v_exp_f32 v7, v7\n"
    "v_mfma_f32_16x16x32_bf16 v[20:23], a[96:99], a[64:67], 0\n"
    "v_exp_f32 v8, v8\n"
    "v_mfma_f32_16x16x32_bf16 v[20:23], a[100:103], a[68:71], v[20:23]\n"
    "v_exp_f32 v9, v9\n"
    "v_mfma_f32_16x16x32_bf16 v[20:23], a[104:107], a[72:75], v[20:23]\n"
    "v_exp_f32 v10, v10\n"
    "v_mfma_f32_16x16x32_bf16 v[20:23], a[108:111], a[76:79], v[20:23]\n"
    "v_exp_f32 v11, v11\n"
    "v_mfma_f32_16x16x32_bf16 v[20:23], a[112:115], a[80:83], v[20:23]\n"
    "v_cvt_pk_bf16_f32 v33, v6, v7\n"
    "v_cvt_pk_bf16_f32 v40, v8, v9\n"
    "v_mfma_f32_16x16x32_bf16 v[20:23], a[116:119], a[84:87], v[20:23]\n"
    "v_cvt_pk_bf16_f32 v41, v10, v11\n"
    "v_mfma_f32_16x16x32_bf16 v[20:23], a[120:123], a[88:91], v[20:23]\n"
    "v_mfma_f32_16x16x32_bf16 v[20:23], a[124:127], a[92:95], v[20:23]\n"
    "s_waitcnt vmcnt(14) lgkmcnt(0)\n"
    "s_barrier\n"
    "v_mfma_f32_16x16x32_bf16 a[168:171], a[160:163], v[56:59], a[168:171]\n"
    "v_fma_f32 v12, v12, s96, v91\n"
    "s_add_u32 m0, s91, 16384\n"
    "v_mfma_f32_16x16x32_bf16 a[168:171], a[160:163], v[60:63], a[168:171]\n"
    "buffer_load_dwordx4 v80, s[20:23], s61 offen lds\n"
    "v_mfma_f32_16x16x32_bf16 v[152:155], a[128:131], v[48:51], v[152:155]\n"
    "ds_read_b128 a[96:99], v72 offset:32768\n"
    "v_fma_f32 v13, v13, s96, v91\n"
    "v_mfma_f32_16x16x32_bf16 v[152:155], a[132:135], v[52:55], v[152:155]\n"
    "ds_read_b128 a[100:103], v73 offset:32768\n"
    "v_fma_f32 v14, v14, s96, v91\n"
    "v_mfma_f32_16x16x32_bf16 v[156:159], a[128:131], v[56:59], v[156:159]\n"
    "ds_read_b128 a[104:107], v74 offset:32768\n"
    "v_fma_f32 v15, v15, s96, v91\n"
    "v_mfma_f32_16x16x32_bf16 v[156:159], a[132:135], v[60:63], v[156:159]\n"
    "buffer_load_dwordx4 v81, s[20:23], s61 offen offset:1024 nt lds\n"
    "v_mfma_f32_16x16x32_bf16 v[160:163], a[128:131], v[64:67], v[160:163]\n"
    "ds_read_b128 a[108:111], v75 offset:32768\n"
    "v_fma_f32 v16, v16, s96, v92\n"
    "v_mfma_f32_16x16x32_bf16 v[160:163], a[132:135], v[68:71], v[160:163]\n"
    "ds_read_b128  a[228:231], v90 offset:19456\n"
    "v_fma_f32 v17, v17, s96, v92\n"
    "v_mfma_f32_16x16x32_bf16 v[164:167], a[136:139], v[48:51], v[164:167]\n"
    "ds_read_b128 a[112:115], v72 offset:33024\n"
    "v_fma_f32 v18, v18, s96, v92\n"
    "v_mfma_f32_16x16x32_bf16 v[164:167], a[140:143], v[52:55], v[164:167]\n"
    "buffer_load_dwordx4 v82, s[24:27], s85 offen offset:2048 nt lds\n"
    "v_mfma_f32_16x16x32_bf16 v[168:171], a[136:139], v[56:59], v[168:171]\n"
    "ds_read_b128 a[116:119], v73 offset:33024\n"
    "v_fma_f32 v19, v19, s96, v92\n"
    "v_mfma_f32_16x16x32_bf16 v[168:171], a[140:143], v[60:63], v[168:171]\n"
    "ds_read_b128 a[120:123], v74 offset:33024\n"
    "v_fma_f32 v20, v20, s96, v93\n"
    "v_mfma_f32_16x16x32_bf16 v[172:175], a[136:139], v[64:67], v[172:175]\n"
    "v_fma_f32 v21, v21, s96, v93\n"
    "v_fma_f32 v22, v22, s96, v93\n"
    "v_mfma_f32_16x16x32_bf16 v[172:175], a[140:143], v[68:71], v[172:175]\n"
    "buffer_load_dwordx4 v83, s[24:27], s85 offen offset:3072 nt lds\n"
    "v_mfma_f32_16x16x32_bf16 v[176:179], a[144:147], v[48:51], v[176:179]\n"
    "ds_read_b128 a[124:127], v75 offset:33024\n"
    "v_fma_f32 v23, v23, s96, v93\n"
    "v_mfma_f32_16x16x32_bf16 v[176:179], a[148:151], v[52:55], v[176:179]\n"
    "v_max3_f32 v97, v12, v13, v14\n"
    "v_max3_f32 v97, v97, v15, v16\n"
    "v_mfma_f32_16x16x32_bf16 v[180:183], a[144:147], v[56:59], v[180:183]\n"
    "v_max3_f32 v97, v97, v17, v18\n"
    "v_max3_f32 v97, v97, v19, v20\n"
    "v_mfma_f32_16x16x32_bf16 v[180:183], a[148:151], v[60:63], v[180:183]\n"
    "s_waitcnt lgkmcnt(4)\n"
    "buffer_store_dwordx4 a[228:231], v89, s[36:39], s89 offen offset:2304 nt\n"
    "v_mfma_f32_16x16x32_bf16 v[184:187], a[144:147], v[64:67], v[184:187]\n"
    "v_max3_f32 v97, v97, v21, v22\n"
    "v_max_f32 v97, v97, v23\n"
    "v_mfma_f32_16x16x32_bf16 v[184:187], a[148:151], v[68:71], v[184:187]\n"
    "v_cmp_lt_f32 vcc, s97, v97\n"
    "s_add_u32 m0, s35, 16384\n"
    "v_mfma_f32_16x16x32_bf16 a[188:191], a[152:155], v[48:51], a[188:191]\n"
    "v_mfma_f32_16x16x32_bf16 a[188:191], a[156:159], v[52:55], a[188:191]\n"
    "buffer_load_dwordx4 v87, s[28:31], s42 offen offset:3072 nt lds\n"
    "v_mfma_f32_16x16x32_bf16 a[192:195], a[152:155], v[56:59], a[192:195]\n"
    "ds_read_b64_tr_b16 a[128:129], v76 offset:32768\n"
    "s_cbranch_vccnz STUBb17c0_%=\n"
    "RETb17c0_%=:\n"
    "v_mfma_f32_16x16x32_bf16 a[192:195], a[156:159], v[60:63], a[192:195]\n"
    "ds_read_b64_tr_b16 a[130:131], v76 offset:36864\n"
    "ds_read_b64_tr_b16 a[132:133], v76 offset:40960\n"
    "v_mfma_f32_16x16x32_bf16 a[196:199], a[152:155], v[64:67], a[196:199]\n"
    "v_exp_f32 v12, v12\n"
    "v_mfma_f32_16x16x32_bf16 a[196:199], a[156:159], v[68:71], a[196:199]\n"
    "ds_read_b64_tr_b16 a[134:135], v76 offset:45056\n"
    "s_waitcnt lgkmcnt(4)\n"
    "v_mfma_f32_16x16x32_bf16 v[0:3], a[96:99], a[0:3], 0\n"
    "v_exp_f32 v13, v13\n"
    "v_mfma_f32_16x16x32_bf16 v[0:3], a[100:103], a[4:7], v[0:3]\n"
    "ds_read_b64_tr_b16 a[136:137], v77 offset:32768\n"
    "v_cvt_pk_bf16_f32 v26, v12, v13\n"
    "v_mfma_f32_16x16x32_bf16 v[0:3], a[104:107], a[8:11], v[0:3]\n"
    "ds_read_b64_tr_b16 a[138:139], v77 offset:36864\n"
    "ds_read_b64_tr_b16 a[140:141], v77 offset:40960\n"
    "v_mfma_f32_16x16x32_bf16 v[0:3], a[108:111], a[12:15], v[0:3]\n"
    "v_exp_f32 v14, v14\n"
    "v_mfma_f32_16x16x32_bf16 v[0:3], a[112:115], a[16:19], v[0:3]\n"
    "ds_read_b64_tr_b16 a[142:143], v77 offset:45056\n"
    "v_mfma_f32_16x16x32_bf16 v[0:3], a[116:119], a[20:23], v[0:3]\n"
    "v_exp_f32 v15, v15\n"
    "v_mfma_f32_16x16x32_bf16 v[0:3], a[120:123], a[24:27], v[0:3]\n"
    "ds_read_b64_tr_b16 a[144:145], v78 offset:32768\n"
    "v_cvt_pk_bf16_f32 v27, v14, v15\n"
    "v_mfma_f32_16x16x32_bf16 v[0:3], a[124:127], a[28:31], v[0:3]\n"
    "ds_read_b64_tr_b16 a[146:147], v78 offset:36864\n"
    "ds_read_b64_tr_b16 a[148:149], v78 offset:40960\n"
    "v_mfma_f32_16x16x32_bf16 v[4:7], a[96:99], a[32:35], 0\n"
    "v_exp_f32 v16, v16\n"
    "v_mfma_f32_16x16x32_bf16 v[4:7], a[100:103], a[36:39], v[4:7]\n"
    "ds_read_b64_tr_b16 a[150:151], v78 offset:45056\n"
    "v_mfma_f32_16x16x32_bf16 v[4:7], a[104:107], a[40:43], v[4:7]\n"
    "v_exp_f32 v17, v17\n"
    "v_mfma_f32_16x16x32_bf16 v[4:7], a[108:111], a[44:47], v[4:7]\n"
    "ds_read_b64_tr_b16 a[152:153], v79 offset:32768\n"
    "v_cvt_pk_bf16_f32 v34, v16, v17\n"
    "v_mfma_f32_16x16x32_bf16 v[4:7], a[112:115], a[48:51], v[4:7]\n"
    "ds_read_b64_tr_b16 a[154:155], v79 offset:36864\n"
    "ds_read_b64_tr_b16 a[156:157], v79 offset:40960\n"
    "v_mfma_f32_16x16x32_bf16 v[4:7], a[116:119], a[52:55], v[4:7]\n"
    "v_exp_f32 v18, v18\n"
    "v_mfma_f32_16x16x32_bf16 v[4:7], a[120:123], a[56:59], v[4:7]\n"
    "ds_read_b64_tr_b16 a[158:159], v79 offset:45056\n"
    "v_mfma_f32_16x16x32_bf16 v[4:7], a[124:127], a[60:63], v[4:7]\n"
    "v_exp_f32 v19, v19\n"
    "v_mfma_f32_16x16x32_bf16 v[8:11], a[96:99], a[64:67], 0\n"
    "v_exp_f32 v20, v20\n"
    "v_mfma_f32_16x16x32_bf16 v[8:11], a[100:103], a[68:71], v[8:11]\n"
    "v_exp_f32 v21, v21\n"
    "v_mfma_f32_16x16x32_bf16 v[8:11], a[104:107], a[72:75], v[8:11]\n"
    "v_exp_f32 v22, v22\n"
    "v_mfma_f32_16x16x32_bf16 v[8:11], a[108:111], a[76:79], v[8:11]\n"
    "v_exp_f32 v23, v23\n"
    "v_mfma_f32_16x16x32_bf16 v[8:11], a[112:115], a[80:83], v[8:11]\n"
    "v_cvt_pk_bf16_f32 v35, v18, v19\n"
    "v_cvt_pk_bf16_f32 v42, v20, v21\n"
    "v_mfma_f32_16x16x32_bf16 v[8:11], a[116:119], a[84:87], v[8:11]\n"
    "v_cvt_pk_bf16_f32 v43, v22, v23\n"
    "v_mfma_f32_16x16x32_bf16 v[8:11], a[120:123], a[88:91], v[8:11]\n"
    "v_mfma_f32_16x16x32_bf16 v[8:11], a[124:127], a[92:95], v[8:11]\n"
    "s_waitcnt vmcnt(14) lgkmcnt(0)\n"
    "s_barrier\n"
    "v_mfma_f32_16x16x32_bf16 a[172:175], a[160:163], v[64:67], a[172:175]\n"
    "v_fma_f32 v0, v0, s96, v91\n"
    "s_add_u32 m0, s91, 32768\n"
    "v_mfma_f32_16x16x32_bf16 a[172:175], a[160:163], v[68:71], a[172:175]\n"
    "buffer_load_dwordx4 v80, s[20:23], s62 offen lds\n"
    "v_mfma_f32_16x16x32_bf16 v[188:191], a[128:131], v[48:51], v[188:191]\n"
    "ds_read_b128 a[96:99], v72 offset:49152\n"
    "v_fma_f32 v1, v1, s96, v91\n"
    "v_mfma_f32_16x16x32_bf16 v[188:191], a[132:135], v[52:55], v[188:191]\n"
    "ds_read_b128 a[100:103], v73 offset:49152\n"
    "v_fma_f32 v2, v2, s96, v91\n"
    "v_mfma_f32_16x16x32_bf16 v[192:195], a[128:131], v[56:59], v[192:195]\n"
    "ds_read_b128 a[104:107], v74 offset:49152\n"
    "v_fma_f32 v3, v3, s96, v91\n"
    "v_mfma_f32_16x16x32_bf16 v[192:195], a[132:135], v[60:63], v[192:195]\n"
    "buffer_load_dwordx4 v81, s[20:23], s62 offen offset:1024 nt lds\n"
    "v_mfma_f32_16x16x32_bf16 v[196:199], a[128:131], v[64:67], v[196:199]\n"
    "ds_read_b128 a[108:111], v75 offset:49152\n"
    "v_fma_f32 v4, v4, s96, v92\n"
    "v_mfma_f32_16x16x32_bf16 v[196:199], a[132:135], v[68:71], v[196:199]\n"
    "ds_read_b128  a[228:231], v88 offset:20480\n"
    "v_fma_f32 v5, v5, s96, v92\n"
    "v_mfma_f32_16x16x32_bf16 v[200:203], a[136:139], v[48:51], v[200:203]\n"
    "ds_read_b128 a[112:115], v72 offset:49408\n"
    "v_fma_f32 v6, v6, s96, v92\n"
    "v_mfma_f32_16x16x32_bf16 v[200:203], a[140:143], v[52:55], v[200:203]\n"
    "buffer_load_dwordx4 v82, s[24:27], s86 offen offset:2048 nt lds\n"
    "v_mfma_f32_16x16x32_bf16 v[204:207], a[136:139], v[56:59], v[204:207]\n"
    "ds_read_b128 a[116:119], v73 offset:49408\n"
    "v_fma_f32 v7, v7, s96, v92\n"
    "v_mfma_f32_16x16x32_bf16 v[204:207], a[140:143], v[60:63], v[204:207]\n"
    "ds_read_b128 a[120:123], v74 offset:49408\n"
    "v_fma_f32 v8, v8, s96, v93\n"
    "v_mfma_f32_16x16x32_bf16 v[208:211], a[136:139], v[64:67], v[208:211]\n"
    "v_fma_f32 v9, v9, s96, v93\n"
    "v_fma_f32 v10, v10, s96, v93\n"
    "v_mfma_f32_16x16x32_bf16 v[208:211], a[140:143], v[68:71], v[208:211]\n"
    "buffer_load_dwordx4 v83, s[24:27], s86 offen offset:3072 nt lds\n"
    "v_mfma_f32_16x16x32_bf16 v[212:215], a[144:147], v[48:51], v[212:215]\n"
    "ds_read_b128 a[124:127], v75 offset:49408\n"
    "v_fma_f32 v11, v11, s96, v93\n"
    "v_mfma_f32_16x16x32_bf16 v[212:215], a[148:151], v[52:55], v[212:215]\n"
    "v_max3_f32 v97, v0, v1, v2\n"
    "v_max3_f32 v97, v97, v3, v4\n"
    "v_mfma_f32_16x16x32_bf16 v[216:219], a[144:147], v[56:59], v[216:219]\n"
    "v_max3_f32 v97, v97, v5, v6\n"
    "v_max3_f32 v97, v97, v7, v8\n"
    "v_mfma_f32_16x16x32_bf16 v[216:219], a[148:151], v[60:63], v[216:219]\n"
    "s_waitcnt lgkmcnt(4)\n"
    "buffer_store_dwordx4 a[228:231], v89, s[36:39], s42 offen offset:256 nt\n"
    "v_mfma_f32_16x16x32_bf16 v[220:223], a[144:147], v[64:67], v[220:223]\n"
    "v_max3_f32 v97, v97, v9, v10\n"
    "v_max_f32 v97, v97, v11\n"
    "v_mfma_f32_16x16x32_bf16 v[220:223], a[148:151], v[68:71], v[220:223]\n"
    "v_cmp_lt_f32 vcc, s97, v97\n"
    "s_add_u32 m0, s35, 20480\n"
    "v_mfma_f32_16x16x32_bf16 a[200:203], a[152:155], v[48:51], a[200:203]\n"
    "v_mfma_f32_16x16x32_bf16 a[200:203], a[156:159], v[52:55], a[200:203]\n"
    "buffer_load_dwordx4 v84, s[28:31], s90 offen nt lds\n"
    "v_mfma_f32_16x16x32_bf16 a[204:207], a[152:155], v[56:59], a[204:207]\n"
    "ds_read_b64_tr_b16 a[128:129], v76 offset:49152\n"
    "s_cbranch_vccnz STUBb18c0_%=\n"
    "RETb18c0_%=:\n"
    "v_mfma_f32_16x16x32_bf16 a[204:207], a[156:159], v[60:63], a[204:207]\n"
    "ds_read_b64_tr_b16 a[130:131], v76 offset:53248\n"
    "ds_read_b64_tr_b16 a[132:133], v76 offset:57344\n"
    "v_mfma_f32_16x16x32_bf16 a[208:211], a[152:155], v[64:67], a[208:211]\n"
    "v_exp_f32 v0, v0\n"
    "v_mfma_f32_16x16x32_bf16 a[208:211], a[156:159], v[68:71], a[208:211]\n"
    "ds_read_b64_tr_b16 a[134:135], v76 offset:61440\n"
    "s_waitcnt lgkmcnt(4)\n"
    "v_mfma_f32_16x16x32_bf16 v[12:15], a[96:99], a[0:3], 0\n"
    "v_exp_f32 v1, v1\n"
    "v_mfma_f32_16x16x32_bf16 v[12:15], a[100:103], a[4:7], v[12:15]\n"
    "ds_read_b64_tr_b16 a[136:137], v77 offset:49152\n"
    "v_cvt_pk_bf16_f32 v28, v0, v1\n"
    "v_mfma_f32_16x16x32_bf16 v[12:15], a[104:107], a[8:11], v[12:15]\n"
    "ds_read_b64_tr_b16 a[138:139], v77 offset:53248\n"
    "ds_read_b64_tr_b16 a[140:141], v77 offset:57344\n"
    "v_mfma_f32_16x16x32_bf16 v[12:15], a[108:111], a[12:15], v[12:15]\n"
    "v_exp_f32 v2, v2\n"
    "v_mfma_f32_16x16x32_bf16 v[12:15], a[112:115], a[16:19], v[12:15]\n"
    "ds_read_b64_tr_b16 a[142:143], v77 offset:61440\n"
    "v_mfma_f32_16x16x32_bf16 v[12:15], a[116:119], a[20:23], v[12:15]\n"
    "v_exp_f32 v3, v3\n"
    "v_mfma_f32_16x16x32_bf16 v[12:15], a[120:123], a[24:27], v[12:15]\n"
    "ds_read_b64_tr_b16 a[144:145], v78 offset:49152\n"
    "v_cvt_pk_bf16_f32 v29, v2, v3\n"
    "v_mfma_f32_16x16x32_bf16 v[12:15], a[124:127], a[28:31], v[12:15]\n"
    "ds_read_b64_tr_b16 a[146:147], v78 offset:53248\n"
    "ds_read_b64_tr_b16 a[148:149], v78 offset:57344\n"
    "v_mfma_f32_16x16x32_bf16 v[16:19], a[96:99], a[32:35], 0\n"
    "v_exp_f32 v4, v4\n"
    "v_mfma_f32_16x16x32_bf16 v[16:19], a[100:103], a[36:39], v[16:19]\n"
    "ds_read_b64_tr_b16 a[150:151], v78 offset:61440\n"
    "v_mfma_f32_16x16x32_bf16 v[16:19], a[104:107], a[40:43], v[16:19]\n"
    "v_exp_f32 v5, v5\n"
    "v_mfma_f32_16x16x32_bf16 v[16:19], a[108:111], a[44:47], v[16:19]\n"
    "ds_read_b64_tr_b16 a[152:153], v79 offset:49152\n"
    "v_cvt_pk_bf16_f32 v36, v4, v5\n"
    "v_mfma_f32_16x16x32_bf16 v[16:19], a[112:115], a[48:51], v[16:19]\n"
    "ds_read_b64_tr_b16 a[154:155], v79 offset:53248\n"
    "ds_read_b64_tr_b16 a[156:157], v79 offset:57344\n"
    "v_mfma_f32_16x16x32_bf16 v[16:19], a[116:119], a[52:55], v[16:19]\n"
    "v_exp_f32 v6, v6\n"
    "v_mfma_f32_16x16x32_bf16 v[16:19], a[120:123], a[56:59], v[16:19]\n"
    "ds_read_b64_tr_b16 a[158:159], v79 offset:61440\n"
    "v_mfma_f32_16x16x32_bf16 v[16:19], a[124:127], a[60:63], v[16:19]\n"
    "v_exp_f32 v7, v7\n"
    "v_mfma_f32_16x16x32_bf16 v[20:23], a[96:99], a[64:67], 0\n"
    "v_exp_f32 v8, v8\n"
    "v_mfma_f32_16x16x32_bf16 v[20:23], a[100:103], a[68:71], v[20:23]\n"
    "v_exp_f32 v9, v9\n"
    "v_mfma_f32_16x16x32_bf16 v[20:23], a[104:107], a[72:75], v[20:23]\n"
    "v_exp_f32 v10, v10\n"
    "v_mfma_f32_16x16x32_bf16 v[20:23], a[108:111], a[76:79], v[20:23]\n"
    "v_exp_f32 v11, v11\n"
    "v_mfma_f32_16x16x32_bf16 v[20:23], a[112:115], a[80:83], v[20:23]\n"
    "v_cvt_pk_bf16_f32 v37, v6, v7\n"
    "v_cvt_pk_bf16_f32 v44, v8, v9\n"
    "v_mfma_f32_16x16x32_bf16 v[20:23], a[116:119], a[84:87], v[20:23]\n"
    "v_cvt_pk_bf16_f32 v45, v10, v11\n"
    "v_mfma_f32_16x16x32_bf16 v[20:23], a[120:123], a[88:91], v[20:23]\n"
    "v_mfma_f32_16x16x32_bf16 v[20:23], a[124:127], a[92:95], v[20:23]\n"
    "s_waitcnt vmcnt(14) lgkmcnt(0)\n"
    "s_barrier\n"
    "v_mfma_f32_16x16x32_bf16 v[224:227], a[128:131], v[48:51], v[224:227]\n"
    "ds_read_b128 a[96:99], v72 offset:0\n"
    "s_add_u32 m0, s91, 49152\n"
    "v_mfma_f32_16x16x32_bf16 v[224:227], a[132:135], v[52:55], v[224:227]\n"
    "buffer_load_dwordx4 v80, s[20:23], s63 offen lds\n"
    "v_mfma_f32_16x16x32_bf16 v[228:231], a[128:131], v[56:59], v[228:231]\n"
    "ds_read_b128 a[100:103], v73 offset:0\n"
    "v_fma_f32 v12, v12, s96, v91\n"
    "v_mfma_f32_16x16x32_bf16 v[228:231], a[132:135], v[60:63], v[228:231]\n"
    "ds_read_b128 a[104:107], v74 offset:0\n"
    "v_fma_f32 v13, v13, s96, v91\n"
    "v_mfma_f32_16x16x32_bf16 v[232:235], a[128:131], v[64:67], v[232:235]\n"
    "v_fma_f32 v14, v14, s96, v91\n"
    "v_fma_f32 v15, v15, s96, v91\n"
    "v_mfma_f32_16x16x32_bf16 v[232:235], a[132:135], v[68:71], v[232:235]\n"
    "buffer_load_dwordx4 v81, s[20:23], s63 offen offset:1024 nt lds\n"
    "v_mfma_f32_16x16x32_bf16 v[236:239], a[136:139], v[48:51], v[236:239]\n"
    "ds_read_b128 a[108:111], v75 offset:0\n"
    "v_fma_f32 v16, v16, s96, v92\n"
    "v_mfma_f32_16x16x32_bf16 v[236:239], a[140:143], v[52:55], v[236:239]\n"
    "ds_read_b128  a[228:231], v90 offset:21504\n"
    "v_fma_f32 v17, v17, s96, v92\n"
    "v_mfma_f32_16x16x32_bf16 v[240:243], a[136:139], v[56:59], v[240:243]\n"
    "ds_read_b128 a[112:115], v72 offset:256\n"
    "v_fma_f32 v18, v18, s96, v92\n"
    "v_mfma_f32_16x16x32_bf16 v[240:243], a[140:143], v[60:63], v[240:243]\n"
    "buffer_load_dwordx4 v82, s[24:27], s87 offen offset:2048 nt lds\n"
    "v_mfma_f32_16x16x32_bf16 v[244:247], a[136:139], v[64:67], v[244:247]\n"
    "ds_read_b128 a[116:119], v73 offset:256\n"
    "v_fma_f32 v19, v19, s96, v92\n"
    "v_mfma_f32_16x16x32_bf16 v[244:247], a[140:143], v[68:71], v[244:247]\n"
    "ds_read_b128 a[120:123], v74 offset:256\n"
    "v_fma_f32 v20, v20, s96, v93\n"
    "v_mfma_f32_16x16x32_bf16 v[248:251], a[144:147], v[48:51], v[248:251]\n"
    "ds_read_b128 a[124:127], v75 offset:256\n"
    "v_fma_f32 v21, v21, s96, v93\n"
    "v_mfma_f32_16x16x32_bf16 v[248:251], a[148:151], v[52:55], v[248:251]\n"
    "buffer_load_dwordx4 v83, s[24:27], s87 offen offset:3072 nt lds\n"
    "v_mfma_f32_16x16x32_bf16 v[252:255], a[144:147], v[56:59], v[252:255]\n"
    "v_fma_f32 v22, v22, s96, v93\n"
    "v_fma_f32 v23, v23, s96, v93\n"
    "v_mfma_f32_16x16x32_bf16 v[252:255], a[148:151], v[60:63], v[252:255]\n"
    "v_max3_f32 v97, v12, v13, v14\n"
    "v_max3_f32 v97, v97, v15, v16\n"
    "v_mfma_f32_16x16x32_bf16 a[224:227], a[144:147], v[64:67], a[224:227]\n"
    "v_max3_f32 v97, v97, v17, v18\n"
    "v_max3_f32 v97, v97, v19, v20\n"
    "v_mfma_f32_16x16x32_bf16 a[224:227], a[148:151], v[68:71], a[224:227]\n"
    "s_waitcnt lgkmcnt(4)\n"
    "buffer_store_dwordx4 a[228:231], v89, s[36:39], s42 offen offset:2304 nt\n"
    "v_mfma_f32_16x16x32_bf16 a[212:215], a[152:155], v[48:51], a[212:215]\n"
    "v_max3_f32 v97, v97, v21, v22\n"
    "v_max_f32 v97, v97, v23\n"
    "v_mfma_f32_16x16x32_bf16 a[212:215], a[156:159], v[52:55], a[212:215]\n"
    "v_cmp_lt_f32 vcc, s97, v97\n"
    "s_add_u32 m0, s35, 20480\n"
    "v_mfma_f32_16x16x32_bf16 a[216:219], a[152:155], v[56:59], a[216:219]\n"
    "ds_read_b64_tr_b16 a[128:129], v76 offset:0\n"
    "ds_read_b64_tr_b16 a[130:131], v76 offset:4096\n"
    "v_mfma_f32_16x16x32_bf16 a[216:219], a[156:159], v[60:63], a[216:219]\n"
    "buffer_load_dwordx4 v85, s[28:31], s90 offen offset:1024 nt lds\n"
    "v_mfma_f32_16x16x32_bf16 a[220:223], a[152:155], v[64:67], a[220:223]\n"
    "ds_read_b64_tr_b16 a[132:133], v76 offset:8192\n"
    "s_cbranch_vccnz STUBb19c0_%=\n"
    "RETb19c0_%=:\n"
    "v_mfma_f32_16x16x32_bf16 a[220:223], a[156:159], v[68:71], a[220:223]\n"
    "ds_read_b64_tr_b16 a[134:135], v76 offset:12288\n"
    "s_waitcnt lgkmcnt(4)\n"
    "v_mfma_f32_16x16x32_bf16 v[0:3], a[96:99], a[0:3], 0\n"
    "v_exp_f32 v12, v12\n"
    "v_mfma_f32_16x16x32_bf16 v[0:3], a[100:103], a[4:7], v[0:3]\n"
    "ds_read_b64_tr_b16 a[136:137], v77 offset:0\n"
    "ds_read_b64_tr_b16 a[138:139], v77 offset:4096\n"
    "v_mfma_f32_16x16x32_bf16 v[0:3], a[104:107], a[8:11], v[0:3]\n"
    "v_exp_f32 v13, v13\n"
    "v_mfma_f32_16x16x32_bf16 v[0:3], a[108:111], a[12:15], v[0:3]\n"
    "ds_read_b64_tr_b16 a[140:141], v77 offset:8192\n"
    "v_cvt_pk_bf16_f32 v30, v12, v13\n"
    "v_mfma_f32_16x16x32_bf16 v[0:3], a[112:115], a[16:19], v[0:3]\n"
    "ds_read_b64_tr_b16 a[142:143], v77 offset:12288\n"
    "v_mfma_f32_16x16x32_bf16 v[0:3], a[116:119], a[20:23], v[0:3]\n"
    "v_exp_f32 v14, v14\n"
    "v_mfma_f32_16x16x32_bf16 v[0:3], a[120:123], a[24:27], v[0:3]\n"
    "ds_read_b64_tr_b16 a[144:145], v78 offset:0\n"
    "ds_read_b64_tr_b16 a[146:147], v78 offset:4096\n"
    "v_mfma_f32_16x16x32_bf16 v[0:3], a[124:127], a[28:31], v[0:3]\n"
    "v_exp_f32 v15, v15\n"
    "v_mfma_f32_16x16x32_bf16 v[4:7], a[96:99], a[32:35], 0\n"
    "ds_read_b64_tr_b16 a[148:149], v78 offset:8192\n"
    "v_cvt_pk_bf16_f32 v31, v14, v15\n"
    "v_mfma_f32_16x16x32_bf16 v[4:7], a[100:103], a[36:39], v[4:7]\n"
    "ds_read_b64_tr_b16 a[150:151], v78 offset:12288\n"
    "v_mfma_f32_16x16x32_bf16 v[4:7], a[104:107], a[40:43], v[4:7]\n"
    "v_exp_f32 v16, v16\n"
    "v_mfma_f32_16x16x32_bf16 v[4:7], a[108:111], a[44:47], v[4:7]\n"
    "ds_read_b64_tr_b16 a[152:153], v79 offset:0\n"
    "ds_read_b64_tr_b16 a[154:155], v79 offset:4096\n"
    "v_mfma_f32_16x16x32_bf16 v[4:7], a[112:115], a[48:51], v[4:7]\n"
    "v_exp_f32 v17, v17\n"
    "v_mfma_f32_16x16x32_bf16 v[4:7], a[116:119], a[52:55], v[4:7]\n"
    "ds_read_b64_tr_b16 a[156:157], v79 offset:8192\n"
    "v_cvt_pk_bf16_f32 v38, v16, v17\n"
    "v_mfma_f32_16x16x32_bf16 v[4:7], a[120:123], a[56:59], v[4:7]\n"
    "ds_read_b64_tr_b16 a[158:159], v79 offset:12288\n"
    "v_mfma_f32_16x16x32_bf16 v[4:7], a[124:127], a[60:63], v[4:7]\n"
    "v_exp_f32 v18, v18\n"
    "v_mfma_f32_16x16x32_bf16 v[8:11], a[96:99], a[64:67], 0\n"
    "v_exp_f32 v19, v19\n"
    "v_mfma_f32_16x16x32_bf16 v[8:11], a[100:103], a[68:71], v[8:11]\n"
    "v_exp_f32 v20, v20\n"
    "v_mfma_f32_16x16x32_bf16 v[8:11], a[104:107], a[72:75], v[8:11]\n"
    "v_exp_f32 v21, v21\n"
    "v_mfma_f32_16x16x32_bf16 v[8:11], a[108:111], a[76:79], v[8:11]\n"
    "v_exp_f32 v22, v22\n"
    "v_mfma_f32_16x16x32_bf16 v[8:11], a[112:115], a[80:83], v[8:11]\n"
    "v_exp_f32 v23, v23\n"
    "v_mfma_f32_16x16x32_bf16 v[8:11], a[116:119], a[84:87], v[8:11]\n"
    "v_cvt_pk_bf16_f32 v39, v18, v19\n"
    "v_cvt_pk_bf16_f32 v46, v20, v21\n"
    "v_mfma_f32_16x16x32_bf16 v[8:11], a[120:123], a[88:91], v[8:11]\n"
    "v_cvt_pk_bf16_f32 v47, v22, v23\n"
    "v_mfma_f32_16x16x32_bf16 v[8:11], a[124:127], a[92:95], v[8:11]\n"
    "s_add_u32 s20, s20, 25165824\n"
    "s_addc_u32 s21, s21, 0\n"
    "s_cmp_lt_u32 s98, 3\n"
    "s_cselect_b32 s22, 196608, 0\n"
    "s_waitcnt vmcnt(14) lgkmcnt(0)\n"
    "s_barrier\n"
    "v_mfma_f32_16x16x32_bf16 a[164:167], a[160:163], v[24:27], a[164:167]\n"
    "v_fma_f32 v0, v0, s96, v91\n"
    "s_add_u32 m0, s91, 0\n"
    "v_mfma_f32_16x16x32_bf16 a[164:167], a[160:163], v[28:31], a[164:167]\n"
    "buffer_load_dwordx4 v80, s[20:23], s40 offen lds\n"
    "v_mfma_f32_16x16x32_bf16 v[116:119], a[128:131], v[24:27], v[116:119]\n"
    "ds_read_b128 a[96:99], v72 offset:16384\n"
    "v_fma_f32 v1, v1, s96, v91\n"
    "v_mfma_f32_16x16x32_bf16 v[116:119], a[132:135], v[28:31], v[116:119]\n"
    "ds_read_b128 a[100:103], v73 offset:16384\n"
    "v_fma_f32 v2, v2, s96, v91\n"
    "v_mfma_f32_16x16x32_bf16 v[120:123], a[128:131], v[32:35], v[120:123]\n"
    "ds_read_b128 a[104:107], v74 offset:16384\n"
    "v_fma_f32 v3, v3, s96, v91\n"
    "v_mfma_f32_16x16x32_bf16 v[120:123], a[132:135], v[36:39], v[120:123]\n"
    "buffer_load_dwordx4 v81, s[20:23], s40 offen offset:1024 nt lds\n"
    "v_mfma_f32_16x16x32_bf16 v[124:127], a[128:131], v[40:43], v[124:127]\n"
    "ds_read_b128 a[108:111], v75 offset:16384\n"
    "v_fma_f32 v4, v4, s96, v92\n"
    "v_mfma_f32_16x16x32_bf16 v[124:127], a[132:135], v[44:47], v[124:127]\n"
    "ds_read_b128  a[228:231], v88 offset:22528\n"
    "v_fma_f32 v5, v5, s96, v92\n"
    "v_mfma_f32_16x16x32_bf16 v[128:131], a[136:139], v[24:27], v[128:131]\n"
    "ds_read_b128 a[112:115], v72 offset:16640\n"
    "v_fma_f32 v6, v6, s96, v92\n"
    "v_mfma_f32_16x16x32_bf16 v[128:131], a[140:143], v[28:31], v[128:131]\n"
    "buffer_load_dwordx4 v82, s[24:27], s64 offen offset:2048 nt lds\n"
    "v_mfma_f32_16x16x32_bf16 v[132:135], a[136:139], v[32:35], v[132:135]\n"
    "ds_read_b128 a[116:119], v73 offset:16640\n"
    "v_fma_f32 v7, v7, s96, v92\n"
    "v_mfma_f32_16x16x32_bf16 v[132:135], a[140:143], v[36:39], v[132:135]\n"
    "ds_read_b128 a[120:123], v74 offset:16640\n"
    "v_fma_f32 v8, v8, s96, v93\n"
    "v_mfma_f32_16x16x32_bf16 v[136:139], a[136:139], v[40:43], v[136:139]\n"
    "v_fma_f32 v9, v9, s96, v93\n"
    "v_fma_f32 v10, v10, s96, v93\n"
    "v_mfma_f32_16x16x32_bf16 v[136:139], a[140:143], v[44:47], v[136:139]\n"
    "buffer_load_dwordx4 v83, s[24:27], s64 offen offset:3072 nt lds\n"
    "v_mfma_f32_16x16x32_bf16 v[140:143], a[144:147], v[24:27], v[140:143]\n"
    "ds_read_b128 a[124:127], v75 offset:16640\n"
    "v_fma_f32 v11, v11, s96, v93\n"
    "v_mfma_f32_16x16x32_bf16 v[140:143], a[148:151], v[28:31], v[140:143]\n"
    "v_max3_f32 v97, v0, v1, v2\n"
    "v_max3_f32 v97, v97, v3, v4\n"
    "v_mfma_f32_16x16x32_bf16 v[144:147], a[144:147], v[32:35], v[144:147]\n"
    "v_max3_f32 v97, v97, v5, v6\n"
    "v_max3_f32 v97, v97, v7, v8\n"
    "v_mfma_f32_16x16x32_bf16 v[144:147], a[148:151], v[36:39], v[144:147]\n"
    "s_waitcnt lgkmcnt(4)\n"
    "buffer_store_dwordx4 a[228:231], v89, s[36:39], s90 offen offset:256 nt\n"
    "v_mfma_f32_16x16x32_bf16 v[148:151], a[144:147], v[40:43], v[148:151]\n"
    "v_max3_f32 v97, v97, v9, v10\n"
    "v_max_f32 v97, v97, v11\n"
    "v_mfma_f32_16x16x32_bf16 v[148:151], a[148:151], v[44:47], v[148:151]\n"
    "v_cmp_lt_f32 vcc, s97, v97\n"
    "s_add_u32 m0, s35, 20480\n"
    "v_mfma_f32_16x16x32_bf16 a[176:179], a[152:155], v[24:27], a[176:179]\n"
    "v_mfma_f32_16x16x32_bf16 a[176:179], a[156:159], v[28:31], a[176:179]\n"
    "buffer_load_dwordx4 v86, s[28:31], s90 offen offset:2048 nt lds\n"
    "v_mfma_f32_16x16x32_bf16 a[180:183], a[152:155], v[32:35], a[180:183]\n"
    "ds_read_b64_tr_b16 a[128:129], v76 offset:16384\n"
    "s_cbranch_vccnz STUBb20c0_%=\n"
    "RETb20c0_%=:\n"
    "v_mfma_f32_16x16x32_bf16 a[180:183], a[156:159], v[36:39], a[180:183]\n"
    "ds_read_b64_tr_b16 a[130:131], v76 offset:20480\n"
    "ds_read_b64_tr_b16 a[132:133], v76 offset:24576\n"
    "v_mfma_f32_16x16x32_bf16 a[184:187], a[152:155], v[40:43], a[184:187]\n"
    "v_exp_f32 v0, v0\n"
    "v_mfma_f32_16x16x32_bf16 a[184:187], a[156:159], v[44:47], a[184:187]\n"
    "ds_read_b64_tr_b16 a[134:135], v76 offset:28672\n"
    "s_waitcnt lgkmcnt(4)\n"
    "v_mfma_f32_16x16x32_bf16 v[12:15], a[96:99], a[0:3], 0\n"
    "v_exp_f32 v1, v1\n"
    "v_mfma_f32_16x16x32_bf16 v[12:15], a[100:103], a[4:7], v[12:15]\n"
    "ds_read_b64_tr_b16 a[136:137], v77 offset:16384\n"
    "v_cvt_pk_bf16_f32 v48, v0, v1\n"
    "v_mfma_f32_16x16x32_bf16 v[12:15], a[104:107], a[8:11], v[12:15]\n"
    "ds_read_b64_tr_b16 a[138:139], v77 offset:20480\n"
    "ds_read_b64_tr_b16 a[140:141], v77 offset:24576\n"
    "v_mfma_f32_16x16x32_bf16 v[12:15], a[108:111], a[12:15], v[12:15]\n"
    "v_exp_f32 v2, v2\n"
    "v_mfma_f32_16x16x32_bf16 v[12:15], a[112:115], a[16:19], v[12:15]\n"
    "ds_read_b64_tr_b16 a[142:143], v77 offset:28672\n"
    "v_mfma_f32_16x16x32_bf16 v[12:15], a[116:119], a[20:23], v[12:15]\n"
    "v_exp_f32 v3, v3\n"
    "v_mfma_f32_16x16x32_bf16 v[12:15], a[120:123], a[24:27], v[12:15]\n"
    "ds_read_b64_tr_b16 a[144:145], v78 offset:16384\n"
    "v_cvt_pk_bf16_f32 v49, v2, v3\n"
    "v_mfma_f32_16x16x32_bf16 v[12:15], a[124:127], a[28:31], v[12:15]\n"
    "ds_read_b64_tr_b16 a[146:147], v78 offset:20480\n"
    "ds_read_b64_tr_b16 a[148:149], v78 offset:24576\n"
    "v_mfma_f32_16x16x32_bf16 v[16:19], a[96:99], a[32:35], 0\n"
    "v_exp_f32 v4, v4\n"
    "v_mfma_f32_16x16x32_bf16 v[16:19], a[100:103], a[36:39], v[16:19]\n"
    "ds_read_b64_tr_b16 a[150:151], v78 offset:28672\n"
    "v_mfma_f32_16x16x32_bf16 v[16:19], a[104:107], a[40:43], v[16:19]\n"
    "v_exp_f32 v5, v5\n"
    "v_mfma_f32_16x16x32_bf16 v[16:19], a[108:111], a[44:47], v[16:19]\n"
    "ds_read_b64_tr_b16 a[152:153], v79 offset:16384\n"
    "v_cvt_pk_bf16_f32 v56, v4, v5\n"
    "v_mfma_f32_16x16x32_bf16 v[16:19], a[112:115], a[48:51], v[16:19]\n"
    "ds_read_b64_tr_b16 a[154:155], v79 offset:20480\n"
    "ds_read_b64_tr_b16 a[156:157], v79 offset:24576\n"
    "v_mfma_f32_16x16x32_bf16 v[16:19], a[116:119], a[52:55], v[16:19]\n"
    "v_exp_f32 v6, v6\n"
    "v_mfma_f32_16x16x32_bf16 v[16:19], a[120:123], a[56:59], v[16:19]\n"
    "ds_read_b64_tr_b16 a[158:159], v79 offset:28672\n"
    "v_mfma_f32_16x16x32_bf16 v[16:19], a[124:127], a[60:63], v[16:19]\n"
    "v_exp_f32 v7, v7\n"
    "v_mfma_f32_16x16x32_bf16 v[20:23], a[96:99], a[64:67], 0\n"
    "v_exp_f32 v8, v8\n"
    "v_mfma_f32_16x16x32_bf16 v[20:23], a[100:103], a[68:71], v[20:23]\n"
    "v_exp_f32 v9, v9\n"
    "v_mfma_f32_16x16x32_bf16 v[20:23], a[104:107], a[72:75], v[20:23]\n"
    "v_exp_f32 v10, v10\n"
    "v_mfma_f32_16x16x32_bf16 v[20:23], a[108:111], a[76:79], v[20:23]\n"
    "v_exp_f32 v11, v11\n"
    "v_mfma_f32_16x16x32_bf16 v[20:23], a[112:115], a[80:83], v[20:23]\n"
    "v_cvt_pk_bf16_f32 v57, v6, v7\n"
    "v_cvt_pk_bf16_f32 v64, v8, v9\n"
    "v_mfma_f32_16x16x32_bf16 v[20:23], a[116:119], a[84:87], v[20:23]\n"
    "v_cvt_pk_bf16_f32 v65, v10, v11\n"
    "v_mfma_f32_16x16x32_bf16 v[20:23], a[120:123], a[88:91], v[20:23]\n"
    "v_mfma_f32_16x16x32_bf16 v[20:23], a[124:127], a[92:95], v[20:23]\n"
    "s_waitcnt vmcnt(14) lgkmcnt(0)\n"
    "s_barrier\n"
    "v_mfma_f32_16x16x32_bf16 a[168:171], a[160:163], v[32:35], a[168:171]\n"
    "v_fma_f32 v12, v12, s96, v91\n"
    "s_add_u32 m0, s91, 16384\n"
    "v_mfma_f32_16x16x32_bf16 a[168:171], a[160:163], v[36:39], a[168:171]\n"
    "buffer_load_dwordx4 v80, s[20:23], s41 offen lds\n"
    "v_mfma_f32_16x16x32_bf16 v[152:155], a[128:131], v[24:27], v[152:155]\n"
    "ds_read_b128 a[96:99], v72 offset:32768\n"
    "v_fma_f32 v13, v13, s96, v91\n"
    "v_mfma_f32_16x16x32_bf16 v[152:155], a[132:135], v[28:31], v[152:155]\n"
    "ds_read_b128 a[100:103], v73 offset:32768\n"
    "v_fma_f32 v14, v14, s96, v91\n"
    "v_mfma_f32_16x16x32_bf16 v[156:159], a[128:131], v[32:35], v[156:159]\n"
    "ds_read_b128 a[104:107], v74 offset:32768\n"
    "v_fma_f32 v15, v15, s96, v91\n"
    "v_mfma_f32_16x16x32_bf16 v[156:159], a[132:135], v[36:39], v[156:159]\n"
    "buffer_load_dwordx4 v81, s[20:23], s41 offen offset:1024 nt lds\n"
    "v_mfma_f32_16x16x32_bf16 v[160:163], a[128:131], v[40:43], v[160:163]\n"
    "ds_read_b128 a[108:111], v75 offset:32768\n"
    "v_fma_f32 v16, v16, s96, v92\n"
    "v_mfma_f32_16x16x32_bf16 v[160:163], a[132:135], v[44:47], v[160:163]\n"
    "ds_read_b128  a[228:231], v90 offset:23552\n"
    "v_fma_f32 v17, v17, s96, v92\n"
    "v_mfma_f32_16x16x32_bf16 v[164:167], a[136:139], v[24:27], v[164:167]\n"
    "ds_read_b128 a[112:115], v72 offset:33024\n"
    "v_fma_f32 v18, v18, s96, v92\n"
    "v_mfma_f32_16x16x32_bf16 v[164:167], a[140:143], v[28:31], v[164:167]\n"
    "buffer_load_dwordx4 v82, s[24:27], s65 offen offset:2048 nt lds\n"
    "v_mfma_f32_16x16x32_bf16 v[168:171], a[136:139], v[32:35], v[168:171]\n"
    "ds_read_b128 a[116:119], v73 offset:33024\n"
    "v_fma_f32 v19, v19, s96, v92\n"
    "v_mfma_f32_16x16x32_bf16 v[168:171], a[140:143], v[36:39], v[168:171]\n"
    "ds_read_b128 a[120:123], v74 offset:33024\n"
    "v_fma_f32 v20, v20, s96, v93\n"
    "v_mfma_f32_16x16x32_bf16 v[172:175], a[136:139], v[40:43], v[172:175]\n"
    "v_fma_f32 v21, v21, s96, v93\n"
    "v_fma_f32 v22, v22, s96, v93\n"
    "v_mfma_f32_16x16x32_bf16 v[172:175], a[140:143], v[44:47], v[172:175]\n"
    "buffer_load_dwordx4 v83, s[24:27], s65 offen offset:3072 nt lds\n"
    "v_mfma_f32_16x16x32_bf16 v[176:179], a[144:147], v[24:27], v[176:179]\n"
    "ds_read_b128 a[124:127], v75 offset:33024\n"
    "v_fma_f32 v23, v23, s96, v93\n"
    "v_mfma_f32_16x16x32_bf16 v[176:179], a[148:151], v[28:31], v[176:179]\n"
    "v_max3_f32 v97, v12, v13, v14\n"
    "v_max3_f32 v97, v97, v15, v16\n"
    "v_mfma_f32_16x16x32_bf16 v[180:183], a[144:147], v[32:35], v[180:183]\n"
    "v_max3_f32 v97, v97, v17, v18\n"
    "v_max3_f32 v97, v97, v19, v20\n"
    "v_mfma_f32_16x16x32_bf16 v[180:183], a[148:151], v[36:39], v[180:183]\n"
    "s_waitcnt lgkmcnt(4)\n"
    "buffer_store_dwordx4 a[228:231], v89, s[36:39], s90 offen offset:2304 nt\n"
    "v_mfma_f32_16x16x32_bf16 v[184:187], a[144:147], v[40:43], v[184:187]\n"
    "v_max3_f32 v97, v97, v21, v22\n"
    "v_max_f32 v97, v97, v23\n"
    "v_mfma_f32_16x16x32_bf16 v[184:187], a[148:151], v[44:47], v[184:187]\n"
    "v_cmp_lt_f32 vcc, s97, v97\n"
    "s_add_u32 m0, s35, 20480\n"
    "v_mfma_f32_16x16x32_bf16 a[188:191], a[152:155], v[24:27], a[188:191]\n"
    "v_mfma_f32_16x16x32_bf16 a[188:191], a[156:159], v[28:31], a[188:191]\n"
    "buffer_load_dwordx4 v87, s[28:31], s90 offen offset:3072 nt lds\n"
    "v_mfma_f32_16x16x32_bf16 a[192:195], a[152:155], v[32:35], a[192:195]\n"
    "ds_read_b64_tr_b16 a[128:129], v76 offset:32768\n"
    "s_cbranch_vccnz STUBb21c0_%=\n"
    "RETb21c0_%=:\n"
    "v_mfma_f32_16x16x32_bf16 a[192:195], a[156:159], v[36:39], a[192:195]\n"
    "ds_read_b64_tr_b16 a[130:131], v76 offset:36864\n"
    "ds_read_b64_tr_b16 a[132:133], v76 offset:40960\n"
    "v_mfma_f32_16x16x32_bf16 a[196:199], a[152:155], v[40:43], a[196:199]\n"
    "v_exp_f32 v12, v12\n"
    "v_mfma_f32_16x16x32_bf16 a[196:199], a[156:159], v[44:47], a[196:199]\n"
    "ds_read_b64_tr_b16 a[134:135], v76 offset:45056\n"
    "s_waitcnt lgkmcnt(4)\n"
    "v_mfma_f32_16x16x32_bf16 v[0:3], a[96:99], a[0:3], 0\n"
    "v_exp_f32 v13, v13\n"
    "v_mfma_f32_16x16x32_bf16 v[0:3], a[100:103], a[4:7], v[0:3]\n"
    "ds_read_b64_tr_b16 a[136:137], v77 offset:32768\n"
    "v_cvt_pk_bf16_f32 v50, v12, v13\n"
    "v_mfma_f32_16x16x32_bf16 v[0:3], a[104:107], a[8:11], v[0:3]\n"
    "ds_read_b64_tr_b16 a[138:139], v77 offset:36864\n"
    "ds_read_b64_tr_b16 a[140:141], v77 offset:40960\n"
    "v_mfma_f32_16x16x32_bf16 v[0:3], a[108:111], a[12:15], v[0:3]\n"
    "v_exp_f32 v14, v14\n"
    "v_mfma_f32_16x16x32_bf16 v[0:3], a[112:115], a[16:19], v[0:3]\n"
    "ds_read_b64_tr_b16 a[142:143], v77 offset:45056\n"
    "v_mfma_f32_16x16x32_bf16 v[0:3], a[116:119], a[20:23], v[0:3]\n"
    "v_exp_f32 v15, v15\n"
    "v_mfma_f32_16x16x32_bf16 v[0:3], a[120:123], a[24:27], v[0:3]\n"
    "ds_read_b64_tr_b16 a[144:145], v78 offset:32768\n"
    "v_cvt_pk_bf16_f32 v51, v14, v15\n"
    "v_mfma_f32_16x16x32_bf16 v[0:3], a[124:127], a[28:31], v[0:3]\n"
    "ds_read_b64_tr_b16 a[146:147], v78 offset:36864\n"
    "ds_read_b64_tr_b16 a[148:149], v78 offset:40960\n"
    "v_mfma_f32_16x16x32_bf16 v[4:7], a[96:99], a[32:35], 0\n"
    "v_exp_f32 v16, v16\n"
    "v_mfma_f32_16x16x32_bf16 v[4:7], a[100:103], a[36:39], v[4:7]\n"
    "ds_read_b64_tr_b16 a[150:151], v78 offset:45056\n"
    "v_mfma_f32_16x16x32_bf16 v[4:7], a[104:107], a[40:43], v[4:7]\n"
    "v_exp_f32 v17, v17\n"
    "v_mfma_f32_16x16x32_bf16 v[4:7], a[108:111], a[44:47], v[4:7]\n"
    "ds_read_b64_tr_b16 a[152:153], v79 offset:32768\n"
    "v_cvt_pk_bf16_f32 v58, v16, v17\n"
    "v_mfma_f32_16x16x32_bf16 v[4:7], a[112:115], a[48:51], v[4:7]\n"
    "ds_read_b64_tr_b16 a[154:155], v79 offset:36864\n"
    "ds_read_b64_tr_b16 a[156:157], v79 offset:40960\n"
    "v_mfma_f32_16x16x32_bf16 v[4:7], a[116:119], a[52:55], v[4:7]\n"
    "v_exp_f32 v18, v18\n"
    "v_mfma_f32_16x16x32_bf16 v[4:7], a[120:123], a[56:59], v[4:7]\n"
    "ds_read_b64_tr_b16 a[158:159], v79 offset:45056\n"
    "v_mfma_f32_16x16x32_bf16 v[4:7], a[124:127], a[60:63], v[4:7]\n"
    "v_exp_f32 v19, v19\n"
    "v_mfma_f32_16x16x32_bf16 v[8:11], a[96:99], a[64:67], 0\n"
    "v_exp_f32 v20, v20\n"
    "v_mfma_f32_16x16x32_bf16 v[8:11], a[100:103], a[68:71], v[8:11]\n"
    "v_exp_f32 v21, v21\n"
    "v_mfma_f32_16x16x32_bf16 v[8:11], a[104:107], a[72:75], v[8:11]\n"
    "v_exp_f32 v22, v22\n"
    "v_mfma_f32_16x16x32_bf16 v[8:11], a[108:111], a[76:79], v[8:11]\n"
    "v_exp_f32 v23, v23\n"
    "v_mfma_f32_16x16x32_bf16 v[8:11], a[112:115], a[80:83], v[8:11]\n"
    "v_cvt_pk_bf16_f32 v59, v18, v19\n"
    "v_cvt_pk_bf16_f32 v66, v20, v21\n"
    "v_mfma_f32_16x16x32_bf16 v[8:11], a[116:119], a[84:87], v[8:11]\n"
    "v_cvt_pk_bf16_f32 v67, v22, v23\n"
    "v_mfma_f32_16x16x32_bf16 v[8:11], a[120:123], a[88:91], v[8:11]\n"
    "v_mfma_f32_16x16x32_bf16 v[8:11], a[124:127], a[92:95], v[8:11]\n"
    "s_waitcnt vmcnt(14) lgkmcnt(0)\n"
    "s_barrier\n"
    "v_mfma_f32_16x16x32_bf16 a[172:175], a[160:163], v[40:43], a[172:175]\n"
    "v_fma_f32 v0, v0, s96, v91\n"
    "s_add_u32 m0, s91, 32768\n"
    "v_mfma_f32_16x16x32_bf16 a[172:175], a[160:163], v[44:47], a[172:175]\n"
    "buffer_load_dwordx4 v80, s[20:23], s42 offen lds\n"
    "v_mfma_f32_16x16x32_bf16 v[188:191], a[128:131], v[24:27], v[188:191]\n"
    "ds_read_b128 a[96:99], v72 offset:49152\n"
    "v_fma_f32 v1, v1, s96, v91\n"
    "v_mfma_f32_16x16x32_bf16 v[188:191], a[132:135], v[28:31], v[188:191]\n"
    "ds_read_b128 a[100:103], v73 offset:49152\n"
    "v_fma_f32 v2, v2, s96, v91\n"
    "v_mfma_f32_16x16x32_bf16 v[192:195], a[128:131], v[32:35], v[192:195]\n"
    "ds_read_b128 a[104:107], v74 offset:49152\n"
    "v_fma_f32 v3, v3, s96, v91\n"
    "v_mfma_f32_16x16x32_bf16 v[192:195], a[132:135], v[36:39], v[192:195]\n"
    "buffer_load_dwordx4 v81, s[20:23], s42 offen offset:1024 nt lds\n"
    "v_mfma_f32_16x16x32_bf16 v[196:199], a[128:131], v[40:43], v[196:199]\n"
    "ds_read_b128 a[108:111], v75 offset:49152\n"
    "v_fma_f32 v4, v4, s96, v92\n"
    "v_mfma_f32_16x16x32_bf16 v[196:199], a[132:135], v[44:47], v[196:199]\n"
    "v_fma_f32 v5, v5, s96, v92\n"
    "v_fma_f32 v6, v6, s96, v92\n"
    "v_mfma_f32_16x16x32_bf16 v[200:203], a[136:139], v[24:27], v[200:203]\n"
    "ds_read_b128 a[112:115], v72 offset:49408\n"
    "v_fma_f32 v7, v7, s96, v92\n"
    "v_mfma_f32_16x16x32_bf16 v[200:203], a[140:143], v[28:31], v[200:203]\n"
    "buffer_load_dwordx4 v82, s[24:27], s66 offen offset:2048 nt lds\n"
    "v_mfma_f32_16x16x32_bf16 v[204:207], a[136:139], v[32:35], v[204:207]\n"
    "ds_read_b128 a[116:119], v73 offset:49408\n"
    "v_fma_f32 v8, v8, s96, v93\n"
    "v_mfma_f32_16x16x32_bf16 v[204:207], a[140:143], v[36:39], v[204:207]\n"
    "ds_read_b128 a[120:123], v74 offset:49408\n"
    "v_fma_f32 v9, v9, s96, v93\n"
    "v_mfma_f32_16x16x32_bf16 v[208:211], a[136:139], v[40:43], v[208:211]\n"
    "v_fma_f32 v10, v10, s96, v93\n"
    "v_fma_f32 v11, v11, s96, v93\n"
    "v_mfma_f32_16x16x32_bf16 v[208:211], a[140:143], v[44:47], v[208:211]\n"
    "buffer_load_dwordx4 v83, s[24:27], s66 offen offset:3072 nt lds\n"
    "v_mfma_f32_16x16x32_bf16 v[212:215], a[144:147], v[24:27], v[212:215]\n"
    "ds_read_b128 a[124:127], v75 offset:49408\n"
    "v_max3_f32 v97, v0, v1, v2\n"
    "v_mfma_f32_16x16x32_bf16 v[212:215], a[148:151], v[28:31], v[212:215]\n"
    "v_max3_f32 v97, v97, v3, v4\n"
    "v_max3_f32 v97, v97, v5, v6\n"
    "v_mfma_f32_16x16x32_bf16 v[216:219], a[144:147], v[32:35], v[216:219]\n"
    "v_max3_f32 v97, v97, v7, v8\n"
    "v_max3_f32 v97, v97, v9, v10\n"
    "v_mfma_f32_16x16x32_bf16 v[216:219], a[148:151], v[36:39], v[216:219]\n"
    "v_max_f32 v97, v97, v11\n"
    "v_cmp_lt_f32 vcc, s97, v97\n"
    "v_mfma_f32_16x16x32_bf16 v[220:223], a[144:147], v[40:43], v[220:223]\n"
    "v_mfma_f32_16x16x32_bf16 v[220:223], a[148:151], v[44:47], v[220:223]\n"
    "v_mfma_f32_16x16x32_bf16 a[200:203], a[152:155], v[24:27], a[200:203]\n"
    "v_mfma_f32_16x16x32_bf16 a[200:203], a[156:159], v[28:31], a[200:203]\n"
    "v_mfma_f32_16x16x32_bf16 a[204:207], a[152:155], v[32:35], a[204:207]\n"
    "ds_read_b64_tr_b16 a[128:129], v76 offset:49152\n"
    "s_cbranch_vccnz STUBb22c0_%=\n"
    "RETb22c0_%=:\n"
    "v_mfma_f32_16x16x32_bf16 a[204:207], a[156:159], v[36:39], a[204:207]\n"
    "ds_read_b64_tr_b16 a[130:131], v76 offset:53248\n"
    "ds_read_b64_tr_b16 a[132:133], v76 offset:57344\n"
    "v_mfma_f32_16x16x32_bf16 a[208:211], a[152:155], v[40:43], a[208:211]\n"
    "v_exp_f32 v0, v0\n"
    "v_mfma_f32_16x16x32_bf16 a[208:211], a[156:159], v[44:47], a[208:211]\n"
    "ds_read_b64_tr_b16 a[134:135], v76 offset:61440\n"
    "s_waitcnt lgkmcnt(4)\n"
    "v_mfma_f32_16x16x32_bf16 v[12:15], a[96:99], a[0:3], 0\n"
    "v_exp_f32 v1, v1\n"
    "v_mfma_f32_16x16x32_bf16 v[12:15], a[100:103], a[4:7], v[12:15]\n"
    "ds_read_b64_tr_b16 a[136:137], v77 offset:49152\n"
    "v_cvt_pk_bf16_f32 v52, v0, v1\n"
    "v_mfma_f32_16x16x32_bf16 v[12:15], a[104:107], a[8:11], v[12:15]\n"
    "ds_read_b64_tr_b16 a[138:139], v77 offset:53248\n"
    "ds_read_b64_tr_b16 a[140:141], v77 offset:57344\n"
    "v_mfma_f32_16x16x32_bf16 v[12:15], a[108:111], a[12:15], v[12:15]\n"
    "v_exp_f32 v2, v2\n"
    "v_mfma_f32_16x16x32_bf16 v[12:15], a[112:115], a[16:19], v[12:15]\n"
    "ds_read_b64_tr_b16 a[142:143], v77 offset:61440\n"
    "v_mfma_f32_16x16x32_bf16 v[12:15], a[116:119], a[20:23], v[12:15]\n"
    "v_exp_f32 v3, v3\n"
    "v_mfma_f32_16x16x32_bf16 v[12:15], a[120:123], a[24:27], v[12:15]\n"
    "ds_read_b64_tr_b16 a[144:145], v78 offset:49152\n"
    "v_cvt_pk_bf16_f32 v53, v2, v3\n"
    "v_mfma_f32_16x16x32_bf16 v[12:15], a[124:127], a[28:31], v[12:15]\n"
    "ds_read_b64_tr_b16 a[146:147], v78 offset:53248\n"
    "ds_read_b64_tr_b16 a[148:149], v78 offset:57344\n"
    "v_mfma_f32_16x16x32_bf16 v[16:19], a[96:99], a[32:35], 0\n"
    "v_exp_f32 v4, v4\n"
    "v_mfma_f32_16x16x32_bf16 v[16:19], a[100:103], a[36:39], v[16:19]\n"
    "ds_read_b64_tr_b16 a[150:151], v78 offset:61440\n"
    "v_mfma_f32_16x16x32_bf16 v[16:19], a[104:107], a[40:43], v[16:19]\n"
    "v_exp_f32 v5, v5\n"
    "v_mfma_f32_16x16x32_bf16 v[16:19], a[108:111], a[44:47], v[16:19]\n"
    "ds_read_b64_tr_b16 a[152:153], v79 offset:49152\n"
    "v_cvt_pk_bf16_f32 v60, v4, v5\n"
    "v_mfma_f32_16x16x32_bf16 v[16:19], a[112:115], a[48:51], v[16:19]\n"
    "ds_read_b64_tr_b16 a[154:155], v79 offset:53248\n"
    "ds_read_b64_tr_b16 a[156:157], v79 offset:57344\n"
    "v_mfma_f32_16x16x32_bf16 v[16:19], a[116:119], a[52:55], v[16:19]\n"
    "v_exp_f32 v6, v6\n"
    "v_mfma_f32_16x16x32_bf16 v[16:19], a[120:123], a[56:59], v[16:19]\n"
    "ds_read_b64_tr_b16 a[158:159], v79 offset:61440\n"
    "v_mfma_f32_16x16x32_bf16 v[16:19], a[124:127], a[60:63], v[16:19]\n"
    "v_exp_f32 v7, v7\n"
    "v_mfma_f32_16x16x32_bf16 v[20:23], a[96:99], a[64:67], 0\n"
    "v_exp_f32 v8, v8\n"
    "v_mfma_f32_16x16x32_bf16 v[20:23], a[100:103], a[68:71], v[20:23]\n"
    "v_exp_f32 v9, v9\n"
    "v_mfma_f32_16x16x32_bf16 v[20:23], a[104:107], a[72:75], v[20:23]\n"
    "v_exp_f32 v10, v10\n"
    "v_mfma_f32_16x16x32_bf16 v[20:23], a[108:111], a[76:79], v[20:23]\n"
    "v_exp_f32 v11, v11\n"
    "v_mfma_f32_16x16x32_bf16 v[20:23], a[112:115], a[80:83], v[20:23]\n"
    "v_cvt_pk_bf16_f32 v61, v6, v7\n"
    "v_cvt_pk_bf16_f32 v68, v8, v9\n"
    "v_mfma_f32_16x16x32_bf16 v[20:23], a[116:119], a[84:87], v[20:23]\n"
    "v_cvt_pk_bf16_f32 v69, v10, v11\n"
    "v_mfma_f32_16x16x32_bf16 v[20:23], a[120:123], a[88:91], v[20:23]\n"
    "v_mfma_f32_16x16x32_bf16 v[20:23], a[124:127], a[92:95], v[20:23]\n"
    "s_cmp_eq_u32 s98, 3\n"
    "s_cbranch_scc1 TAIL0_%=\n"
    "s_waitcnt vmcnt(12) lgkmcnt(0)\n"
    "s_barrier\n"
    "v_mfma_f32_16x16x32_bf16 v[224:227], a[128:131], v[24:27], v[224:227]\n"
    "ds_read_b128 a[96:99], v72 offset:0\n"
    "s_add_u32 m0, s91, 49152\n"
    "v_mfma_f32_16x16x32_bf16 v[224:227], a[132:135], v[28:31], v[224:227]\n"
    "buffer_load_dwordx4 v80, s[20:23], s43 offen lds\n"
    "v_mfma_f32_16x16x32_bf16 v[228:231], a[128:131], v[32:35], v[228:231]\n"
    "ds_read_b128 a[100:103], v73 offset:0\n"
    "v_add_u32 v108, v103, v72\n"
    "v_mfma_f32_16x16x32_bf16 v[228:231], a[132:135], v[36:39], v[228:231]\n"
    "ds_read_b128 a[104:107], v74 offset:0\n"
    "v_add_u32 v109, v103, v73\n"
    "v_mfma_f32_16x16x32_bf16 v[232:235], a[128:131], v[40:43], v[232:235]\n"
    "ds_read_b128 a[0:3], v108 offset:0\n"
    "v_add_u32 v110, v103, v74\n"
    "v_mfma_f32_16x16x32_bf16 v[232:235], a[132:135], v[44:47], v[232:235]\n"
    "buffer_load_dwordx4 v81, s[20:23], s43 offen offset:1024 nt lds\n"
    "v_mfma_f32_16x16x32_bf16 v[236:239], a[136:139], v[24:27], v[236:239]\n"
    "ds_read_b128 a[4:7], v109 offset:0\n"
    "ds_read_b128 a[8:11], v110 offset:0\n"
    "v_mfma_f32_16x16x32_bf16 v[236:239], a[140:143], v[28:31], v[236:239]\n"
    "ds_read_b128 a[108:111], v75 offset:0\n"
    "v_add_u32 v111, v103, v75\n"
    "v_mfma_f32_16x16x32_bf16 v[240:243], a[136:139], v[32:35], v[240:243]\n"
    "ds_read_b128 a[112:115], v72 offset:256\n"
    "ds_read_b128 a[12:15], v111 offset:0\n"
    "v_mfma_f32_16x16x32_bf16 v[240:243], a[140:143], v[36:39], v[240:243]\n"
    "buffer_load_dwordx4 v82, s[24:27], s67 offen offset:2048 nt lds\n"
    "v_mfma_f32_16x16x32_bf16 v[244:247], a[136:139], v[40:43], v[244:247]\n"
    "ds_read_b128 a[16:19], v108 offset:256\n"
    "ds_read_b128 a[116:119], v73 offset:256\n"
    "v_mfma_f32_16x16x32_bf16 v[244:247], a[140:143], v[44:47], v[244:247]\n"
    "ds_read_b128 a[20:23], v109 offset:256\n"
    "v_fma_f32 v12, v12, s96, v91\n"
    "v_mfma_f32_16x16x32_bf16 v[248:251], a[144:147], v[24:27], v[248:251]\n"
    "ds_read_b128 a[120:123], v74 offset:256\n"
    "ds_read_b128 a[24:27], v110 offset:256\n"
    "v_mfma_f32_16x16x32_bf16 v[248:251], a[148:151], v[28:31], v[248:251]\n"
    "buffer_load_dwordx4 v83, s[24:27], s67 offen offset:3072 nt lds\n"
    "v_mfma_f32_16x16x32_bf16 v[252:255], a[144:147], v[32:35], v[252:255]\n"
    "ds_read_b128 a[28:31], v111 offset:256\n"
    "ds_read_b128 a[124:127], v75 offset:256\n"
    "v_mfma_f32_16x16x32_bf16 v[252:255], a[148:151], v[36:39], v[252:255]\n"
    "ds_read_b128 a[32:35], v108 offset:8192\n"
    "v_fma_f32 v13, v13, s96, v91\n"
    "v_mfma_f32_16x16x32_bf16 a[224:227], a[144:147], v[40:43], a[224:227]\n"
    "ds_read_b128 a[36:39], v109 offset:8192\n"
    "v_fma_f32 v14, v14, s96, v91\n"
    "v_mfma_f32_16x16x32_bf16 a[224:227], a[148:151], v[44:47], a[224:227]\n"
    "ds_read_b128 a[40:43], v110 offset:8192\n"
    "v_fma_f32 v15, v15, s96, v91\n"
    "v_mfma_f32_16x16x32_bf16 a[212:215], a[152:155], v[24:27], a[212:215]\n"
    "ds_read_b128 a[44:47], v111 offset:8192\n"
    "v_fma_f32 v16, v16, s96, v92\n"
    "v_mfma_f32_16x16x32_bf16 a[212:215], a[156:159], v[28:31], a[212:215]\n"
    "ds_read_b128 a[48:51], v108 offset:8448\n"
    "v_fma_f32 v17, v17, s96, v92\n"
    "v_mfma_f32_16x16x32_bf16 a[216:219], a[152:155], v[32:35], a[216:219]\n"
    "ds_read_b64_tr_b16 a[128:129], v76 offset:0\n"
    "v_fma_f32 v18, v18, s96, v92\n"
    "v_mfma_f32_16x16x32_bf16 a[216:219], a[156:159], v[36:39], a[216:219]\n"
    "ds_read_b128 a[52:55], v109 offset:8448\n"
    "v_fma_f32 v19, v19, s96, v92\n"
    "v_mfma_f32_16x16x32_bf16 a[220:223], a[152:155], v[40:43], a[220:223]\n"
    "ds_read_b64_tr_b16 a[130:131], v76 offset:4096\n"
    "v_fma_f32 v20, v20, s96, v93\n"
    "v_mfma_f32_16x16x32_bf16 a[220:223], a[156:159], v[44:47], a[220:223]\n"
    "ds_read_b128 a[56:59], v110 offset:8448\n"
    "v_fma_f32 v21, v21, s96, v93\n"
    "s_waitcnt lgkmcnt(15)\n"
    "v_mfma_f32_16x16x32_bf16 v[0:3], a[96:99], a[0:3], 0\n"
    "ds_read_b64_tr_b16 a[132:133], v76 offset:8192\n"
    "v_fma_f32 v22, v22, s96, v93\n"
    "v_mfma_f32_16x16x32_bf16 v[0:3], a[100:103], a[4:7], v[0:3]\n"
    "ds_read_b64_tr_b16 a[134:135], v76 offset:12288\n"
    "ds_read_b128 a[60:63], v111 offset:8448\n"
    "v_mfma_f32_16x16x32_bf16 v[0:3], a[104:107], a[8:11], v[0:3]\n"
    "s_waitcnt vmcnt(8)\n"
    "ds_read_b128 a[64:67], v108 offset:16384\n"
    "v_fma_f32 v23, v23, s96, v93\n"
    "v_mfma_f32_16x16x32_bf16 v[0:3], a[108:111], a[12:15], v[0:3]\n"
    "ds_read_b64_tr_b16 a[136:137], v77 offset:0\n"
    "v_max3_f32 v97, v12, v13, v14\n"
    "v_mfma_f32_16x16x32_bf16 v[0:3], a[112:115], a[16:19], v[0:3]\n"
    "ds_read_b64_tr_b16 a[138:139], v77 offset:4096\n"
    "ds_read_b128 a[68:71], v109 offset:16384\n"
    "s_waitcnt lgkmcnt(15)\n"
    "v_mfma_f32_16x16x32_bf16 v[0:3], a[116:119], a[20:23], v[0:3]\n"
    "ds_read_b64_tr_b16 a[140:141], v77 offset:8192\n"
    "ds_read_b128 a[72:75], v110 offset:16384\n"
    "v_mfma_f32_16x16x32_bf16 v[0:3], a[120:123], a[24:27], v[0:3]\n"
    "ds_read_b64_tr_b16 a[142:143], v77 offset:12288\n"
    "ds_read_b128 a[76:79], v111 offset:16384\n"
    "v_mfma_f32_16x16x32_bf16 v[0:3], a[124:127], a[28:31], v[0:3]\n"
    "ds_read_b64_tr_b16 a[144:145], v78 offset:0\n"
    "v_max3_f32 v97, v97, v15, v16\n"
    "v_mfma_f32_16x16x32_bf16 v[4:7], a[96:99], a[32:35], 0\n"
    "ds_read_b128 a[80:83], v108 offset:16640\n"
    "v_max3_f32 v97, v97, v17, v18\n"
    "s_waitcnt lgkmcnt(15)\n"
    "v_mfma_f32_16x16x32_bf16 v[4:7], a[100:103], a[36:39], v[4:7]\n"
    "ds_read_b64_tr_b16 a[146:147], v78 offset:4096\n"
    "ds_read_b128 a[84:87], v109 offset:16640\n"
    "v_mfma_f32_16x16x32_bf16 v[4:7], a[104:107], a[40:43], v[4:7]\n"
    "ds_read_b64_tr_b16 a[148:149], v78 offset:8192\n"
    "v_max3_f32 v97, v97, v19, v20\n"
    "v_mfma_f32_16x16x32_bf16 v[4:7], a[108:111], a[44:47], v[4:7]\n"
    "ds_read_b64_tr_b16 a[150:151], v78 offset:12288\n"
    "ds_read_b128 a[88:91], v110 offset:16640\n"
    "v_mfma_f32_16x16x32_bf16 v[4:7], a[112:115], a[48:51], v[4:7]\n"
    "ds_read_b128 a[92:95], v111 offset:16640\n"
    "v_max3_f32 v97, v97, v21, v22\n"
    "v_mfma_f32_16x16x32_bf16 v[4:7], a[116:119], a[52:55], v[4:7]\n"
    "ds_read_b64_tr_b16 a[152:153], v79 offset:0\n"
    "v_max_f32 v97, v97, v23\n"
    "s_waitcnt lgkmcnt(15)\n"
    "v_mfma_f32_16x16x32_bf16 v[4:7], a[120:123], a[56:59], v[4:7]\n"
    "ds_read_b64_tr_b16 a[154:155], v79 offset:4096\n"
    "v_cmp_lt_f32 vcc, s97, v97\n"
    "v_mfma_f32_16x16x32_bf16 v[4:7], a[124:127], a[60:63], v[4:7]\n"
    "ds_read_b64_tr_b16 a[156:157], v79 offset:8192\n"
    "ds_read_b64_tr_b16 a[158:159], v79 offset:12288\n"
    "v_mfma_f32_16x16x32_bf16 v[8:11], a[96:99], a[64:67], 0\n"
    "s_add_u32 s24, s24, 25165824\n"
    "s_addc_u32 s25, s25, 0\n"
    "s_waitcnt lgkmcnt(15)\n"
    "v_mfma_f32_16x16x32_bf16 v[8:11], a[100:103], a[68:71], v[8:11]\n"
    "s_cbranch_vccnz STUBb23c0_%=\n"
    "RETb23c0_%=:\n"
    "s_waitcnt lgkmcnt(14)\n"
    "v_mfma_f32_16x16x32_bf16 v[8:11], a[104:107], a[72:75], v[8:11]\n"
    "v_exp_f32 v12, v12\n"
    "s_waitcnt lgkmcnt(12)\n"
    "v_mfma_f32_16x16x32_bf16 v[8:11], a[108:111], a[76:79], v[8:11]\n"
    "v_exp_f32 v13, v13\n"
    "s_waitcnt lgkmcnt(10)\n"
    "v_mfma_f32_16x16x32_bf16 v[8:11], a[112:115], a[80:83], v[8:11]\n"
    "v_exp_f32 v14, v14\n"
    "s_waitcnt lgkmcnt(8)\n"
    "v_mfma_f32_16x16x32_bf16 v[8:11], a[116:119], a[84:87], v[8:11]\n"
    "v_exp_f32 v15, v15\n"
    "s_waitcnt lgkmcnt(5)\n"
    "v_mfma_f32_16x16x32_bf16 v[8:11], a[120:123], a[88:91], v[8:11]\n"
    "v_exp_f32 v16, v16\n"
    "s_waitcnt lgkmcnt(4)\n"
    "v_mfma_f32_16x16x32_bf16 v[8:11], a[124:127], a[92:95], v[8:11]\n"
    "v_exp_f32 v17, v17\n"
    "v_exp_f32 v18, v18\n"
    "v_exp_f32 v19, v19\n"
    "v_exp_f32 v20, v20\n"
    "v_exp_f32 v21, v21\n"
    "v_exp_f32 v22, v22\n"
    "v_exp_f32 v23, v23\n"
    "v_cvt_pk_bf16_f32 v54, v12, v13\n"
    "v_cvt_pk_bf16_f32 v55, v14, v15\n"
    "v_cvt_pk_bf16_f32 v62, v16, v17\n"
    "v_cvt_pk_bf16_f32 v63, v18, v19\n"
    "v_cvt_pk_bf16_f32 v70, v20, v21\n"
    "v_cvt_pk_bf16_f32 v71, v22, v23\n"
    "s_add_u32 s98, s98, 1\n"
    "s_waitcnt lgkmcnt(0)\n"
    "s_barrier\n"
    "v_mfma_f32_16x16x32_bf16 a[164:167], a[160:163], v[48:51], a[164:167]\n"
    "v_max3_f32 v97, v0, v1, v2\n"
    "v_max3_f32 v98, v4, v5, v6\n"
    "v_mfma_f32_16x16x32_bf16 a[164:167], a[160:163], v[52:55], a[164:167]\n"
    "ds_read_b128 a[96:99], v72 offset:16384\n"
    "v_max3_f32 v99, v8, v9, v10\n"
    "v_mfma_f32_16x16x32_bf16 a[168:171], a[160:163], v[56:59], a[168:171]\n"
    "ds_read_b128 a[100:103], v73 offset:16384\n"
    "v_max_f32 v97, v97, v3\n"
    "v_mfma_f32_16x16x32_bf16 a[168:171], a[160:163], v[60:63], a[168:171]\n"
    "v_max_f32 v98, v98, v7\n"
    "v_max_f32 v99, v99, v11\n"
    "v_mfma_f32_16x16x32_bf16 a[172:175], a[160:163], v[64:67], a[172:175]\n"
    "ds_read_b128 a[104:107], v74 offset:16384\n"
    "v_mov_b32 v100, v97\n"
    "v_mfma_f32_16x16x32_bf16 a[172:175], a[160:163], v[68:71], a[172:175]\n"
    "v_mov_b32 v101, v98\n"
    "v_mov_b32 v102, v99\n"
    "v_mfma_f32_16x16x32_bf16 v[116:119], a[128:131], v[48:51], v[116:119]\n"
    "ds_read_b128 a[108:111], v75 offset:16384\n"
    "v_permlane32_swap_b32 v97, v100\n"
    "v_mfma_f32_16x16x32_bf16 v[116:119], a[132:135], v[52:55], v[116:119]\n"
    "v_permlane32_swap_b32 v98, v101\n"
    "v_permlane32_swap_b32 v99, v102\n"
    "v_mfma_f32_16x16x32_bf16 v[120:123], a[128:131], v[56:59], v[120:123]\n"
    "v_max_f32 v97, v97, v100\n"
    "v_max_f32 v98, v98, v101\n"
    "v_mfma_f32_16x16x32_bf16 v[120:123], a[132:135], v[60:63], v[120:123]\n"
    "ds_read_b128 a[112:115], v72 offset:16640\n"
    "v_max_f32 v99, v99, v102\n"
    "v_mfma_f32_16x16x32_bf16 v[124:127], a[128:131], v[64:67], v[124:127]\n"
    "ds_read_b128 a[116:119], v73 offset:16640\n"
    "v_mov_b32 v100, v97\n"
    "v_mfma_f32_16x16x32_bf16 v[124:127], a[132:135], v[68:71], v[124:127]\n"
    "v_mov_b32 v101, v98\n"
    "v_mov_b32 v102, v99\n"
    "v_mfma_f32_16x16x32_bf16 v[128:131], a[136:139], v[48:51], v[128:131]\n"
    "ds_read_b128 a[120:123], v74 offset:16640\n"
    "v_permlane16_swap_b32 v97, v100\n"
    "v_mfma_f32_16x16x32_bf16 v[128:131], a[140:143], v[52:55], v[128:131]\n"
    "v_permlane16_swap_b32 v98, v101\n"
    "v_permlane16_swap_b32 v99, v102\n"
    "v_mfma_f32_16x16x32_bf16 v[132:135], a[136:139], v[56:59], v[132:135]\n"
    "ds_read_b128 a[124:127], v75 offset:16640\n"
    "v_max_f32 v97, v97, v100\n"
    "v_mfma_f32_16x16x32_bf16 v[132:135], a[140:143], v[60:63], v[132:135]\n"
    "v_max_f32 v98, v98, v101\n"
    "v_max_f32 v99, v99, v102\n"
    "v_mfma_f32_16x16x32_bf16 v[136:139], a[136:139], v[64:67], v[136:139]\n"
    "v_mul_f32 v91, s99, v97\n"
    "v_mul_f32 v92, s99, v98\n"
    "v_mfma_f32_16x16x32_bf16 v[136:139], a[140:143], v[68:71], v[136:139]\n"
    "v_mul_f32 v93, s99, v99\n"
    "v_fma_f32 v0, v0, s96, v91\n"
    "v_mfma_f32_16x16x32_bf16 v[140:143], a[144:147], v[48:51], v[140:143]\n"
    "v_fma_f32 v1, v1, s96, v91\n"
    "v_fma_f32 v2, v2, s96, v91\n"
    "v_mfma_f32_16x16x32_bf16 v[140:143], a[148:151], v[52:55], v[140:143]\n"
    "v_fma_f32 v3, v3, s96, v91\n"
    "v_fma_f32 v4, v4, s96, v92\n"
    "v_mfma_f32_16x16x32_bf16 v[144:147], a[144:147], v[56:59], v[144:147]\n"
    "v_fma_f32 v5, v5, s96, v92\n"
    "v_fma_f32 v6, v6, s96, v92\n"
    "v_mfma_f32_16x16x32_bf16 v[144:147], a[148:151], v[60:63], v[144:147]\n"
    "v_fma_f32 v7, v7, s96, v92\n"
    "v_fma_f32 v8, v8, s96, v93\n"
    "v_mfma_f32_16x16x32_bf16 v[148:151], a[144:147], v[64:67], v[148:151]\n"
    "v_fma_f32 v9, v9, s96, v93\n"
    "v_fma_f32 v10, v10, s96, v93\n"
    "v_mfma_f32_16x16x32_bf16 v[148:151], a[148:151], v[68:71], v[148:151]\n"
    "v_fma_f32 v11, v11, s96, v93\n"
    "s_mov_b32 s38, 24576\n"
    "v_mfma_f32_16x16x32_bf16 a[176:179], a[152:155], v[48:51], a[176:179]\n"
    "v_exp_f32 v0, v0\n"
    "v_mfma_f32_16x16x32_bf16 a[176:179], a[156:159], v[52:55], a[176:179]\n"
    "v_exp_f32 v1, v1\n"
    "v_mfma_f32_16x16x32_bf16 a[180:183], a[152:155], v[56:59], a[180:183]\n"
    "ds_read_b64_tr_b16 a[128:129], v76 offset:16384\n"
    "v_cvt_pk_bf16_f32 v24, v0, v1\n"
    "v_mfma_f32_16x16x32_bf16 a[180:183], a[156:159], v[60:63], a[180:183]\n"
    "ds_read_b64_tr_b16 a[130:131], v76 offset:20480\n"
    "ds_read_b64_tr_b16 a[132:133], v76 offset:24576\n"
    "v_mfma_f32_16x16x32_bf16 a[184:187], a[152:155], v[64:67], a[184:187]\n"
    "v_exp_f32 v2, v2\n"
    "v_mfma_f32_16x16x32_bf16 a[184:187], a[156:159], v[68:71], a[184:187]\n"
    "ds_read_b64_tr_b16 a[134:135], v76 offset:28672\n"
    "v_accvgpr_read_b32 v94, a164\n"
    "v_accvgpr_read_b32 v95, a168\n"
    "v_accvgpr_read_b32 v96, a172\n"
    "v_rcp_f32 v94, v94\n"
    "v_rcp_f32 v95, v95\n"
    "v_rcp_f32 v96, v96\n"
    "v_pk_mul_f32 v[116:117], v[116:117], v[94:95] op_sel_hi:[1,0]\n"
    "v_pk_mul_f32 v[118:119], v[118:119], v[94:95] op_sel_hi:[1,0]\n"
    "v_cvt_pk_bf16_f32 v116, v116, v117\n"
    "v_cvt_pk_bf16_f32 v117, v118, v119\n"
    "v_pk_mul_f32 v[120:121], v[120:121], v[94:95] op_sel:[0,1] op_sel_hi:[1,1]\n"
    "v_pk_mul_f32 v[122:123], v[122:123], v[94:95] op_sel:[0,1] op_sel_hi:[1,1]\n"
    "v_cvt_pk_bf16_f32 v120, v120, v121\n"
    "v_cvt_pk_bf16_f32 v121, v122, v123\n"
    "v_pk_mul_f32 v[124:125], v[124:125], v[96:97] op_sel_hi:[1,0]\n"
    "v_pk_mul_f32 v[126:127], v[126:127], v[96:97] op_sel_hi:[1,0]\n"
    "v_cvt_pk_bf16_f32 v124, v124, v125\n"
    "v_cvt_pk_bf16_f32 v125, v126, v127\n"
    "ds_write_b64 v112, v[116:117] offset:0\n"
    "ds_write_b64 v112, v[120:121] offset:4096\n"
    "ds_write_b64 v112, v[124:125] offset:8192\n"
    "v_pk_mul_f32 v[128:129], v[128:129], v[94:95] op_sel_hi:[1,0]\n"
    "v_pk_mul_f32 v[130:131], v[130:131], v[94:95] op_sel_hi:[1,0]\n"
    "v_cvt_pk_bf16_f32 v128, v128, v129\n"
    "v_cvt_pk_bf16_f32 v129, v130, v131\n"
    "v_pk_mul_f32 v[132:133], v[132:133], v[94:95] op_sel:[0,1] op_sel_hi:[1,1]\n"
    "v_pk_mul_f32 v[134:135], v[134:135], v[94:95] op_sel:[0,1] op_sel_hi:[1,1]\n"
    "v_cvt_pk_bf16_f32 v132, v132, v133\n"
    "v_cvt_pk_bf16_f32 v133, v134, v135\n"
    "v_pk_mul_f32 v[136:137], v[136:137], v[96:97] op_sel_hi:[1,0]\n"
    "v_pk_mul_f32 v[138:139], v[138:139], v[96:97] op_sel_hi:[1,0]\n"
    "v_cvt_pk_bf16_f32 v136, v136, v137\n"
    "v_cvt_pk_bf16_f32 v137, v138, v139\n"
    "ds_write_b64 v113, v[128:129] offset:0\n"
    "ds_write_b64 v113, v[132:133] offset:4096\n"
    "ds_write_b64 v113, v[136:137] offset:8192\n"
    "v_pk_mul_f32 v[140:141], v[140:141], v[94:95] op_sel_hi:[1,0]\n"
    "v_pk_mul_f32 v[142:143], v[142:143], v[94:95] op_sel_hi:[1,0]\n"
    "v_cvt_pk_bf16_f32 v140, v140, v141\n"
    "v_cvt_pk_bf16_f32 v141, v142, v143\n"
    "v_pk_mul_f32 v[144:145], v[144:145], v[94:95] op_sel:[0,1] op_sel_hi:[1,1]\n"
    "v_pk_mul_f32 v[146:147], v[146:147], v[94:95] op_sel:[0,1] op_sel_hi:[1,1]\n"
    "v_cvt_pk_bf16_f32 v144, v144, v145\n"
    "v_cvt_pk_bf16_f32 v145, v146, v147\n"
    "v_pk_mul_f32 v[148:149], v[148:149], v[96:97] op_sel_hi:[1,0]\n"
    "v_pk_mul_f32 v[150:151], v[150:151], v[96:97] op_sel_hi:[1,0]\n"
    "v_cvt_pk_bf16_f32 v148, v148, v149\n"
    "v_cvt_pk_bf16_f32 v149, v150, v151\n"
    "ds_write_b64 v114, v[140:141] offset:0\n"
    "ds_write_b64 v114, v[144:145] offset:4096\n"
    "ds_write_b64 v114, v[148:149] offset:8192\n"
    "v_accvgpr_read_b32 v12, a176\n"
    "v_accvgpr_read_b32 v13, a177\n"
    "v_accvgpr_read_b32 v14, a178\n"
    "v_accvgpr_read_b32 v15, a179\n"
    "v_pk_mul_f32 v[12:13], v[12:13], v[94:95] op_sel_hi:[1,0]\n"
    "v_pk_mul_f32 v[14:15], v[14:15], v[94:95] op_sel_hi:[1,0]\n"
    "v_cvt_pk_bf16_f32 v12, v12, v13\n"
    "v_cvt_pk_bf16_f32 v13, v14, v15\n"
    "v_accvgpr_read_b32 v16, a180\n"
    "v_accvgpr_read_b32 v17, a181\n"
    "v_accvgpr_read_b32 v18, a182\n"
    "v_accvgpr_read_b32 v19, a183\n"
    "v_pk_mul_f32 v[16:17], v[16:17], v[94:95] op_sel:[0,1] op_sel_hi:[1,1]\n"
    "v_pk_mul_f32 v[18:19], v[18:19], v[94:95] op_sel:[0,1] op_sel_hi:[1,1]\n"
    "v_cvt_pk_bf16_f32 v16, v16, v17\n"
    "v_cvt_pk_bf16_f32 v17, v18, v19\n"
    "v_accvgpr_read_b32 v20, a184\n"
    "v_accvgpr_read_b32 v21, a185\n"
    "v_accvgpr_read_b32 v22, a186\n"
    "v_accvgpr_read_b32 v23, a187\n"
    "v_pk_mul_f32 v[20:21], v[20:21], v[96:97] op_sel_hi:[1,0]\n"
    "v_pk_mul_f32 v[22:23], v[22:23], v[96:97] op_sel_hi:[1,0]\n"
    "v_cvt_pk_bf16_f32 v20, v20, v21\n"
    "v_cvt_pk_bf16_f32 v21, v22, v23\n"
    "ds_write_b64 v115, v[12:13] offset:0\n"
    "ds_write_b64 v115, v[16:17] offset:4096\n"
    "ds_write_b64 v115, v[20:21] offset:8192\n"
    "s_waitcnt lgkmcnt(15)\n"
    "v_mfma_f32_16x16x32_bf16 v[12:15], a[96:99], a[0:3], 0\n"
    "v_exp_f32 v3, v3\n"
    "v_mfma_f32_16x16x32_bf16 v[12:15], a[100:103], a[4:7], v[12:15]\n"
    "ds_read_b64_tr_b16 a[136:137], v77 offset:16384\n"
    "v_cvt_pk_bf16_f32 v25, v2, v3\n"
    "v_mfma_f32_16x16x32_bf16 v[12:15], a[104:107], a[8:11], v[12:15]\n"
    "ds_read_b64_tr_b16 a[138:139], v77 offset:20480\n"
    "ds_read_b64_tr_b16 a[140:141], v77 offset:24576\n"
    "v_mfma_f32_16x16x32_bf16 v[12:15], a[108:111], a[12:15], v[12:15]\n"
    "v_exp_f32 v4, v4\n"
    "v_mfma_f32_16x16x32_bf16 v[12:15], a[112:115], a[16:19], v[12:15]\n"
    "ds_read_b64_tr_b16 a[142:143], v77 offset:28672\n"
    "v_mfma_f32_16x16x32_bf16 v[12:15], a[116:119], a[20:23], v[12:15]\n"
    "v_exp_f32 v5, v5\n"
    "v_mfma_f32_16x16x32_bf16 v[12:15], a[120:123], a[24:27], v[12:15]\n"
    "ds_read_b64_tr_b16 a[144:145], v78 offset:16384\n"
    "v_cvt_pk_bf16_f32 v32, v4, v5\n"
    "v_mfma_f32_16x16x32_bf16 v[12:15], a[124:127], a[28:31], v[12:15]\n"
    "ds_read_b64_tr_b16 a[146:147], v78 offset:20480\n"
    "ds_read_b64_tr_b16 a[148:149], v78 offset:24576\n"
    "v_mfma_f32_16x16x32_bf16 v[16:19], a[96:99], a[32:35], 0\n"
    "v_exp_f32 v6, v6\n"
    "v_mfma_f32_16x16x32_bf16 v[16:19], a[100:103], a[36:39], v[16:19]\n"
    "ds_read_b64_tr_b16 a[150:151], v78 offset:28672\n"
    "v_mfma_f32_16x16x32_bf16 v[16:19], a[104:107], a[40:43], v[16:19]\n"
    "v_exp_f32 v7, v7\n"
    "v_mfma_f32_16x16x32_bf16 v[16:19], a[108:111], a[44:47], v[16:19]\n"
    "ds_read_b64_tr_b16 a[152:153], v79 offset:16384\n"
    "v_cvt_pk_bf16_f32 v33, v6, v7\n"
    "v_mfma_f32_16x16x32_bf16 v[16:19], a[112:115], a[48:51], v[16:19]\n"
    "ds_read_b64_tr_b16 a[154:155], v79 offset:20480\n"
    "ds_read_b64_tr_b16 a[156:157], v79 offset:24576\n"
    "v_mfma_f32_16x16x32_bf16 v[16:19], a[116:119], a[52:55], v[16:19]\n"
    "v_exp_f32 v8, v8\n"
    "v_mfma_f32_16x16x32_bf16 v[16:19], a[120:123], a[56:59], v[16:19]\n"
    "ds_read_b64_tr_b16 a[158:159], v79 offset:28672\n"
    "v_mfma_f32_16x16x32_bf16 v[16:19], a[124:127], a[60:63], v[16:19]\n"
    "v_exp_f32 v9, v9\n"
    "v_mfma_f32_16x16x32_bf16 v[20:23], a[96:99], a[64:67], 0\n"
    "v_exp_f32 v10, v10\n"
    "v_mfma_f32_16x16x32_bf16 v[20:23], a[100:103], a[68:71], v[20:23]\n"
    "v_exp_f32 v11, v11\n"
    "v_mfma_f32_16x16x32_bf16 v[20:23], a[104:107], a[72:75], v[20:23]\n"
    "v_cvt_pk_bf16_f32 v40, v8, v9\n"
    "v_cvt_pk_bf16_f32 v41, v10, v11\n"
    "v_mfma_f32_16x16x32_bf16 v[20:23], a[108:111], a[76:79], v[20:23]\n"
    "s_add_u32 s28, s28, 25165824\n"
    "s_addc_u32 s29, s29, 0\n"
    "v_mfma_f32_16x16x32_bf16 v[20:23], a[112:115], a[80:83], v[20:23]\n"
    "s_cmp_lt_u32 s98, 3\n"
    "s_cselect_b32 s30, s34, 0\n"
    "v_mfma_f32_16x16x32_bf16 v[20:23], a[116:119], a[84:87], v[20:23]\n"
    "s_add_u32 s36, s36, 25165824\n"
    "s_addc_u32 s37, s37, 0\n"
    "v_mfma_f32_16x16x32_bf16 v[20:23], a[120:123], a[88:91], v[20:23]\n"
    "s_add_u32 m0, s91, 0\n"
    "v_mfma_f32_16x16x32_bf16 v[20:23], a[124:127], a[92:95], v[20:23]\n"
    "buffer_load_dwordx4 v80, s[20:23], s44 offen lds\n"
    "buffer_load_dwordx4 v81, s[20:23], s44 offen offset:1024 nt lds\n"
    "buffer_load_dwordx4 v82, s[24:27], s68 offen offset:2048 nt lds\n"
    "buffer_load_dwordx4 v83, s[24:27], s68 offen offset:3072 nt lds\n"
    "s_waitcnt vmcnt(8) lgkmcnt(0)\n"
    "s_barrier\n"
    "v_mfma_f32_16x16x32_bf16 v[152:155], a[128:131], v[48:51], v[152:155]\n"
    "ds_read_b128 a[96:99], v72 offset:32768\n"
    "s_add_u32 m0, s91, 16384\n"
    "v_mfma_f32_16x16x32_bf16 v[152:155], a[132:135], v[52:55], v[152:155]\n"
    "buffer_load_dwordx4 v80, s[20:23], s45 offen lds\n"
    "v_mfma_f32_16x16x32_bf16 v[156:159], a[128:131], v[56:59], v[156:159]\n"
    "ds_read_b128 a[100:103], v73 offset:32768\n"
    "v_fma_f32 v12, v12, s96, v91\n"
    "v_mfma_f32_16x16x32_bf16 v[156:159], a[132:135], v[60:63], v[156:159]\n"
    "ds_read_b128 a[104:107], v74 offset:32768\n"
    "v_fma_f32 v13, v13, s96, v91\n"
    "v_mfma_f32_16x16x32_bf16 v[160:163], a[128:131], v[64:67], v[160:163]\n"
    "v_fma_f32 v14, v14, s96, v91\n"
    "v_fma_f32 v15, v15, s96, v91\n"
    "v_mfma_f32_16x16x32_bf16 v[160:163], a[132:135], v[68:71], v[160:163]\n"
    "buffer_load_dwordx4 v81, s[20:23], s45 offen offset:1024 nt lds\n"
    "v_mfma_f32_16x16x32_bf16 v[164:167], a[136:139], v[48:51], v[164:167]\n"
    "ds_read_b128 a[108:111], v75 offset:32768\n"
    "v_fma_f32 v16, v16, s96, v92\n"
    "v_mfma_f32_16x16x32_bf16 v[164:167], a[140:143], v[52:55], v[164:167]\n"
    "ds_read_b128 a[112:115], v72 offset:33024\n"
    "v_fma_f32 v17, v17, s96, v92\n"
    "v_mfma_f32_16x16x32_bf16 v[168:171], a[136:139], v[56:59], v[168:171]\n"
    "ds_read_b128 a[116:119], v73 offset:33024\n"
    "v_fma_f32 v18, v18, s96, v92\n"
    "v_mfma_f32_16x16x32_bf16 v[168:171], a[140:143], v[60:63], v[168:171]\n"
    "buffer_load_dwordx4 v82, s[24:27], s69 offen offset:2048 nt lds\n"
    "v_mfma_f32_16x16x32_bf16 v[172:175], a[136:139], v[64:67], v[172:175]\n"
    "ds_read_b128 a[120:123], v74 offset:33024\n"
    "v_fma_f32 v19, v19, s96, v92\n"
    "v_mfma_f32_16x16x32_bf16 v[172:175], a[140:143], v[68:71], v[172:175]\n"
    "v_fma_f32 v20, v20, s96, v93\n"
    "v_fma_f32 v21, v21, s96, v93\n"
    "v_mfma_f32_16x16x32_bf16 v[176:179], a[144:147], v[48:51], v[176:179]\n"
    "ds_read_b128 a[124:127], v75 offset:33024\n"
    "v_fma_f32 v22, v22, s96, v93\n"
    "v_mfma_f32_16x16x32_bf16 v[176:179], a[148:151], v[52:55], v[176:179]\n"
    "buffer_load_dwordx4 v83, s[24:27], s69 offen offset:3072 nt lds\n"
    "v_mfma_f32_16x16x32_bf16 v[180:183], a[144:147], v[56:59], v[180:183]\n"
    "v_fma_f32 v23, v23, s96, v93\n"
    "v_max3_f32 v97, v12, v13, v14\n"
    "v_mfma_f32_16x16x32_bf16 v[180:183], a[148:151], v[60:63], v[180:183]\n"
    "v_max3_f32 v97, v97, v15, v16\n"
    "v_max3_f32 v97, v97, v17, v18\n"
    "v_mfma_f32_16x16x32_bf16 v[184:187], a[144:147], v[64:67], v[184:187]\n"
    "v_max3_f32 v97, v97, v19, v20\n"
    "v_max3_f32 v97, v97, v21, v22\n"
    "v_mfma_f32_16x16x32_bf16 v[184:187], a[148:151], v[68:71], v[184:187]\n"
    "v_max_f32 v97, v97, v23\n"
    "v_cmp_lt_f32 vcc, s97, v97\n"
    "v_mfma_f32_16x16x32_bf16 a[188:191], a[152:155], v[48:51], a[188:191]\n"
    "v_mfma_f32_16x16x32_bf16 a[188:191], a[156:159], v[52:55], a[188:191]\n"
    "v_mfma_f32_16x16x32_bf16 a[192:195], a[152:155], v[56:59], a[192:195]\n"
    "ds_read_b64_tr_b16 a[128:129], v76 offset:32768\n"
    "ds_read_b64_tr_b16 a[130:131], v76 offset:36864\n"
    "v_mfma_f32_16x16x32_bf16 a[192:195], a[156:159], v[60:63], a[192:195]\n"
    "s_cbranch_vccnz STUBb1c0_%=\n"
    "RETb1c0_%=:\n"
    "v_mfma_f32_16x16x32_bf16 a[196:199], a[152:155], v[64:67], a[196:199]\n"
    "ds_read_b64_tr_b16 a[132:133], v76 offset:40960\n"
    "ds_read_b64_tr_b16 a[134:135], v76 offset:45056\n"
    "v_mfma_f32_16x16x32_bf16 a[196:199], a[156:159], v[68:71], a[196:199]\n"
    "v_exp_f32 v12, v12\n"
    "v_pk_mul_f32 v[152:153], v[152:153], v[94:95] op_sel_hi:[1,0]\n"
    "v_pk_mul_f32 v[154:155], v[154:155], v[94:95] op_sel_hi:[1,0]\n"
    "v_cvt_pk_bf16_f32 v152, v152, v153\n"
    "v_cvt_pk_bf16_f32 v153, v154, v155\n"
    "v_pk_mul_f32 v[156:157], v[156:157], v[94:95] op_sel:[0,1] op_sel_hi:[1,1]\n"
    "v_pk_mul_f32 v[158:159], v[158:159], v[94:95] op_sel:[0,1] op_sel_hi:[1,1]\n"
    "v_cvt_pk_bf16_f32 v156, v156, v157\n"
    "v_cvt_pk_bf16_f32 v157, v158, v159\n"
    "v_pk_mul_f32 v[160:161], v[160:161], v[96:97] op_sel_hi:[1,0]\n"
    "v_pk_mul_f32 v[162:163], v[162:163], v[96:97] op_sel_hi:[1,0]\n"
    "v_cvt_pk_bf16_f32 v160, v160, v161\n"
    "v_cvt_pk_bf16_f32 v161, v162, v163\n"
    "ds_write_b64 v112, v[152:153] offset:128\n"
    "ds_write_b64 v112, v[156:157] offset:4224\n"
    "ds_write_b64 v112, v[160:161] offset:8320\n"
    "v_pk_mul_f32 v[164:165], v[164:165], v[94:95] op_sel_hi:[1,0]\n"
    "v_pk_mul_f32 v[166:167], v[166:167], v[94:95] op_sel_hi:[1,0]\n"
    "v_cvt_pk_bf16_f32 v164, v164, v165\n"
    "v_cvt_pk_bf16_f32 v165, v166, v167\n"
    "v_pk_mul_f32 v[168:169], v[168:169], v[94:95] op_sel:[0,1] op_sel_hi:[1,1]\n"
    "v_pk_mul_f32 v[170:171], v[170:171], v[94:95] op_sel:[0,1] op_sel_hi:[1,1]\n"
    "v_cvt_pk_bf16_f32 v168, v168, v169\n"
    "v_cvt_pk_bf16_f32 v169, v170, v171\n"
    "v_pk_mul_f32 v[172:173], v[172:173], v[96:97] op_sel_hi:[1,0]\n"
    "v_pk_mul_f32 v[174:175], v[174:175], v[96:97] op_sel_hi:[1,0]\n"
    "v_cvt_pk_bf16_f32 v172, v172, v173\n"
    "v_cvt_pk_bf16_f32 v173, v174, v175\n"
    "ds_write_b64 v113, v[164:165] offset:128\n"
    "ds_write_b64 v113, v[168:169] offset:4224\n"
    "ds_write_b64 v113, v[172:173] offset:8320\n"
    "v_pk_mul_f32 v[176:177], v[176:177], v[94:95] op_sel_hi:[1,0]\n"
    "v_pk_mul_f32 v[178:179], v[178:179], v[94:95] op_sel_hi:[1,0]\n"
    "v_cvt_pk_bf16_f32 v176, v176, v177\n"
    "v_cvt_pk_bf16_f32 v177, v178, v179\n"
    "v_pk_mul_f32 v[180:181], v[180:181], v[94:95] op_sel:[0,1] op_sel_hi:[1,1]\n"
    "v_pk_mul_f32 v[182:183], v[182:183], v[94:95] op_sel:[0,1] op_sel_hi:[1,1]\n"
    "v_cvt_pk_bf16_f32 v180, v180, v181\n"
    "v_cvt_pk_bf16_f32 v181, v182, v183\n"
    "v_pk_mul_f32 v[184:185], v[184:185], v[96:97] op_sel_hi:[1,0]\n"
    "v_pk_mul_f32 v[186:187], v[186:187], v[96:97] op_sel_hi:[1,0]\n"
    "v_cvt_pk_bf16_f32 v184, v184, v185\n"
    "v_cvt_pk_bf16_f32 v185, v186, v187\n"
    "ds_write_b64 v114, v[176:177] offset:128\n"
    "ds_write_b64 v114, v[180:181] offset:4224\n"
    "ds_write_b64 v114, v[184:185] offset:8320\n"
    "v_accvgpr_read_b32 v0, a188\n"
    "v_accvgpr_read_b32 v1, a189\n"
    "v_accvgpr_read_b32 v2, a190\n"
    "v_accvgpr_read_b32 v3, a191\n"
    "v_pk_mul_f32 v[0:1], v[0:1], v[94:95] op_sel_hi:[1,0]\n"
    "v_pk_mul_f32 v[2:3], v[2:3], v[94:95] op_sel_hi:[1,0]\n"
    "v_cvt_pk_bf16_f32 v0, v0, v1\n"
    "v_cvt_pk_bf16_f32 v1, v2, v3\n"
    "v_accvgpr_read_b32 v4, a192\n"
    "v_accvgpr_read_b32 v5, a193\n"
    "v_accvgpr_read_b32 v6, a194\n"
    "v_accvgpr_read_b32 v7, a195\n"
    "v_pk_mul_f32 v[4:5], v[4:5], v[94:95] op_sel:[0,1] op_sel_hi:[1,1]\n"
    "v_pk_mul_f32 v[6:7], v[6:7], v[94:95] op_sel:[0,1] op_sel_hi:[1,1]\n"
    "v_cvt_pk_bf16_f32 v4, v4, v5\n"
    "v_cvt_pk_bf16_f32 v5, v6, v7\n"
    "v_accvgpr_read_b32 v8, a196\n"
    "v_accvgpr_read_b32 v9, a197\n"
    "v_accvgpr_read_b32 v10, a198\n"
    "v_accvgpr_read_b32 v11, a199\n"
    "v_pk_mul_f32 v[8:9], v[8:9], v[96:97] op_sel_hi:[1,0]\n"
    "v_pk_mul_f32 v[10:11], v[10:11], v[96:97] op_sel_hi:[1,0]\n"
    "v_cvt_pk_bf16_f32 v8, v8, v9\n"
    "v_cvt_pk_bf16_f32 v9, v10, v11\n"
    "ds_write_b64 v115, v[0:1] offset:128\n"
    "ds_write_b64 v115, v[4:5] offset:4224\n"
    "ds_write_b64 v115, v[8:9] offset:8320\n"
    "s_waitcnt lgkmcnt(15)\n"
    "v_mfma_f32_16x16x32_bf16 v[0:3], a[96:99], a[0:3], 0\n"
    "ds_read_b128  a[228:231], v88 offset:0\n"
    "ds_read_b64_tr_b16 a[136:137], v77 offset:32768\n"
    "v_mfma_f32_16x16x32_bf16 v[0:3], a[100:103], a[4:7], v[0:3]\n"
    "v_exp_f32 v13, v13\n"
    "v_mfma_f32_16x16x32_bf16 v[0:3], a[104:107], a[8:11], v[0:3]\n"
    "ds_read_b64_tr_b16 a[138:139], v77 offset:36864\n"
    "v_cvt_pk_bf16_f32 v26, v12, v13\n"
    "v_mfma_f32_16x16x32_bf16 v[0:3], a[108:111], a[12:15], v[0:3]\n"
    "ds_read_b64_tr_b16 a[140:141], v77 offset:40960\n"
    "ds_read_b64_tr_b16 a[142:143], v77 offset:45056\n"
    "v_mfma_f32_16x16x32_bf16 v[0:3], a[112:115], a[16:19], v[0:3]\n"
    "v_exp_f32 v14, v14\n"
    "v_mfma_f32_16x16x32_bf16 v[0:3], a[116:119], a[20:23], v[0:3]\n"
    "v_exp_f32 v15, v15\n"
    "v_mfma_f32_16x16x32_bf16 v[0:3], a[120:123], a[24:27], v[0:3]\n"
    "ds_read_b64_tr_b16 a[144:145], v78 offset:32768\n"
    "v_cvt_pk_bf16_f32 v27, v14, v15\n"
    "v_mfma_f32_16x16x32_bf16 v[0:3], a[124:127], a[28:31], v[0:3]\n"
    "ds_read_b64_tr_b16 a[146:147], v78 offset:36864\n"
    "ds_read_b64_tr_b16 a[148:149], v78 offset:40960\n"
    "v_mfma_f32_16x16x32_bf16 v[4:7], a[96:99], a[32:35], 0\n"
    "v_exp_f32 v16, v16\n"
    "v_mfma_f32_16x16x32_bf16 v[4:7], a[100:103], a[36:39], v[4:7]\n"
    "s_waitcnt lgkmcnt(7)\n"
    "buffer_store_dwordx4 a[228:231], v89, s[36:39], s40 offen nt\n"
    "v_mfma_f32_16x16x32_bf16 v[4:7], a[104:107], a[40:43], v[4:7]\n"
    "ds_read_b64_tr_b16 a[150:151], v78 offset:45056\n"
    "ds_read_b64_tr_b16 a[152:153], v79 offset:32768\n"
    "v_mfma_f32_16x16x32_bf16 v[4:7], a[108:111], a[44:47], v[4:7]\n"
    "s_add_u32 m0, s35, 0\n"
    "v_mfma_f32_16x16x32_bf16 v[4:7], a[112:115], a[48:51], v[4:7]\n"
    "ds_read_b64_tr_b16 a[154:155], v79 offset:36864\n"
    "ds_read_b64_tr_b16 a[156:157], v79 offset:40960\n"
    "v_mfma_f32_16x16x32_bf16 v[4:7], a[116:119], a[52:55], v[4:7]\n"
    "buffer_load_dwordx4 v84, s[28:31], s40 offen nt lds\n"
    "v_mfma_f32_16x16x32_bf16 v[4:7], a[120:123], a[56:59], v[4:7]\n"
    "ds_read_b64_tr_b16 a[158:159], v79 offset:45056\n"
    "v_mfma_f32_16x16x32_bf16 v[4:7], a[124:127], a[60:63], v[4:7]\n"
    "v_exp_f32 v17, v17\n"
    "v_mfma_f32_16x16x32_bf16 v[8:11], a[96:99], a[64:67], 0\n"
    "v_exp_f32 v18, v18\n"
    "v_mfma_f32_16x16x32_bf16 v[8:11], a[100:103], a[68:71], v[8:11]\n"
    "v_exp_f32 v19, v19\n"
    "v_mfma_f32_16x16x32_bf16 v[8:11], a[104:107], a[72:75], v[8:11]\n"
    "v_exp_f32 v20, v20\n"
    "v_mfma_f32_16x16x32_bf16 v[8:11], a[108:111], a[76:79], v[8:11]\n"
    "v_exp_f32 v21, v21\n"
    "v_mfma_f32_16x16x32_bf16 v[8:11], a[112:115], a[80:83], v[8:11]\n"
    "v_exp_f32 v22, v22\n"
    "v_mfma_f32_16x16x32_bf16 v[8:11], a[116:119], a[84:87], v[8:11]\n"
    "v_exp_f32 v23, v23\n"
    "v_mfma_f32_16x16x32_bf16 v[8:11], a[120:123], a[88:91], v[8:11]\n"
    "v_cvt_pk_bf16_f32 v34, v16, v17\n"
    "v_cvt_pk_bf16_f32 v35, v18, v19\n"
    "v_mfma_f32_16x16x32_bf16 v[8:11], a[124:127], a[92:95], v[8:11]\n"
    "v_cvt_pk_bf16_f32 v42, v20, v21\n"
    "v_cvt_pk_bf16_f32 v43, v22, v23\n"
    "s_waitcnt vmcnt(10) lgkmcnt(0)\n"
    "s_barrier\n"
    "v_mfma_f32_16x16x32_bf16 v[188:191], a[128:131], v[48:51], v[188:191]\n"
    "ds_read_b128 a[96:99], v72 offset:49152\n"
    "s_add_u32 m0, s91, 32768\n"
    "v_mfma_f32_16x16x32_bf16 v[188:191], a[132:135], v[52:55], v[188:191]\n"
    "buffer_load_dwordx4 v80, s[20:23], s46 offen lds\n"
    "v_mfma_f32_16x16x32_bf16 v[192:195], a[128:131], v[56:59], v[192:195]\n"
    "ds_read_b128 a[100:103], v73 offset:49152\n"
    "v_fma_f32 v0, v0, s96, v91\n"
    "v_mfma_f32_16x16x32_bf16 v[192:195], a[132:135], v[60:63], v[192:195]\n"
    "ds_read_b128 a[104:107], v74 offset:49152\n"
    "v_fma_f32 v1, v1, s96, v91\n"
    "v_mfma_f32_16x16x32_bf16 v[196:199], a[128:131], v[64:67], v[196:199]\n"
    "v_fma_f32 v2, v2, s96, v91\n"
    "v_fma_f32 v3, v3, s96, v91\n"
    "v_mfma_f32_16x16x32_bf16 v[196:199], a[132:135], v[68:71], v[196:199]\n"
    "buffer_load_dwordx4 v81, s[20:23], s46 offen offset:1024 nt lds\n"
    "v_mfma_f32_16x16x32_bf16 v[200:203], a[136:139], v[48:51], v[200:203]\n"
    "ds_read_b128 a[108:111], v75 offset:49152\n"
    "v_fma_f32 v4, v4, s96, v92\n"
    "v_mfma_f32_16x16x32_bf16 v[200:203], a[140:143], v[52:55], v[200:203]\n"
    "ds_read_b128  a[228:231], v90 offset:1024\n"
    "v_fma_f32 v5, v5, s96, v92\n"
    "v_mfma_f32_16x16x32_bf16 v[204:207], a[136:139], v[56:59], v[204:207]\n"
    "ds_read_b128 a[112:115], v72 offset:49408\n"
    "v_fma_f32 v6, v6, s96, v92\n"
    "v_mfma_f32_16x16x32_bf16 v[204:207], a[140:143], v[60:63], v[204:207]\n"
    "buffer_load_dwordx4 v82, s[24:27], s70 offen offset:2048 nt lds\n"
    "v_mfma_f32_16x16x32_bf16 v[208:211], a[136:139], v[64:67], v[208:211]\n"
    "ds_read_b128  a[232:235], v88 offset:2048\n"
    "ds_read_b128 a[116:119], v73 offset:49408\n"
    "v_mfma_f32_16x16x32_bf16 v[208:211], a[140:143], v[68:71], v[208:211]\n"
    "ds_read_b128 a[120:123], v74 offset:49408\n"
    "v_fma_f32 v7, v7, s96, v92\n"
    "v_mfma_f32_16x16x32_bf16 v[212:215], a[144:147], v[48:51], v[212:215]\n"
    "ds_read_b128 a[124:127], v75 offset:49408\n"
    "v_fma_f32 v8, v8, s96, v93\n"
    "v_mfma_f32_16x16x32_bf16 v[212:215], a[148:151], v[52:55], v[212:215]\n"
    "buffer_load_dwordx4 v83, s[24:27], s70 offen offset:3072 nt lds\n"
    "v_mfma_f32_16x16x32_bf16 v[216:219], a[144:147], v[56:59], v[216:219]\n"
    "v_fma_f32 v9, v9, s96, v93\n"
    "v_fma_f32 v10, v10, s96, v93\n"
    "v_mfma_f32_16x16x32_bf16 v[216:219], a[148:151], v[60:63], v[216:219]\n"
    "v_fma_f32 v11, v11, s96, v93\n"
    "v_max3_f32 v97, v0, v1, v2\n"
    "v_mfma_f32_16x16x32_bf16 v[220:223], a[144:147], v[64:67], v[220:223]\n"
    "v_max3_f32 v97, v97, v3, v4\n"
    "v_max3_f32 v97, v97, v5, v6\n"
    "v_mfma_f32_16x16x32_bf16 v[220:223], a[148:151], v[68:71], v[220:223]\n"
    "s_waitcnt lgkmcnt(5)\n"
    "buffer_store_dwordx4 a[228:231], v89, s[36:39], s40 offen offset:2048 nt\n"
    "v_mfma_f32_16x16x32_bf16 a[200:203], a[152:155], v[48:51], a[200:203]\n"
    "v_max3_f32 v97, v97, v7, v8\n"
    "v_max3_f32 v97, v97, v9, v10\n"
    "v_mfma_f32_16x16x32_bf16 a[200:203], a[156:159], v[52:55], a[200:203]\n"
    "v_max_f32 v97, v97, v11\n"
    "s_add_u32 m0, s35, 0\n"
    "v_mfma_f32_16x16x32_bf16 a[204:207], a[152:155], v[56:59], a[204:207]\n"
    "ds_read_b64_tr_b16 a[128:129], v76 offset:49152\n"
    "v_cmp_lt_f32 vcc, s97, v97\n"
    "v_mfma_f32_16x16x32_bf16 a[204:207], a[156:159], v[60:63], a[204:207]\n"
    "buffer_load_dwordx4 v85, s[28:31], s40 offen offset:1024 nt lds\n"
    "v_mfma_f32_16x16x32_bf16 a[208:211], a[152:155], v[64:67], a[208:211]\n"
    "ds_read_b64_tr_b16 a[130:131], v76 offset:53248\n"
    "ds_read_b64_tr_b16 a[132:133], v76 offset:57344\n"
    "v_mfma_f32_16x16x32_bf16 a[208:211], a[156:159], v[68:71], a[208:211]\n"
    "ds_read_b64_tr_b16 a[134:135], v76 offset:61440\n"
    "s_cbranch_vccnz STUBb2c0_%=\n"
    "RETb2c0_%=:\n"
    "v_pk_mul_f32 v[188:189], v[188:189], v[94:95] op_sel_hi:[1,0]\n"
    "v_pk_mul_f32 v[190:191], v[190:191], v[94:95] op_sel_hi:[1,0]\n"
    "v_cvt_pk_bf16_f32 v188, v188, v189\n"
    "v_cvt_pk_bf16_f32 v189, v190, v191\n"
    "v_pk_mul_f32 v[192:193], v[192:193], v[94:95] op_sel:[0,1] op_sel_hi:[1,1]\n"
    "v_pk_mul_f32 v[194:195], v[194:195], v[94:95] op_sel:[0,1] op_sel_hi:[1,1]\n"
    "v_cvt_pk_bf16_f32 v192, v192, v193\n"
    "v_cvt_pk_bf16_f32 v193, v194, v195\n"
    "v_pk_mul_f32 v[196:197], v[196:197], v[96:97] op_sel_hi:[1,0]\n"
    "v_pk_mul_f32 v[198:199], v[198:199], v[96:97] op_sel_hi:[1,0]\n"
    "v_cvt_pk_bf16_f32 v196, v196, v197\n"
    "v_cvt_pk_bf16_f32 v197, v198, v199\n"
    "ds_write_b64 v112, v[188:189] offset:12288\n"
    "ds_write_b64 v112, v[192:193] offset:16384\n"
    "ds_write_b64 v112, v[196:197] offset:20480\n"
    "v_pk_mul_f32 v[200:201], v[200:201], v[94:95] op_sel_hi:[1,0]\n"
    "v_pk_mul_f32 v[202:203], v[202:203], v[94:95] op_sel_hi:[1,0]\n"
    "v_cvt_pk_bf16_f32 v200, v200, v201\n"
    "v_cvt_pk_bf16_f32 v201, v202, v203\n"
    "v_pk_mul_f32 v[204:205], v[204:205], v[94:95] op_sel:[0,1] op_sel_hi:[1,1]\n"
    "v_pk_mul_f32 v[206:207], v[206:207], v[94:95] op_sel:[0,1] op_sel_hi:[1,1]\n"
    "v_cvt_pk_bf16_f32 v204, v204, v205\n"
    "v_cvt_pk_bf16_f32 v205, v206, v207\n"
    "v_pk_mul_f32 v[208:209], v[208:209], v[96:97] op_sel_hi:[1,0]\n"
    "v_pk_mul_f32 v[210:211], v[210:211], v[96:97] op_sel_hi:[1,0]\n"
    "v_cvt_pk_bf16_f32 v208, v208, v209\n"
    "v_cvt_pk_bf16_f32 v209, v210, v211\n"
    "ds_write_b64 v113, v[200:201] offset:12288\n"
    "ds_write_b64 v113, v[204:205] offset:16384\n"
    "ds_write_b64 v113, v[208:209] offset:20480\n"
    "v_pk_mul_f32 v[212:213], v[212:213], v[94:95] op_sel_hi:[1,0]\n"
    "v_pk_mul_f32 v[214:215], v[214:215], v[94:95] op_sel_hi:[1,0]\n"
    "v_cvt_pk_bf16_f32 v212, v212, v213\n"
    "v_cvt_pk_bf16_f32 v213, v214, v215\n"
    "v_pk_mul_f32 v[216:217], v[216:217], v[94:95] op_sel:[0,1] op_sel_hi:[1,1]\n"
    "v_pk_mul_f32 v[218:219], v[218:219], v[94:95] op_sel:[0,1] op_sel_hi:[1,1]\n"
    "v_cvt_pk_bf16_f32 v216, v216, v217\n"
    "v_cvt_pk_bf16_f32 v217, v218, v219\n"
    "v_pk_mul_f32 v[220:221], v[220:221], v[96:97] op_sel_hi:[1,0]\n"
    "v_pk_mul_f32 v[222:223], v[222:223], v[96:97] op_sel_hi:[1,0]\n"
    "v_cvt_pk_bf16_f32 v220, v220, v221\n"
    "v_cvt_pk_bf16_f32 v221, v222, v223\n"
    "ds_write_b64 v114, v[212:213] offset:12288\n"
    "ds_write_b64 v114, v[216:217] offset:16384\n"
    "ds_write_b64 v114, v[220:221] offset:20480\n"
    "v_accvgpr_read_b32 v12, a200\n"
    "v_accvgpr_read_b32 v13, a201\n"
    "v_accvgpr_read_b32 v14, a202\n"
    "v_accvgpr_read_b32 v15, a203\n"
    "v_pk_mul_f32 v[12:13], v[12:13], v[94:95] op_sel_hi:[1,0]\n"
    "v_pk_mul_f32 v[14:15], v[14:15], v[94:95] op_sel_hi:[1,0]\n"
    "v_cvt_pk_bf16_f32 v12, v12, v13\n"
    "v_cvt_pk_bf16_f32 v13, v14, v15\n"
    "v_accvgpr_read_b32 v16, a204\n"
    "v_accvgpr_read_b32 v17, a205\n"
    "v_accvgpr_read_b32 v18, a206\n"
    "v_accvgpr_read_b32 v19, a207\n"
    "v_pk_mul_f32 v[16:17], v[16:17], v[94:95] op_sel:[0,1] op_sel_hi:[1,1]\n"
    "v_pk_mul_f32 v[18:19], v[18:19], v[94:95] op_sel:[0,1] op_sel_hi:[1,1]\n"
    "v_cvt_pk_bf16_f32 v16, v16, v17\n"
    "v_cvt_pk_bf16_f32 v17, v18, v19\n"
    "v_accvgpr_read_b32 v20, a208\n"
    "v_accvgpr_read_b32 v21, a209\n"
    "v_accvgpr_read_b32 v22, a210\n"
    "v_accvgpr_read_b32 v23, a211\n"
    "v_pk_mul_f32 v[20:21], v[20:21], v[96:97] op_sel_hi:[1,0]\n"
    "v_pk_mul_f32 v[22:23], v[22:23], v[96:97] op_sel_hi:[1,0]\n"
    "v_cvt_pk_bf16_f32 v20, v20, v21\n"
    "v_cvt_pk_bf16_f32 v21, v22, v23\n"
    "ds_write_b64 v115, v[12:13] offset:12288\n"
    "ds_write_b64 v115, v[16:17] offset:16384\n"
    "ds_write_b64 v115, v[20:21] offset:20480\n"
    "s_waitcnt lgkmcnt(15)\n"
    "v_mfma_f32_16x16x32_bf16 v[12:15], a[96:99], a[0:3], 0\n"
    "v_exp_f32 v0, v0\n"
    "v_mfma_f32_16x16x32_bf16 v[12:15], a[100:103], a[4:7], v[12:15]\n"
    "buffer_store_dwordx4 a[232:235], v89, s[36:39], s88 offen nt\n"
    "v_mfma_f32_16x16x32_bf16 v[12:15], a[104:107], a[8:11], v[12:15]\n"
    "ds_read_b64_tr_b16 a[136:137], v77 offset:49152\n"
    "ds_read_b64_tr_b16 a[138:139], v77 offset:53248\n"
    "v_mfma_f32_16x16x32_bf16 v[12:15], a[108:111], a[12:15], v[12:15]\n"
    "ds_read_b64_tr_b16 a[140:141], v77 offset:57344\n"
    "ds_read_b64_tr_b16 a[142:143], v77 offset:61440\n"
    "v_mfma_f32_16x16x32_bf16 v[12:15], a[112:115], a[16:19], v[12:15]\n"
    "v_exp_f32 v1, v1\n"
    "v_mfma_f32_16x16x32_bf16 v[12:15], a[116:119], a[20:23], v[12:15]\n"
    "buffer_load_dwordx4 v86, s[28:31], s40 offen offset:2048 nt lds\n"
    "v_mfma_f32_16x16x32_bf16 v[12:15], a[120:123], a[24:27], v[12:15]\n"
    "ds_read_b64_tr_b16 a[144:145], v78 offset:49152\n"
    "v_cvt_pk_bf16_f32 v28, v0, v1\n"
    "v_mfma_f32_16x16x32_bf16 v[12:15], a[124:127], a[28:31], v[12:15]\n"
    "ds_read_b64_tr_b16 a[146:147], v78 offset:53248\n"
    "ds_read_b64_tr_b16 a[148:149], v78 offset:57344\n"
    "v_mfma_f32_16x16x32_bf16 v[16:19], a[96:99], a[32:35], 0\n"
    "v_exp_f32 v2, v2\n"
    "v_mfma_f32_16x16x32_bf16 v[16:19], a[100:103], a[36:39], v[16:19]\n"
    "ds_read_b64_tr_b16 a[150:151], v78 offset:61440\n"
    "v_mfma_f32_16x16x32_bf16 v[16:19], a[104:107], a[40:43], v[16:19]\n"
    "v_exp_f32 v3, v3\n"
    "v_mfma_f32_16x16x32_bf16 v[16:19], a[108:111], a[44:47], v[16:19]\n"
    "ds_read_b64_tr_b16 a[152:153], v79 offset:49152\n"
    "v_cvt_pk_bf16_f32 v29, v2, v3\n"
    "v_mfma_f32_16x16x32_bf16 v[16:19], a[112:115], a[48:51], v[16:19]\n"
    "ds_read_b64_tr_b16 a[154:155], v79 offset:53248\n"
    "ds_read_b64_tr_b16 a[156:157], v79 offset:57344\n"
    "v_mfma_f32_16x16x32_bf16 v[16:19], a[116:119], a[52:55], v[16:19]\n"
    "v_exp_f32 v4, v4\n"
    "v_mfma_f32_16x16x32_bf16 v[16:19], a[120:123], a[56:59], v[16:19]\n"
    "ds_read_b64_tr_b16 a[158:159], v79 offset:61440\n"
    "v_mfma_f32_16x16x32_bf16 v[16:19], a[124:127], a[60:63], v[16:19]\n"
    "v_exp_f32 v5, v5\n"
    "v_mfma_f32_16x16x32_bf16 v[20:23], a[96:99], a[64:67], 0\n"
    "v_exp_f32 v6, v6\n"
    "v_mfma_f32_16x16x32_bf16 v[20:23], a[100:103], a[68:71], v[20:23]\n"
    "v_exp_f32 v7, v7\n"
    "v_mfma_f32_16x16x32_bf16 v[20:23], a[104:107], a[72:75], v[20:23]\n"
    "v_exp_f32 v8, v8\n"
    "v_mfma_f32_16x16x32_bf16 v[20:23], a[108:111], a[76:79], v[20:23]\n"
    "v_exp_f32 v9, v9\n"
    "v_mfma_f32_16x16x32_bf16 v[20:23], a[112:115], a[80:83], v[20:23]\n"
    "v_exp_f32 v10, v10\n"
    "v_mfma_f32_16x16x32_bf16 v[20:23], a[116:119], a[84:87], v[20:23]\n"
    "v_exp_f32 v11, v11\n"
    "v_mfma_f32_16x16x32_bf16 v[20:23], a[120:123], a[88:91], v[20:23]\n"
    "v_cvt_pk_bf16_f32 v36, v4, v5\n"
    "v_cvt_pk_bf16_f32 v37, v6, v7\n"
    "v_mfma_f32_16x16x32_bf16 v[20:23], a[124:127], a[92:95], v[20:23]\n"
    "v_cvt_pk_bf16_f32 v44, v8, v9\n"
    "v_cvt_pk_bf16_f32 v45, v10, v11\n"
    "s_waitcnt vmcnt(14) lgkmcnt(0)\n"
    "s_barrier\n"
    "v_mfma_f32_16x16x32_bf16 v[224:227], a[128:131], v[48:51], v[224:227]\n"
    "ds_read_b128 a[96:99], v72 offset:0\n"
    "s_add_u32 m0, s91, 49152\n"
    "v_mfma_f32_16x16x32_bf16 v[224:227], a[132:135], v[52:55], v[224:227]\n"
    "buffer_load_dwordx4 v80, s[20:23], s47 offen lds\n"
    "v_mfma_f32_16x16x32_bf16 v[228:231], a[128:131], v[56:59], v[228:231]\n"
    "ds_read_b128 a[100:103], v73 offset:0\n"
    "v_fma_f32 v12, v12, s96, v91\n"
    "v_mfma_f32_16x16x32_bf16 v[228:231], a[132:135], v[60:63], v[228:231]\n"
    "ds_read_b128 a[104:107], v74 offset:0\n"
    "v_fma_f32 v13, v13, s96, v91\n"
    "v_mfma_f32_16x16x32_bf16 v[232:235], a[128:131], v[64:67], v[232:235]\n"
    "v_fma_f32 v14, v14, s96, v91\n"
    "v_fma_f32 v15, v15, s96, v91\n"
    "v_mfma_f32_16x16x32_bf16 v[232:235], a[132:135], v[68:71], v[232:235]\n"
    "buffer_load_dwordx4 v81, s[20:23], s47 offen offset:1024 nt lds\n"
    "v_mfma_f32_16x16x32_bf16 v[236:239], a[136:139], v[48:51], v[236:239]\n"
    "ds_read_b128 a[108:111], v75 offset:0\n"
    "v_fma_f32 v16, v16, s96, v92\n"
    "v_mfma_f32_16x16x32_bf16 v[236:239], a[140:143], v[52:55], v[236:239]\n"
    "ds_read_b128  a[228:231], v90 offset:3072\n"
    "v_fma_f32 v17, v17, s96, v92\n"
    "v_mfma_f32_16x16x32_bf16 v[240:243], a[136:139], v[56:59], v[240:243]\n"
    "ds_read_b128 a[112:115], v72 offset:256\n"
    "v_fma_f32 v18, v18, s96, v92\n"
    "v_mfma_f32_16x16x32_bf16 v[240:243], a[140:143], v[60:63], v[240:243]\n"
    "buffer_load_dwordx4 v82, s[24:27], s71 offen offset:2048 nt lds\n"
    "v_mfma_f32_16x16x32_bf16 v[244:247], a[136:139], v[64:67], v[244:247]\n"
    "ds_read_b128  a[232:235], v88 offset:4096\n"
    "ds_read_b128 a[116:119], v73 offset:256\n"
    "v_mfma_f32_16x16x32_bf16 v[244:247], a[140:143], v[68:71], v[244:247]\n"
    "ds_read_b128 a[120:123], v74 offset:256\n"
    "v_fma_f32 v19, v19, s96, v92\n"
    "v_mfma_f32_16x16x32_bf16 v[248:251], a[144:147], v[48:51], v[248:251]\n"
    "ds_read_b128 a[124:127], v75 offset:256\n"
    "v_fma_f32 v20, v20, s96, v93\n"
    "v_mfma_f32_16x16x32_bf16 v[248:251], a[148:151], v[52:55], v[248:251]\n"
    "buffer_load_dwordx4 v83, s[24:27], s71 offen offset:3072 nt lds\n"
    "v_mfma_f32_16x16x32_bf16 v[252:255], a[144:147], v[56:59], v[252:255]\n"
    "v_fma_f32 v21, v21, s96, v93\n"
    "v_fma_f32 v22, v22, s96, v93\n"
    "v_mfma_f32_16x16x32_bf16 v[252:255], a[148:151], v[60:63], v[252:255]\n"
    "v_fma_f32 v23, v23, s96, v93\n"
    "v_max3_f32 v97, v12, v13, v14\n"
    "v_mfma_f32_16x16x32_bf16 a[224:227], a[144:147], v[64:67], a[224:227]\n"
    "v_max3_f32 v97, v97, v15, v16\n"
    "v_max3_f32 v97, v97, v17, v18\n"
    "v_mfma_f32_16x16x32_bf16 a[224:227], a[148:151], v[68:71], a[224:227]\n"
    "s_waitcnt lgkmcnt(5)\n"
    "buffer_store_dwordx4 a[228:231], v89, s[36:39], s88 offen offset:2048 nt\n"
    "v_mfma_f32_16x16x32_bf16 a[212:215], a[152:155], v[48:51], a[212:215]\n"
    "v_max3_f32 v97, v97, v19, v20\n"
    "v_max3_f32 v97, v97, v21, v22\n"
    "v_mfma_f32_16x16x32_bf16 a[212:215], a[156:159], v[52:55], a[212:215]\n"
    "v_max_f32 v97, v97, v23\n"
    "s_add_u32 m0, s35, 0\n"
    "v_mfma_f32_16x16x32_bf16 a[216:219], a[152:155], v[56:59], a[216:219]\n"
    "ds_read_b64_tr_b16 a[128:129], v76 offset:0\n"
    "v_cmp_lt_f32 vcc, s97, v97\n"
    "v_mfma_f32_16x16x32_bf16 a[216:219], a[156:159], v[60:63], a[216:219]\n"
    "buffer_load_dwordx4 v87, s[28:31], s40 offen offset:3072 nt lds\n"
    "v_mfma_f32_16x16x32_bf16 a[220:223], a[152:155], v[64:67], a[220:223]\n"
    "ds_read_b64_tr_b16 a[130:131], v76 offset:4096\n"
    "ds_read_b64_tr_b16 a[132:133], v76 offset:8192\n"
    "v_mfma_f32_16x16x32_bf16 a[220:223], a[156:159], v[68:71], a[220:223]\n"
    "ds_read_b64_tr_b16 a[134:135], v76 offset:12288\n"
    "s_cbranch_vccnz STUBb3c0_%=\n"
    "RETb3c0_%=:\n"
    "v_pk_mul_f32 v[224:225], v[224:225], v[94:95] op_sel_hi:[1,0]\n"
    "v_pk_mul_f32 v[226:227], v[226:227], v[94:95] op_sel_hi:[1,0]\n"
    "v_cvt_pk_bf16_f32 v224, v224, v225\n"
    "v_cvt_pk_bf16_f32 v225, v226, v227\n"
    "v_pk_mul_f32 v[228:229], v[228:229], v[94:95] op_sel:[0,1] op_sel_hi:[1,1]\n"
    "v_pk_mul_f32 v[230:231], v[230:231], v[94:95] op_sel:[0,1] op_sel_hi:[1,1]\n"
    "v_cvt_pk_bf16_f32 v228, v228, v229\n"
    "v_cvt_pk_bf16_f32 v229, v230, v231\n"
    "v_pk_mul_f32 v[232:233], v[232:233], v[96:97] op_sel_hi:[1,0]\n"
    "v_pk_mul_f32 v[234:235], v[234:235], v[96:97] op_sel_hi:[1,0]\n"
    "v_cvt_pk_bf16_f32 v232, v232, v233\n"
    "v_cvt_pk_bf16_f32 v233, v234, v235\n"
    "ds_write_b64 v112, v[224:225] offset:12416\n"
    "ds_write_b64 v112, v[228:229] offset:16512\n"
    "ds_write_b64 v112, v[232:233] offset:20608\n"
    "v_pk_mul_f32 v[236:237], v[236:237], v[94:95] op_sel_hi:[1,0]\n"
    "v_pk_mul_f32 v[238:239], v[238:239], v[94:95] op_sel_hi:[1,0]\n"
    "v_cvt_pk_bf16_f32 v236, v236, v237\n"
    "v_cvt_pk_bf16_f32 v237, v238, v239\n"
    "v_pk_mul_f32 v[240:241], v[240:241], v[94:95] op_sel:[0,1] op_sel_hi:[1,1]\n"
    "v_pk_mul_f32 v[242:243], v[242:243], v[94:95] op_sel:[0,1] op_sel_hi:[1,1]\n"
    "v_cvt_pk_bf16_f32 v240, v240, v241\n"
    "v_cvt_pk_bf16_f32 v241, v242, v243\n"
    "v_pk_mul_f32 v[244:245], v[244:245], v[96:97] op_sel_hi:[1,0]\n"
    "v_pk_mul_f32 v[246:247], v[246:247], v[96:97] op_sel_hi:[1,0]\n"
    "v_cvt_pk_bf16_f32 v244, v244, v245\n"
    "v_cvt_pk_bf16_f32 v245, v246, v247\n"
    "ds_write_b64 v113, v[236:237] offset:12416\n"
    "ds_write_b64 v113, v[240:241] offset:16512\n"
    "ds_write_b64 v113, v[244:245] offset:20608\n"
    "v_pk_mul_f32 v[248:249], v[248:249], v[94:95] op_sel_hi:[1,0]\n"
    "v_pk_mul_f32 v[250:251], v[250:251], v[94:95] op_sel_hi:[1,0]\n"
    "v_cvt_pk_bf16_f32 v248, v248, v249\n"
    "v_cvt_pk_bf16_f32 v249, v250, v251\n"
    "v_pk_mul_f32 v[252:253], v[252:253], v[94:95] op_sel:[0,1] op_sel_hi:[1,1]\n"
    "v_pk_mul_f32 v[254:255], v[254:255], v[94:95] op_sel:[0,1] op_sel_hi:[1,1]\n"
    "v_cvt_pk_bf16_f32 v252, v252, v253\n"
    "v_cvt_pk_bf16_f32 v253, v254, v255\n"
    "v_accvgpr_read_b32 v0, a224\n"
    "v_accvgpr_read_b32 v1, a225\n"
    "v_accvgpr_read_b32 v2, a226\n"
    "v_accvgpr_read_b32 v3, a227\n"
    "v_pk_mul_f32 v[0:1], v[0:1], v[96:97] op_sel_hi:[1,0]\n"
    "v_pk_mul_f32 v[2:3], v[2:3], v[96:97] op_sel_hi:[1,0]\n"
    "v_cvt_pk_bf16_f32 v0, v0, v1\n"
    "v_cvt_pk_bf16_f32 v1, v2, v3\n"
    "ds_write_b64 v114, v[248:249] offset:12416\n"
    "ds_write_b64 v114, v[252:253] offset:16512\n"
    "ds_write_b64 v114, v[0:1] offset:20608\n"
    "v_accvgpr_read_b32 v4, a212\n"
    "v_accvgpr_read_b32 v5, a213\n"
    "v_accvgpr_read_b32 v6, a214\n"
    "v_accvgpr_read_b32 v7, a215\n"
    "v_pk_mul_f32 v[4:5], v[4:5], v[94:95] op_sel_hi:[1,0]\n"
    "v_pk_mul_f32 v[6:7], v[6:7], v[94:95] op_sel_hi:[1,0]\n"
    "v_cvt_pk_bf16_f32 v4, v4, v5\n"
    "v_cvt_pk_bf16_f32 v5, v6, v7\n"
    "v_accvgpr_read_b32 v8, a216\n"
    "v_accvgpr_read_b32 v9, a217\n"
    "v_accvgpr_read_b32 v10, a218\n"
    "v_accvgpr_read_b32 v11, a219\n"
    "v_pk_mul_f32 v[8:9], v[8:9], v[94:95] op_sel:[0,1] op_sel_hi:[1,1]\n"
    "v_pk_mul_f32 v[10:11], v[10:11], v[94:95] op_sel:[0,1] op_sel_hi:[1,1]\n"
    "v_cvt_pk_bf16_f32 v8, v8, v9\n"
    "v_cvt_pk_bf16_f32 v9, v10, v11\n"
    "v_accvgpr_read_b32 v0, a220\n"
    "v_accvgpr_read_b32 v1, a221\n"
    "v_accvgpr_read_b32 v2, a222\n"
    "v_accvgpr_read_b32 v3, a223\n"
    "v_pk_mul_f32 v[0:1], v[0:1], v[96:97] op_sel_hi:[1,0]\n"
    "v_pk_mul_f32 v[2:3], v[2:3], v[96:97] op_sel_hi:[1,0]\n"
    "v_cvt_pk_bf16_f32 v0, v0, v1\n"
    "v_cvt_pk_bf16_f32 v1, v2, v3\n"
    "ds_write_b64 v115, v[4:5] offset:12416\n"
    "ds_write_b64 v115, v[8:9] offset:16512\n"
    "ds_write_b64 v115, v[0:1] offset:20608\n"
    "s_waitcnt lgkmcnt(15)\n"
    "v_mfma_f32_16x16x32_bf16 v[0:3], a[96:99], a[0:3], 0\n"
    "v_exp_f32 v12, v12\n"
    "v_mfma_f32_16x16x32_bf16 v[0:3], a[100:103], a[4:7], v[0:3]\n"
    "buffer_store_dwordx4 a[232:235], v89, s[36:39], s41 offen nt\n"
    "v_mfma_f32_16x16x32_bf16 v[0:3], a[104:107], a[8:11], v[0:3]\n"
    "ds_read_b64_tr_b16 a[136:137], v77 offset:0\n"
    "ds_read_b64_tr_b16 a[138:139], v77 offset:4096\n"
    "v_mfma_f32_16x16x32_bf16 v[0:3], a[108:111], a[12:15], v[0:3]\n"
    "ds_read_b64_tr_b16 a[140:141], v77 offset:8192\n"
    "s_add_u32 m0, s35, 4096\n"
    "v_mfma_f32_16x16x32_bf16 v[0:3], a[112:115], a[16:19], v[0:3]\n"
    "ds_read_b64_tr_b16 a[142:143], v77 offset:12288\n"
    "v_mfma_f32_16x16x32_bf16 v[0:3], a[116:119], a[20:23], v[0:3]\n"
    "buffer_load_dwordx4 v84, s[28:31], s88 offen nt lds\n"
    "v_mfma_f32_16x16x32_bf16 v[0:3], a[120:123], a[24:27], v[0:3]\n"
    "ds_read_b64_tr_b16 a[144:145], v78 offset:0\n"
    "ds_read_b64_tr_b16 a[146:147], v78 offset:4096\n"
    "v_mfma_f32_16x16x32_bf16 v[0:3], a[124:127], a[28:31], v[0:3]\n"
    "v_exp_f32 v13, v13\n"
    "v_mfma_f32_16x16x32_bf16 v[4:7], a[96:99], a[32:35], 0\n"
    "ds_read_b64_tr_b16 a[148:149], v78 offset:8192\n"
    "v_cvt_pk_bf16_f32 v30, v12, v13\n"
    "v_mfma_f32_16x16x32_bf16 v[4:7], a[100:103], a[36:39], v[4:7]\n"
    "ds_read_b64_tr_b16 a[150:151], v78 offset:12288\n"
    "v_mfma_f32_16x16x32_bf16 v[4:7], a[104:107], a[40:43], v[4:7]\n"
    "v_exp_f32 v14, v14\n"
    "v_mfma_f32_16x16x32_bf16 v[4:7], a[108:111], a[44:47], v[4:7]\n"
    "ds_read_b64_tr_b16 a[152:153], v79 offset:0\n"
    "ds_read_b64_tr_b16 a[154:155], v79 offset:4096\n"
    "v_mfma_f32_16x16x32_bf16 v[4:7], a[112:115], a[48:51], v[4:7]\n"
    "v_exp_f32 v15, v15\n"
    "v_mfma_f32_16x16x32_bf16 v[4:7], a[116:119], a[52:55], v[4:7]\n"
    "ds_read_b64_tr_b16 a[156:157], v79 offset:8192\n"
    "v_cvt_pk_bf16_f32 v31, v14, v15\n"
    "v_mfma_f32_16x16x32_bf16 v[4:7], a[120:123], a[56:59], v[4:7]\n"
    "ds_read_b64_tr_b16 a[158:159], v79 offset:12288\n"
    "v_mfma_f32_16x16x32_bf16 v[4:7], a[124:127], a[60:63], v[4:7]\n"
    "v_exp_f32 v16, v16\n"
    "v_mfma_f32_16x16x32_bf16 v[8:11], a[96:99], a[64:67], 0\n"
    "v_exp_f32 v17, v17\n"
    "v_mfma_f32_16x16x32_bf16 v[8:11], a[100:103], a[68:71], v[8:11]\n"
    "v_exp_f32 v18, v18\n"
    "v_mfma_f32_16x16x32_bf16 v[8:11], a[104:107], a[72:75], v[8:11]\n"
    "v_exp_f32 v19, v19\n"
    "v_mfma_f32_16x16x32_bf16 v[8:11], a[108:111], a[76:79], v[8:11]\n"
    "v_exp_f32 v20, v20\n"
    "v_mfma_f32_16x16x32_bf16 v[8:11], a[112:115], a[80:83], v[8:11]\n"
    "v_exp_f32 v21, v21\n"
    "v_mfma_f32_16x16x32_bf16 v[8:11], a[116:119], a[84:87], v[8:11]\n"
    "v_exp_f32 v22, v22\n"
    "v_mfma_f32_16x16x32_bf16 v[8:11], a[120:123], a[88:91], v[8:11]\n"
    "v_exp_f32 v23, v23\n"
    "v_mfma_f32_16x16x32_bf16 v[8:11], a[124:127], a[92:95], v[8:11]\n"
    "v_cvt_pk_bf16_f32 v38, v16, v17\n"
    "v_cvt_pk_bf16_f32 v39, v18, v19\n"
    "v_cvt_pk_bf16_f32 v46, v20, v21\n"
    "v_cvt_pk_bf16_f32 v47, v22, v23\n"
    "s_branch LOOP0_%=\n"
    "TAIL0_%=:\n"
    "s_add_u32 s36, s36, 25165824\n"
    "s_addc_u32 s37, s37, 0\n"
    "s_mov_b32 s38, 24576\n"
    "s_waitcnt vmcnt(12) lgkmcnt(0)\n"
    "s_barrier\n"
    "v_mfma_f32_16x16x32_bf16 v[224:227], a[128:131], v[24:27], v[224:227]\n"
    "v_fma_f32 v12, v12, s96, v91\n"
    "s_add_u32 m0, s91, 49152\n"
    "v_mfma_f32_16x16x32_bf16 v[224:227], a[132:135], v[28:31], v[224:227]\n"
    "buffer_load_dwordx4 v82, s[24:27], s67 offen offset:2048 nt lds\n"
    "v_mfma_f32_16x16x32_bf16 v[228:231], a[128:131], v[32:35], v[228:231]\n"
    "v_fma_f32 v13, v13, s96, v91\n"
    "v_fma_f32 v14, v14, s96, v91\n"
    "v_mfma_f32_16x16x32_bf16 v[228:231], a[132:135], v[36:39], v[228:231]\n"
    "v_fma_f32 v15, v15, s96, v91\n"
    "v_fma_f32 v16, v16, s96, v92\n"
    "v_mfma_f32_16x16x32_bf16 v[232:235], a[128:131], v[40:43], v[232:235]\n"
    "v_fma_f32 v17, v17, s96, v92\n"
    "v_fma_f32 v18, v18, s96, v92\n"
    "v_mfma_f32_16x16x32_bf16 v[232:235], a[132:135], v[44:47], v[232:235]\n"
    "buffer_load_dwordx4 v83, s[24:27], s67 offen offset:3072 nt lds\n"
    "v_mfma_f32_16x16x32_bf16 v[236:239], a[136:139], v[24:27], v[236:239]\n"
    "ds_read_b64_tr_b16 a[128:129], v76 offset:0\n"
    "v_fma_f32 v19, v19, s96, v92\n"
    "v_mfma_f32_16x16x32_bf16 v[236:239], a[140:143], v[28:31], v[236:239]\n"
    "ds_read_b64_tr_b16 a[130:131], v76 offset:4096\n"
    "ds_read_b64_tr_b16 a[132:133], v76 offset:8192\n"
    "v_mfma_f32_16x16x32_bf16 v[240:243], a[136:139], v[32:35], v[240:243]\n"
    "ds_read_b64_tr_b16 a[134:135], v76 offset:12288\n"
    "v_fma_f32 v20, v20, s96, v93\n"
    "v_mfma_f32_16x16x32_bf16 v[240:243], a[140:143], v[36:39], v[240:243]\n"
    "v_fma_f32 v21, v21, s96, v93\n"
    "v_fma_f32 v22, v22, s96, v93\n"
    "v_mfma_f32_16x16x32_bf16 v[244:247], a[136:139], v[40:43], v[244:247]\n"
    "v_fma_f32 v23, v23, s96, v93\n"
    "v_max3_f32 v97, v12, v13, v14\n"
    "v_mfma_f32_16x16x32_bf16 v[244:247], a[140:143], v[44:47], v[244:247]\n"
    "ds_read_b64_tr_b16 a[136:137], v77 offset:0\n"
    "v_max3_f32 v97, v97, v15, v16\n"
    "v_mfma_f32_16x16x32_bf16 v[248:251], a[144:147], v[24:27], v[248:251]\n"
    "ds_read_b64_tr_b16 a[138:139], v77 offset:4096\n"
    "v_max3_f32 v97, v97, v17, v18\n"
    "v_mfma_f32_16x16x32_bf16 v[248:251], a[148:151], v[28:31], v[248:251]\n"
    "ds_read_b64_tr_b16 a[140:141], v77 offset:8192\n"
    "ds_read_b64_tr_b16 a[142:143], v77 offset:12288\n"
    "v_mfma_f32_16x16x32_bf16 v[252:255], a[144:147], v[32:35], v[252:255]\n"
    "v_max3_f32 v97, v97, v19, v20\n"
    "v_max3_f32 v97, v97, v21, v22\n"
    "v_mfma_f32_16x16x32_bf16 v[252:255], a[148:151], v[36:39], v[252:255]\n"
    "v_max_f32 v97, v97, v23\n"
    "v_cmp_lt_f32 vcc, s97, v97\n"
    "v_mfma_f32_16x16x32_bf16 a[224:227], a[144:147], v[40:43], a[224:227]\n"
    "v_mfma_f32_16x16x32_bf16 a[224:227], a[148:151], v[44:47], a[224:227]\n"
    "ds_read_b64_tr_b16 a[144:145], v78 offset:0\n"
    "ds_read_b64_tr_b16 a[146:147], v78 offset:4096\n"
    "v_mfma_f32_16x16x32_bf16 a[212:215], a[152:155], v[24:27], a[212:215]\n"
    "ds_read_b64_tr_b16 a[148:149], v78 offset:8192\n"
    "s_cbranch_vccnz STUBt23c0_%=\n"
    "RETt23c0_%=:\n"
    "v_mfma_f32_16x16x32_bf16 a[212:215], a[156:159], v[28:31], a[212:215]\n"
    "ds_read_b64_tr_b16 a[150:151], v78 offset:12288\n"
    "v_mfma_f32_16x16x32_bf16 a[216:219], a[152:155], v[32:35], a[216:219]\n"
    "v_exp_f32 v12, v12\n"
    "v_mfma_f32_16x16x32_bf16 a[216:219], a[156:159], v[36:39], a[216:219]\n"
    "v_exp_f32 v13, v13\n"
    "v_mfma_f32_16x16x32_bf16 a[220:223], a[152:155], v[40:43], a[220:223]\n"
    "v_exp_f32 v14, v14\n"
    "v_mfma_f32_16x16x32_bf16 a[220:223], a[156:159], v[44:47], a[220:223]\n"
    "ds_read_b64_tr_b16 a[152:153], v79 offset:0\n"
    "v_cvt_pk_bf16_f32 v54, v12, v13\n"
    "ds_read_b64_tr_b16 a[154:155], v79 offset:4096\n"
    "ds_read_b64_tr_b16 a[156:157], v79 offset:8192\n"
    "ds_read_b64_tr_b16 a[158:159], v79 offset:12288\n"
    "v_exp_f32 v15, v15\n"
    "v_exp_f32 v16, v16\n"
    "v_exp_f32 v17, v17\n"
    "v_exp_f32 v18, v18\n"
    "v_exp_f32 v19, v19\n"
    "v_exp_f32 v20, v20\n"
    "v_exp_f32 v21, v21\n"
    "v_exp_f32 v22, v22\n"
    "v_exp_f32 v23, v23\n"
    "v_cvt_pk_bf16_f32 v55, v14, v15\n"
    "v_cvt_pk_bf16_f32 v62, v16, v17\n"
    "v_cvt_pk_bf16_f32 v63, v18, v19\n"
    "v_cvt_pk_bf16_f32 v70, v20, v21\n"
    "v_cvt_pk_bf16_f32 v71, v22, v23\n"
    "s_waitcnt vmcnt(8) lgkmcnt(0)\n"
    "s_barrier\n"
    "v_mfma_f32_16x16x32_bf16 a[164:167], a[160:163], v[48:51], a[164:167]\n"
    "v_mfma_f32_16x16x32_bf16 a[164:167], a[160:163], v[52:55], a[164:167]\n"
    "v_mfma_f32_16x16x32_bf16 a[168:171], a[160:163], v[56:59], a[168:171]\n"
    "v_mfma_f32_16x16x32_bf16 a[168:171], a[160:163], v[60:63], a[168:171]\n"
    "v_mfma_f32_16x16x32_bf16 a[172:175], a[160:163], v[64:67], a[172:175]\n"
    "v_mfma_f32_16x16x32_bf16 a[172:175], a[160:163], v[68:71], a[172:175]\n"
    "v_mfma_f32_16x16x32_bf16 v[116:119], a[128:131], v[48:51], v[116:119]\n"
    "v_mfma_f32_16x16x32_bf16 v[116:119], a[132:135], v[52:55], v[116:119]\n"
    "v_mfma_f32_16x16x32_bf16 v[120:123], a[128:131], v[56:59], v[120:123]\n"
    "v_mfma_f32_16x16x32_bf16 v[120:123], a[132:135], v[60:63], v[120:123]\n"
    "v_accvgpr_read_b32 v94, a164\n"
    "v_mfma_f32_16x16x32_bf16 v[124:127], a[128:131], v[64:67], v[124:127]\n"
    "v_accvgpr_read_b32 v95, a168\n"
    "v_mfma_f32_16x16x32_bf16 v[124:127], a[132:135], v[68:71], v[124:127]\n"
    "ds_read_b64_tr_b16 a[128:129], v76 offset:16384\n"
    "v_accvgpr_read_b32 v96, a172\n"
    "v_mfma_f32_16x16x32_bf16 v[128:131], a[136:139], v[48:51], v[128:131]\n"
    "ds_read_b64_tr_b16 a[130:131], v76 offset:20480\n"
    "ds_read_b64_tr_b16 a[132:133], v76 offset:24576\n"
    "v_mfma_f32_16x16x32_bf16 v[128:131], a[140:143], v[52:55], v[128:131]\n"
    "ds_read_b64_tr_b16 a[134:135], v76 offset:28672\n"
    "v_mfma_f32_16x16x32_bf16 v[132:135], a[136:139], v[56:59], v[132:135]\n"
    "v_rcp_f32 v94, v94\n"
    "v_mfma_f32_16x16x32_bf16 v[132:135], a[140:143], v[60:63], v[132:135]\n"
    "v_rcp_f32 v95, v95\n"
    "v_mfma_f32_16x16x32_bf16 v[136:139], a[136:139], v[64:67], v[136:139]\n"
    "v_rcp_f32 v96, v96\n"
    "v_mfma_f32_16x16x32_bf16 v[136:139], a[140:143], v[68:71], v[136:139]\n"
    "ds_read_b64_tr_b16 a[136:137], v77 offset:16384\n"
    "ds_read_b64_tr_b16 a[138:139], v77 offset:20480\n"
    "v_mfma_f32_16x16x32_bf16 v[140:143], a[144:147], v[48:51], v[140:143]\n"
    "ds_read_b64_tr_b16 a[140:141], v77 offset:24576\n"
    "ds_read_b64_tr_b16 a[142:143], v77 offset:28672\n"
    "v_mfma_f32_16x16x32_bf16 v[140:143], a[148:151], v[52:55], v[140:143]\n"
    "v_mfma_f32_16x16x32_bf16 v[144:147], a[144:147], v[56:59], v[144:147]\n"
    "v_mfma_f32_16x16x32_bf16 v[144:147], a[148:151], v[60:63], v[144:147]\n"
    "v_mfma_f32_16x16x32_bf16 v[148:151], a[144:147], v[64:67], v[148:151]\n"
    "v_mfma_f32_16x16x32_bf16 v[148:151], a[148:151], v[68:71], v[148:151]\n"
    "ds_read_b64_tr_b16 a[144:145], v78 offset:16384\n"
    "ds_read_b64_tr_b16 a[146:147], v78 offset:20480\n"
    "v_mfma_f32_16x16x32_bf16 a[176:179], a[152:155], v[48:51], a[176:179]\n"
    "ds_read_b64_tr_b16 a[148:149], v78 offset:24576\n"
    "ds_read_b64_tr_b16 a[150:151], v78 offset:28672\n"
    "v_mfma_f32_16x16x32_bf16 a[176:179], a[156:159], v[52:55], a[176:179]\n"
    "v_mfma_f32_16x16x32_bf16 a[180:183], a[152:155], v[56:59], a[180:183]\n"
    "v_mfma_f32_16x16x32_bf16 a[180:183], a[156:159], v[60:63], a[180:183]\n"
    "v_mfma_f32_16x16x32_bf16 a[184:187], a[152:155], v[64:67], a[184:187]\n"
    "v_mfma_f32_16x16x32_bf16 a[184:187], a[156:159], v[68:71], a[184:187]\n"
    "ds_read_b64_tr_b16 a[152:153], v79 offset:16384\n"
    "ds_read_b64_tr_b16 a[154:155], v79 offset:20480\n"
    "ds_read_b64_tr_b16 a[156:157], v79 offset:24576\n"
    "ds_read_b64_tr_b16 a[158:159], v79 offset:28672\n"
    "s_waitcnt vmcnt(2) lgkmcnt(0)\n"
    "s_barrier\n"
    "v_mfma_f32_16x16x32_bf16 v[152:155], a[128:131], v[48:51], v[152:155]\n"
    "v_mul_f32 v116, v116, v94\n"
    "v_mul_f32 v117, v117, v94\n"
    "v_mfma_f32_16x16x32_bf16 v[152:155], a[132:135], v[52:55], v[152:155]\n"
    "v_mul_f32 v118, v118, v94\n"
    "v_mul_f32 v119, v119, v94\n"
    "v_mfma_f32_16x16x32_bf16 v[156:159], a[128:131], v[56:59], v[156:159]\n"
    "v_cvt_pk_bf16_f32 v116, v116, v117\n"
    "v_cvt_pk_bf16_f32 v117, v118, v119\n"
    "v_mfma_f32_16x16x32_bf16 v[156:159], a[132:135], v[60:63], v[156:159]\n"
    "ds_write_b64 v112, v[116:117] offset:0\n"
    "v_mul_f32 v128, v128, v94\n"
    "v_mfma_f32_16x16x32_bf16 v[160:163], a[128:131], v[64:67], v[160:163]\n"
    "v_mul_f32 v129, v129, v94\n"
    "v_mul_f32 v130, v130, v94\n"
    "v_mfma_f32_16x16x32_bf16 v[160:163], a[132:135], v[68:71], v[160:163]\n"
    "ds_read_b64_tr_b16 a[128:129], v76 offset:32768\n"
    "v_mul_f32 v131, v131, v94\n"
    "v_mfma_f32_16x16x32_bf16 v[164:167], a[136:139], v[48:51], v[164:167]\n"
    "ds_read_b64_tr_b16 a[130:131], v76 offset:36864\n"
    "v_cvt_pk_bf16_f32 v128, v128, v129\n"
    "v_mfma_f32_16x16x32_bf16 v[164:167], a[140:143], v[52:55], v[164:167]\n"
    "ds_read_b64_tr_b16 a[132:133], v76 offset:40960\n"
    "ds_read_b64_tr_b16 a[134:135], v76 offset:45056\n"
    "v_mfma_f32_16x16x32_bf16 v[168:171], a[136:139], v[56:59], v[168:171]\n"
    "v_cvt_pk_bf16_f32 v129, v130, v131\n"
    "v_mul_f32 v140, v140, v94\n"
    "v_mfma_f32_16x16x32_bf16 v[168:171], a[140:143], v[60:63], v[168:171]\n"
    "ds_write_b64 v113, v[128:129] offset:0\n"
    "v_mul_f32 v141, v141, v94\n"
    "v_mfma_f32_16x16x32_bf16 v[172:175], a[136:139], v[64:67], v[172:175]\n"
    "v_mul_f32 v142, v142, v94\n"
    "v_mul_f32 v143, v143, v94\n"
    "v_mfma_f32_16x16x32_bf16 v[172:175], a[140:143], v[68:71], v[172:175]\n"
    "ds_read_b64_tr_b16 a[136:137], v77 offset:32768\n"
    "v_cvt_pk_bf16_f32 v140, v140, v141\n"
    "v_mfma_f32_16x16x32_bf16 v[176:179], a[144:147], v[48:51], v[176:179]\n"
    "ds_read_b64_tr_b16 a[138:139], v77 offset:36864\n"
    "v_cvt_pk_bf16_f32 v141, v142, v143\n"
    "v_mfma_f32_16x16x32_bf16 v[176:179], a[148:151], v[52:55], v[176:179]\n"
    "ds_read_b64_tr_b16 a[140:141], v77 offset:40960\n"
    "ds_write_b64 v114, v[140:141] offset:0\n"
    "v_mfma_f32_16x16x32_bf16 v[180:183], a[144:147], v[56:59], v[180:183]\n"
    "ds_read_b64_tr_b16 a[142:143], v77 offset:45056\n"
    "v_accvgpr_read_b32 v0, a176\n"
    "v_mfma_f32_16x16x32_bf16 v[180:183], a[148:151], v[60:63], v[180:183]\n"
    "v_accvgpr_read_b32 v1, a177\n"
    "v_accvgpr_read_b32 v2, a178\n"
    "v_mfma_f32_16x16x32_bf16 v[184:187], a[144:147], v[64:67], v[184:187]\n"
    "v_accvgpr_read_b32 v3, a179\n"
    "v_mul_f32 v0, v0, v94\n"
    "v_mfma_f32_16x16x32_bf16 v[184:187], a[148:151], v[68:71], v[184:187]\n"
    "ds_read_b64_tr_b16 a[144:145], v78 offset:32768\n"
    "v_mul_f32 v1, v1, v94\n"
    "v_mfma_f32_16x16x32_bf16 a[188:191], a[152:155], v[48:51], a[188:191]\n"
    "ds_read_b64_tr_b16 a[146:147], v78 offset:36864\n"
    "v_mul_f32 v2, v2, v94\n"
    "v_mfma_f32_16x16x32_bf16 a[188:191], a[156:159], v[52:55], a[188:191]\n"
    "ds_read_b64_tr_b16 a[148:149], v78 offset:40960\n"
    "ds_read_b64_tr_b16 a[150:151], v78 offset:45056\n"
    "v_mfma_f32_16x16x32_bf16 a[192:195], a[152:155], v[56:59], a[192:195]\n"
    "v_mul_f32 v3, v3, v94\n"
    "v_cvt_pk_bf16_f32 v0, v0, v1\n"
    "v_mfma_f32_16x16x32_bf16 a[192:195], a[156:159], v[60:63], a[192:195]\n"
    "v_cvt_pk_bf16_f32 v1, v2, v3\n"
    "v_mul_f32 v120, v120, v95\n"
    "v_mfma_f32_16x16x32_bf16 a[196:199], a[152:155], v[64:67], a[196:199]\n"
    "ds_write_b64 v115, v[0:1] offset:0\n"
    "v_mul_f32 v121, v121, v95\n"
    "v_mfma_f32_16x16x32_bf16 a[196:199], a[156:159], v[68:71], a[196:199]\n"
    "ds_read_b64_tr_b16 a[152:153], v79 offset:32768\n"
    "v_mul_f32 v122, v122, v95\n"
    "ds_read_b64_tr_b16 a[154:155], v79 offset:36864\n"
    "v_mul_f32 v123, v123, v95\n"
    "v_cvt_pk_bf16_f32 v120, v120, v121\n"
    "v_cvt_pk_bf16_f32 v121, v122, v123\n"
    "ds_write_b64 v112, v[120:121] offset:4096\n"
    "v_pk_mul_f32 v[132:133], v[132:133], v[94:95] op_sel:[0,1] op_sel_hi:[1,1]\n"
    "v_pk_mul_f32 v[134:135], v[134:135], v[94:95] op_sel:[0,1] op_sel_hi:[1,1]\n"
    "v_cvt_pk_bf16_f32 v132, v132, v133\n"
    "v_cvt_pk_bf16_f32 v133, v134, v135\n"
    "ds_write_b64 v113, v[132:133] offset:4096\n"
    "v_pk_mul_f32 v[144:145], v[144:145], v[94:95] op_sel:[0,1] op_sel_hi:[1,1]\n"
    "v_pk_mul_f32 v[146:147], v[146:147], v[94:95] op_sel:[0,1] op_sel_hi:[1,1]\n"
    "v_cvt_pk_bf16_f32 v144, v144, v145\n"
    "v_cvt_pk_bf16_f32 v145, v146, v147\n"
    "ds_write_b64 v114, v[144:145] offset:4096\n"
    "v_accvgpr_read_b32 v4, a180\n"
    "v_accvgpr_read_b32 v5, a181\n"
    "v_accvgpr_read_b32 v6, a182\n"
    "v_accvgpr_read_b32 v7, a183\n"
    "v_pk_mul_f32 v[4:5], v[4:5], v[94:95] op_sel:[0,1] op_sel_hi:[1,1]\n"
    "v_pk_mul_f32 v[6:7], v[6:7], v[94:95] op_sel:[0,1] op_sel_hi:[1,1]\n"
    "v_cvt_pk_bf16_f32 v4, v4, v5\n"
    "v_cvt_pk_bf16_f32 v5, v6, v7\n"
    "ds_write_b64 v115, v[4:5] offset:4096\n"
    "v_pk_mul_f32 v[124:125], v[124:125], v[96:97] op_sel_hi:[1,0]\n"
    "v_pk_mul_f32 v[126:127], v[126:127], v[96:97] op_sel_hi:[1,0]\n"
    "v_cvt_pk_bf16_f32 v124, v124, v125\n"
    "v_cvt_pk_bf16_f32 v125, v126, v127\n"
    "ds_write_b64 v112, v[124:125] offset:8192\n"
    "v_pk_mul_f32 v[136:137], v[136:137], v[96:97] op_sel_hi:[1,0]\n"
    "v_pk_mul_f32 v[138:139], v[138:139], v[96:97] op_sel_hi:[1,0]\n"
    "v_cvt_pk_bf16_f32 v136, v136, v137\n"
    "v_cvt_pk_bf16_f32 v137, v138, v139\n"
    "ds_write_b64 v113, v[136:137] offset:8192\n"
    "v_pk_mul_f32 v[148:149], v[148:149], v[96:97] op_sel_hi:[1,0]\n"
    "v_pk_mul_f32 v[150:151], v[150:151], v[96:97] op_sel_hi:[1,0]\n"
    "v_cvt_pk_bf16_f32 v148, v148, v149\n"
    "v_cvt_pk_bf16_f32 v149, v150, v151\n"
    "ds_write_b64 v114, v[148:149] offset:8192\n"
    "v_accvgpr_read_b32 v8, a184\n"
    "v_accvgpr_read_b32 v9, a185\n"
    "v_accvgpr_read_b32 v10, a186\n"
    "v_accvgpr_read_b32 v11, a187\n"
    "v_pk_mul_f32 v[8:9], v[8:9], v[96:97] op_sel_hi:[1,0]\n"
    "v_pk_mul_f32 v[10:11], v[10:11], v[96:97] op_sel_hi:[1,0]\n"
    "v_cvt_pk_bf16_f32 v8, v8, v9\n"
    "v_cvt_pk_bf16_f32 v9, v10, v11\n"
    "ds_write_b64 v115, v[8:9] offset:8192\n"
    "ds_read_b64_tr_b16 a[156:157], v79 offset:40960\n"
    "ds_read_b64_tr_b16 a[158:159], v79 offset:45056\n"
    "s_waitcnt vmcnt(0) lgkmcnt(0)\n"
    "s_barrier\n"
    "v_mfma_f32_16x16x32_bf16 v[188:191], a[128:131], v[48:51], v[188:191]\n"
    "v_mul_f32 v152, v152, v94\n"
    "v_mul_f32 v153, v153, v94\n"
    "v_mfma_f32_16x16x32_bf16 v[188:191], a[132:135], v[52:55], v[188:191]\n"
    "v_mul_f32 v154, v154, v94\n"
    "v_mul_f32 v155, v155, v94\n"
    "v_mfma_f32_16x16x32_bf16 v[192:195], a[128:131], v[56:59], v[192:195]\n"
    "v_cvt_pk_bf16_f32 v152, v152, v153\n"
    "v_cvt_pk_bf16_f32 v153, v154, v155\n"
    "v_mfma_f32_16x16x32_bf16 v[192:195], a[132:135], v[60:63], v[192:195]\n"
    "ds_write_b64 v112, v[152:153] offset:128\n"
    "v_mul_f32 v164, v164, v94\n"
    "v_mfma_f32_16x16x32_bf16 v[196:199], a[128:131], v[64:67], v[196:199]\n"
    "v_mul_f32 v165, v165, v94\n"
    "v_mul_f32 v166, v166, v94\n"
    "v_mfma_f32_16x16x32_bf16 v[196:199], a[132:135], v[68:71], v[196:199]\n"
    "ds_read_b64_tr_b16 a[128:129], v76 offset:49152\n"
    "v_mul_f32 v167, v167, v94\n"
    "v_mfma_f32_16x16x32_bf16 v[200:203], a[136:139], v[48:51], v[200:203]\n"
    "ds_read_b64_tr_b16 a[130:131], v76 offset:53248\n"
    "v_cvt_pk_bf16_f32 v164, v164, v165\n"
    "v_mfma_f32_16x16x32_bf16 v[200:203], a[140:143], v[52:55], v[200:203]\n"
    "ds_read_b64_tr_b16 a[132:133], v76 offset:57344\n"
    "ds_read_b64_tr_b16 a[134:135], v76 offset:61440\n"
    "v_mfma_f32_16x16x32_bf16 v[204:207], a[136:139], v[56:59], v[204:207]\n"
    "v_cvt_pk_bf16_f32 v165, v166, v167\n"
    "v_mul_f32 v176, v176, v94\n"
    "v_mfma_f32_16x16x32_bf16 v[204:207], a[140:143], v[60:63], v[204:207]\n"
    "ds_write_b64 v113, v[164:165] offset:128\n"
    "v_mul_f32 v177, v177, v94\n"
    "v_mfma_f32_16x16x32_bf16 v[208:211], a[136:139], v[64:67], v[208:211]\n"
    "v_mul_f32 v178, v178, v94\n"
    "v_mul_f32 v179, v179, v94\n"
    "v_mfma_f32_16x16x32_bf16 v[208:211], a[140:143], v[68:71], v[208:211]\n"
    "ds_read_b64_tr_b16 a[136:137], v77 offset:49152\n"
    "v_cvt_pk_bf16_f32 v176, v176, v177\n"
    "v_mfma_f32_16x16x32_bf16 v[212:215], a[144:147], v[48:51], v[212:215]\n"
    "ds_read_b64_tr_b16 a[138:139], v77 offset:53248\n"
    "v_cvt_pk_bf16_f32 v177, v178, v179\n"
    "v_mfma_f32_16x16x32_bf16 v[212:215], a[148:151], v[52:55], v[212:215]\n"
    "ds_read_b64_tr_b16 a[140:141], v77 offset:57344\n"
    "ds_write_b64 v114, v[176:177] offset:128\n"
    "v_mfma_f32_16x16x32_bf16 v[216:219], a[144:147], v[56:59], v[216:219]\n"
    "ds_read_b64_tr_b16 a[142:143], v77 offset:61440\n"
    "v_accvgpr_read_b32 v0, a188\n"
    "v_mfma_f32_16x16x32_bf16 v[216:219], a[148:151], v[60:63], v[216:219]\n"
    "v_accvgpr_read_b32 v1, a189\n"
    "v_accvgpr_read_b32 v2, a190\n"
    "v_mfma_f32_16x16x32_bf16 v[220:223], a[144:147], v[64:67], v[220:223]\n"
    "v_accvgpr_read_b32 v3, a191\n"
    "v_mul_f32 v0, v0, v94\n"
    "v_mfma_f32_16x16x32_bf16 v[220:223], a[148:151], v[68:71], v[220:223]\n"
    "ds_read_b64_tr_b16 a[144:145], v78 offset:49152\n"
    "v_mul_f32 v1, v1, v94\n"
    "v_mfma_f32_16x16x32_bf16 a[200:203], a[152:155], v[48:51], a[200:203]\n"
    "ds_read_b64_tr_b16 a[146:147], v78 offset:53248\n"
    "v_mul_f32 v2, v2, v94\n"
    "v_mfma_f32_16x16x32_bf16 a[200:203], a[156:159], v[52:55], a[200:203]\n"
    "ds_read_b64_tr_b16 a[148:149], v78 offset:57344\n"
    "ds_read_b64_tr_b16 a[150:151], v78 offset:61440\n"
    "v_mfma_f32_16x16x32_bf16 a[204:207], a[152:155], v[56:59], a[204:207]\n"
    "v_mul_f32 v3, v3, v94\n"
    "v_cvt_pk_bf16_f32 v0, v0, v1\n"
    "v_mfma_f32_16x16x32_bf16 a[204:207], a[156:159], v[60:63], a[204:207]\n"
    "v_cvt_pk_bf16_f32 v1, v2, v3\n"
    "v_mul_f32 v156, v156, v95\n"
    "v_mfma_f32_16x16x32_bf16 a[208:211], a[152:155], v[64:67], a[208:211]\n"
    "ds_write_b64 v115, v[0:1] offset:128\n"
    "ds_read_b128  a[0:3], v88 offset:0\n"
    "v_mfma_f32_16x16x32_bf16 a[208:211], a[156:159], v[68:71], a[208:211]\n"
    "ds_read_b128  a[4:7], v90 offset:1024\n"
    "ds_read_b128  a[8:11], v88 offset:2048\n"
    "ds_read_b128  a[12:15], v90 offset:3072\n"
    "v_mul_f32 v157, v157, v95\n"
    "v_pk_mul_f32 v[158:159], v[158:159], v[94:95] op_sel:[0,1] op_sel_hi:[1,1]\n"
    "v_cvt_pk_bf16_f32 v156, v156, v157\n"
    "v_cvt_pk_bf16_f32 v157, v158, v159\n"
    "ds_write_b64 v112, v[156:157] offset:4224\n"
    "v_pk_mul_f32 v[168:169], v[168:169], v[94:95] op_sel:[0,1] op_sel_hi:[1,1]\n"
    "v_mul_f32 v170, v170, v95\n"
    "s_waitcnt lgkmcnt(4)\n"
    "buffer_store_dwordx4 a[0:3], v89, s[36:39], s40 offen nt\n"
    "v_mul_f32 v171, v171, v95\n"
    "s_waitcnt lgkmcnt(3)\n"
    "buffer_store_dwordx4 a[4:7], v89, s[36:39], s40 offen offset:2048 nt\n"
    "s_waitcnt lgkmcnt(2)\n"
    "buffer_store_dwordx4 a[8:11], v89, s[36:39], s88 offen nt\n"
    "s_waitcnt lgkmcnt(1)\n"
    "buffer_store_dwordx4 a[12:15], v89, s[36:39], s88 offen offset:2048 nt\n"
    "v_cvt_pk_bf16_f32 v168, v168, v169\n"
    "v_cvt_pk_bf16_f32 v169, v170, v171\n"
    "ds_write_b64 v113, v[168:169] offset:4224\n"
    "v_pk_mul_f32 v[180:181], v[180:181], v[94:95] op_sel:[0,1] op_sel_hi:[1,1]\n"
    "v_pk_mul_f32 v[182:183], v[182:183], v[94:95] op_sel:[0,1] op_sel_hi:[1,1]\n"
    "v_cvt_pk_bf16_f32 v180, v180, v181\n"
    "v_cvt_pk_bf16_f32 v181, v182, v183\n"
    "ds_write_b64 v114, v[180:181] offset:4224\n"
    "v_accvgpr_read_b32 v4, a192\n"
    "v_accvgpr_read_b32 v5, a193\n"
    "v_accvgpr_read_b32 v6, a194\n"
    "v_accvgpr_read_b32 v7, a195\n"
    "v_pk_mul_f32 v[4:5], v[4:5], v[94:95] op_sel:[0,1] op_sel_hi:[1,1]\n"
    "v_pk_mul_f32 v[6:7], v[6:7], v[94:95] op_sel:[0,1] op_sel_hi:[1,1]\n"
    "v_cvt_pk_bf16_f32 v4, v4, v5\n"
    "v_cvt_pk_bf16_f32 v5, v6, v7\n"
    "ds_write_b64 v115, v[4:5] offset:4224\n"
    "ds_read_b128  a[16:19], v88 offset:4096\n"
    "ds_read_b128  a[20:23], v90 offset:5120\n"
    "ds_read_b128  a[24:27], v88 offset:6144\n"
    "ds_read_b128  a[28:31], v90 offset:7168\n"
    "v_pk_mul_f32 v[160:161], v[160:161], v[96:97] op_sel_hi:[1,0]\n"
    "v_pk_mul_f32 v[162:163], v[162:163], v[96:97] op_sel_hi:[1,0]\n"
    "v_cvt_pk_bf16_f32 v160, v160, v161\n"
    "v_cvt_pk_bf16_f32 v161, v162, v163\n"
    "ds_write_b64 v112, v[160:161] offset:8320\n"
    "v_pk_mul_f32 v[172:173], v[172:173], v[96:97] op_sel_hi:[1,0]\n"
    "v_mul_f32 v174, v174, v96\n"
    "s_waitcnt lgkmcnt(4)\n"
    "buffer_store_dwordx4 a[16:19], v89, s[36:39], s41 offen nt\n"
    "s_waitcnt lgkmcnt(3)\n"
    "buffer_store_dwordx4 a[20:23], v89, s[36:39], s41 offen offset:2048 nt\n"
    "s_waitcnt lgkmcnt(2)\n"
    "buffer_store_dwordx4 a[24:27], v89, s[36:39], s89 offen nt\n"
    "s_waitcnt lgkmcnt(1)\n"
    "buffer_store_dwordx4 a[28:31], v89, s[36:39], s89 offen offset:2048 nt\n"
    "v_mul_f32 v175, v175, v96\n"
    "v_cvt_pk_bf16_f32 v172, v172, v173\n"
    "v_cvt_pk_bf16_f32 v173, v174, v175\n"
    "ds_write_b64 v113, v[172:173] offset:8320\n"
    "v_pk_mul_f32 v[184:185], v[184:185], v[96:97] op_sel_hi:[1,0]\n"
    "v_pk_mul_f32 v[186:187], v[186:187], v[96:97] op_sel_hi:[1,0]\n"
    "v_cvt_pk_bf16_f32 v184, v184, v185\n"
    "v_cvt_pk_bf16_f32 v185, v186, v187\n"
    "ds_write_b64 v114, v[184:185] offset:8320\n"
    "v_accvgpr_read_b32 v8, a196\n"
    "v_accvgpr_read_b32 v9, a197\n"
    "v_accvgpr_read_b32 v10, a198\n"
    "v_accvgpr_read_b32 v11, a199\n"
    "v_pk_mul_f32 v[8:9], v[8:9], v[96:97] op_sel_hi:[1,0]\n"
    "v_pk_mul_f32 v[10:11], v[10:11], v[96:97] op_sel_hi:[1,0]\n"
    "v_cvt_pk_bf16_f32 v8, v8, v9\n"
    "v_cvt_pk_bf16_f32 v9, v10, v11\n"
    "ds_write_b64 v115, v[8:9] offset:8320\n"
    "ds_read_b128  a[32:35], v88 offset:8192\n"
    "ds_read_b128  a[36:39], v90 offset:9216\n"
    "ds_read_b128  a[40:43], v88 offset:10240\n"
    "ds_read_b128  a[44:47], v90 offset:11264\n"
    "ds_read_b64_tr_b16 a[152:153], v79 offset:49152\n"
    "ds_read_b64_tr_b16 a[154:155], v79 offset:53248\n"
    "ds_read_b64_tr_b16 a[156:157], v79 offset:57344\n"
    "ds_read_b64_tr_b16 a[158:159], v79 offset:61440\n"
    "s_waitcnt lgkmcnt(7)\n"
    "buffer_store_dwordx4 a[32:35], v89, s[36:39], s42 offen nt\n"
    "s_waitcnt lgkmcnt(6)\n"
    "buffer_store_dwordx4 a[36:39], v89, s[36:39], s42 offen offset:2048 nt\n"
    "s_waitcnt lgkmcnt(5)\n"
    "buffer_store_dwordx4 a[40:43], v89, s[36:39], s90 offen nt\n"
    "s_waitcnt lgkmcnt(4)\n"
    "buffer_store_dwordx4 a[44:47], v89, s[36:39], s90 offen offset:2048 nt\n"
    "s_waitcnt lgkmcnt(0)\n"
    "s_barrier\n"
    "v_mfma_f32_16x16x32_bf16 v[224:227], a[128:131], v[48:51], v[224:227]\n"
    "v_mul_f32 v188, v188, v94\n"
    "v_mul_f32 v189, v189, v94\n"
    "v_mfma_f32_16x16x32_bf16 v[224:227], a[132:135], v[52:55], v[224:227]\n"
    "v_mul_f32 v190, v190, v94\n"
    "v_mul_f32 v191, v191, v94\n"
    "v_mfma_f32_16x16x32_bf16 v[228:231], a[128:131], v[56:59], v[228:231]\n"
    "v_cvt_pk_bf16_f32 v188, v188, v189\n"
    "v_cvt_pk_bf16_f32 v189, v190, v191\n"
    "v_mfma_f32_16x16x32_bf16 v[228:231], a[132:135], v[60:63], v[228:231]\n"
    "ds_write_b64 v112, v[188:189] offset:12288\n"
    "v_mul_f32 v200, v200, v94\n"
    "v_mfma_f32_16x16x32_bf16 v[232:235], a[128:131], v[64:67], v[232:235]\n"
    "v_mul_f32 v201, v201, v94\n"
    "v_mul_f32 v202, v202, v94\n"
    "v_mfma_f32_16x16x32_bf16 v[232:235], a[132:135], v[68:71], v[232:235]\n"
    "v_mul_f32 v203, v203, v94\n"
    "v_cvt_pk_bf16_f32 v200, v200, v201\n"
    "v_mfma_f32_16x16x32_bf16 v[236:239], a[136:139], v[48:51], v[236:239]\n"
    "v_cvt_pk_bf16_f32 v201, v202, v203\n"
    "v_mul_f32 v212, v212, v94\n"
    "v_mfma_f32_16x16x32_bf16 v[236:239], a[140:143], v[52:55], v[236:239]\n"
    "ds_write_b64 v113, v[200:201] offset:12288\n"
    "v_mul_f32 v213, v213, v94\n"
    "v_mfma_f32_16x16x32_bf16 v[240:243], a[136:139], v[56:59], v[240:243]\n"
    "v_mul_f32 v214, v214, v94\n"
    "v_mul_f32 v215, v215, v94\n"
    "v_mfma_f32_16x16x32_bf16 v[240:243], a[140:143], v[60:63], v[240:243]\n"
    "v_cvt_pk_bf16_f32 v212, v212, v213\n"
    "v_cvt_pk_bf16_f32 v213, v214, v215\n"
    "v_mfma_f32_16x16x32_bf16 v[244:247], a[136:139], v[64:67], v[244:247]\n"
    "ds_write_b64 v114, v[212:213] offset:12288\n"
    "v_accvgpr_read_b32 v0, a200\n"
    "v_mfma_f32_16x16x32_bf16 v[244:247], a[140:143], v[68:71], v[244:247]\n"
    "v_accvgpr_read_b32 v1, a201\n"
    "v_accvgpr_read_b32 v2, a202\n"
    "v_mfma_f32_16x16x32_bf16 v[248:251], a[144:147], v[48:51], v[248:251]\n"
    "v_accvgpr_read_b32 v3, a203\n"
    "v_mul_f32 v0, v0, v94\n"
    "v_mfma_f32_16x16x32_bf16 v[248:251], a[148:151], v[52:55], v[248:251]\n"
    "v_mul_f32 v1, v1, v94\n"
    "v_mul_f32 v2, v2, v94\n"
    "v_mfma_f32_16x16x32_bf16 v[252:255], a[144:147], v[56:59], v[252:255]\n"
    "v_mul_f32 v3, v3, v94\n"
    "v_cvt_pk_bf16_f32 v0, v0, v1\n"
    "v_mfma_f32_16x16x32_bf16 v[252:255], a[148:151], v[60:63], v[252:255]\n"
    "v_cvt_pk_bf16_f32 v1, v2, v3\n"
    "v_mul_f32 v192, v192, v95\n"
    "v_mfma_f32_16x16x32_bf16 a[224:227], a[144:147], v[64:67], a[224:227]\n"
    "ds_write_b64 v115, v[0:1] offset:12288\n"
    "v_mul_f32 v193, v193, v95\n"
    "v_mfma_f32_16x16x32_bf16 a[224:227], a[148:151], v[68:71], a[224:227]\n"
    "v_mul_f32 v194, v194, v95\n"
    "v_mul_f32 v195, v195, v95\n"
    "v_mfma_f32_16x16x32_bf16 a[212:215], a[152:155], v[48:51], a[212:215]\n"
    "v_cvt_pk_bf16_f32 v192, v192, v193\n"
    "v_cvt_pk_bf16_f32 v193, v194, v195\n"
    "v_mfma_f32_16x16x32_bf16 a[212:215], a[156:159], v[52:55], a[212:215]\n"
    "ds_write_b64 v112, v[192:193] offset:16384\n"
    "v_mul_f32 v204, v204, v95\n"
    "v_mfma_f32_16x16x32_bf16 a[216:219], a[152:155], v[56:59], a[216:219]\n"
    "v_mul_f32 v205, v205, v95\n"
    "v_mul_f32 v206, v206, v95\n"
    "v_mfma_f32_16x16x32_bf16 a[216:219], a[156:159], v[60:63], a[216:219]\n"
    "v_mul_f32 v207, v207, v95\n"
    "v_cvt_pk_bf16_f32 v204, v204, v205\n"
    "v_mfma_f32_16x16x32_bf16 a[220:223], a[152:155], v[64:67], a[220:223]\n"
    "v_cvt_pk_bf16_f32 v205, v206, v207\n"
    "v_mul_f32 v216, v216, v95\n"
    "v_mfma_f32_16x16x32_bf16 a[220:223], a[156:159], v[68:71], a[220:223]\n"
    "ds_write_b64 v113, v[204:205] offset:16384\n"
    "v_mul_f32 v217, v217, v95\n"
    "v_mul_f32 v218, v218, v95\n"
    "v_mul_f32 v219, v219, v95\n"
    "v_cvt_pk_bf16_f32 v216, v216, v217\n"
    "v_cvt_pk_bf16_f32 v217, v218, v219\n"
    "ds_write_b64 v114, v[216:217] offset:16384\n"
    "v_accvgpr_read_b32 v4, a204\n"
    "v_accvgpr_read_b32 v5, a205\n"
    "v_accvgpr_read_b32 v6, a206\n"
    "v_accvgpr_read_b32 v7, a207\n"
    "v_pk_mul_f32 v[4:5], v[4:5], v[94:95] op_sel:[0,1] op_sel_hi:[1,1]\n"
    "v_pk_mul_f32 v[6:7], v[6:7], v[94:95] op_sel:[0,1] op_sel_hi:[1,1]\n"
    "v_cvt_pk_bf16_f32 v4, v4, v5\n"
    "v_cvt_pk_bf16_f32 v5, v6, v7\n"
    "ds_write_b64 v115, v[4:5] offset:16384\n"
    "v_pk_mul_f32 v[196:197], v[196:197], v[96:97] op_sel_hi:[1,0]\n"
    "v_pk_mul_f32 v[198:199], v[198:199], v[96:97] op_sel_hi:[1,0]\n"
    "v_cvt_pk_bf16_f32 v196, v196, v197\n"
    "v_cvt_pk_bf16_f32 v197, v198, v199\n"
    "ds_write_b64 v112, v[196:197] offset:20480\n"
    "v_pk_mul_f32 v[208:209], v[208:209], v[96:97] op_sel_hi:[1,0]\n"
    "v_pk_mul_f32 v[210:211], v[210:211], v[96:97] op_sel_hi:[1,0]\n"
    "v_cvt_pk_bf16_f32 v208, v208, v209\n"
    "v_cvt_pk_bf16_f32 v209, v210, v211\n"
    "ds_write_b64 v113, v[208:209] offset:20480\n"
    "v_pk_mul_f32 v[220:221], v[220:221], v[96:97] op_sel_hi:[1,0]\n"
    "v_pk_mul_f32 v[222:223], v[222:223], v[96:97] op_sel_hi:[1,0]\n"
    "v_cvt_pk_bf16_f32 v220, v220, v221\n"
    "v_cvt_pk_bf16_f32 v221, v222, v223\n"
    "ds_write_b64 v114, v[220:221] offset:20480\n"
    "v_accvgpr_read_b32 v8, a208\n"
    "v_accvgpr_read_b32 v9, a209\n"
    "v_accvgpr_read_b32 v10, a210\n"
    "v_accvgpr_read_b32 v11, a211\n"
    "v_pk_mul_f32 v[8:9], v[8:9], v[96:97] op_sel_hi:[1,0]\n"
    "v_pk_mul_f32 v[10:11], v[10:11], v[96:97] op_sel_hi:[1,0]\n"
    "v_cvt_pk_bf16_f32 v8, v8, v9\n"
    "v_cvt_pk_bf16_f32 v9, v10, v11\n"
    "ds_write_b64 v115, v[8:9] offset:20480\n"
    "v_pk_mul_f32 v[224:225], v[224:225], v[94:95] op_sel_hi:[1,0]\n"
    "v_pk_mul_f32 v[226:227], v[226:227], v[94:95] op_sel_hi:[1,0]\n"
    "v_cvt_pk_bf16_f32 v224, v224, v225\n"
    "v_cvt_pk_bf16_f32 v225, v226, v227\n"
    "v_pk_mul_f32 v[236:237], v[236:237], v[94:95] op_sel_hi:[1,0]\n"
    "v_pk_mul_f32 v[238:239], v[238:239], v[94:95] op_sel_hi:[1,0]\n"
    "v_cvt_pk_bf16_f32 v236, v236, v237\n"
    "v_cvt_pk_bf16_f32 v237, v238, v239\n"
    "v_pk_mul_f32 v[248:249], v[248:249], v[94:95] op_sel_hi:[1,0]\n"
    "v_pk_mul_f32 v[250:251], v[250:251], v[94:95] op_sel_hi:[1,0]\n"
    "v_cvt_pk_bf16_f32 v248, v248, v249\n"
    "v_cvt_pk_bf16_f32 v249, v250, v251\n"
    "v_accvgpr_read_b32 v0, a212\n"
    "v_accvgpr_read_b32 v1, a213\n"
    "v_accvgpr_read_b32 v2, a214\n"
    "v_accvgpr_read_b32 v3, a215\n"
    "v_pk_mul_f32 v[0:1], v[0:1], v[94:95] op_sel_hi:[1,0]\n"
    "v_pk_mul_f32 v[2:3], v[2:3], v[94:95] op_sel_hi:[1,0]\n"
    "v_cvt_pk_bf16_f32 v0, v0, v1\n"
    "v_cvt_pk_bf16_f32 v1, v2, v3\n"
    "ds_write_b64 v112, v[224:225] offset:12416\n"
    "ds_write_b64 v113, v[236:237] offset:12416\n"
    "ds_write_b64 v114, v[248:249] offset:12416\n"
    "ds_write_b64 v115, v[0:1] offset:12416\n"
    "ds_read_b128 a[48:51], v88 offset:12288\n"
    "ds_read_b128 a[52:55], v90 offset:13312\n"
    "ds_read_b128 a[56:59], v88 offset:14336\n"
    "ds_read_b128 a[60:63], v90 offset:15360\n"
    "v_pk_mul_f32 v[228:229], v[228:229], v[94:95] op_sel:[0,1] op_sel_hi:[1,1]\n"
    "v_pk_mul_f32 v[230:231], v[230:231], v[94:95] op_sel:[0,1] op_sel_hi:[1,1]\n"
    "v_cvt_pk_bf16_f32 v228, v228, v229\n"
    "v_cvt_pk_bf16_f32 v229, v230, v231\n"
    "v_pk_mul_f32 v[240:241], v[240:241], v[94:95] op_sel:[0,1] op_sel_hi:[1,1]\n"
    "v_pk_mul_f32 v[242:243], v[242:243], v[94:95] op_sel:[0,1] op_sel_hi:[1,1]\n"
    "v_cvt_pk_bf16_f32 v240, v240, v241\n"
    "v_cvt_pk_bf16_f32 v241, v242, v243\n"
    "v_pk_mul_f32 v[252:253], v[252:253], v[94:95] op_sel:[0,1] op_sel_hi:[1,1]\n"
    "v_pk_mul_f32 v[254:255], v[254:255], v[94:95] op_sel:[0,1] op_sel_hi:[1,1]\n"
    "v_cvt_pk_bf16_f32 v252, v252, v253\n"
    "v_cvt_pk_bf16_f32 v253, v254, v255\n"
    "v_accvgpr_read_b32 v4, a216\n"
    "v_accvgpr_read_b32 v5, a217\n"
    "v_accvgpr_read_b32 v6, a218\n"
    "v_accvgpr_read_b32 v7, a219\n"
    "v_pk_mul_f32 v[4:5], v[4:5], v[94:95] op_sel:[0,1] op_sel_hi:[1,1]\n"
    "v_pk_mul_f32 v[6:7], v[6:7], v[94:95] op_sel:[0,1] op_sel_hi:[1,1]\n"
    "v_cvt_pk_bf16_f32 v4, v4, v5\n"
    "v_cvt_pk_bf16_f32 v5, v6, v7\n"
    "s_waitcnt lgkmcnt(3)\n"
    "buffer_store_dwordx4 a[48:51], v89, s[36:39], s40 offen offset:256 nt\n"
    "s_waitcnt lgkmcnt(2)\n"
    "buffer_store_dwordx4 a[52:55], v89, s[36:39], s40 offen offset:2304 nt\n"
    "s_waitcnt lgkmcnt(1)\n"
    "buffer_store_dwordx4 a[56:59], v89, s[36:39], s88 offen offset:256 nt\n"
    "s_waitcnt lgkmcnt(0)\n"
    "buffer_store_dwordx4 a[60:63], v89, s[36:39], s88 offen offset:2304 nt\n"
    "ds_write_b64 v112, v[228:229] offset:16512\n"
    "ds_write_b64 v113, v[240:241] offset:16512\n"
    "ds_write_b64 v114, v[252:253] offset:16512\n"
    "ds_write_b64 v115, v[4:5] offset:16512\n"
    "ds_read_b128 a[64:67], v88 offset:16384\n"
    "ds_read_b128 a[68:71], v90 offset:17408\n"
    "ds_read_b128 a[72:75], v88 offset:18432\n"
    "ds_read_b128 a[76:79], v90 offset:19456\n"
    "v_pk_mul_f32 v[232:233], v[232:233], v[96:97] op_sel_hi:[1,0]\n"
    "v_pk_mul_f32 v[234:235], v[234:235], v[96:97] op_sel_hi:[1,0]\n"
    "v_cvt_pk_bf16_f32 v232, v232, v233\n"
    "v_cvt_pk_bf16_f32 v233, v234, v235\n"
    "v_pk_mul_f32 v[244:245], v[244:245], v[96:97] op_sel_hi:[1,0]\n"
    "v_pk_mul_f32 v[246:247], v[246:247], v[96:97] op_sel_hi:[1,0]\n"
    "v_cvt_pk_bf16_f32 v244, v244, v245\n"
    "v_cvt_pk_bf16_f32 v245, v246, v247\n"
    "v_accvgpr_read_b32 v8, a224\n"
    "v_accvgpr_read_b32 v9, a225\n"
    "v_accvgpr_read_b32 v10, a226\n"
    "v_accvgpr_read_b32 v11, a227\n"
    "v_pk_mul_f32 v[8:9], v[8:9], v[96:97] op_sel_hi:[1,0]\n"
    "v_pk_mul_f32 v[10:11], v[10:11], v[96:97] op_sel_hi:[1,0]\n"
    "v_cvt_pk_bf16_f32 v8, v8, v9\n"
    "v_cvt_pk_bf16_f32 v9, v10, v11\n"
    "v_accvgpr_read_b32 v12, a220\n"
    "v_accvgpr_read_b32 v13, a221\n"
    "v_accvgpr_read_b32 v14, a222\n"
    "v_accvgpr_read_b32 v15, a223\n"
    "v_pk_mul_f32 v[12:13], v[12:13], v[96:97] op_sel_hi:[1,0]\n"
    "v_pk_mul_f32 v[14:15], v[14:15], v[96:97] op_sel_hi:[1,0]\n"
    "v_cvt_pk_bf16_f32 v12, v12, v13\n"
    "v_cvt_pk_bf16_f32 v13, v14, v15\n"
    "s_waitcnt lgkmcnt(3)\n"
    "buffer_store_dwordx4 a[64:67], v89, s[36:39], s41 offen offset:256 nt\n"
    "s_waitcnt lgkmcnt(2)\n"
    "buffer_store_dwordx4 a[68:71], v89, s[36:39], s41 offen offset:2304 nt\n"
    "s_waitcnt lgkmcnt(1)\n"
    "buffer_store_dwordx4 a[72:75], v89, s[36:39], s89 offen offset:256 nt\n"
    "s_waitcnt lgkmcnt(0)\n"
    "buffer_store_dwordx4 a[76:79], v89, s[36:39], s89 offen offset:2304 nt\n"
    "ds_write_b64 v112, v[232:233] offset:20608\n"
    "ds_write_b64 v113, v[244:245] offset:20608\n"
    "ds_write_b64 v114, v[8:9] offset:20608\n"
    "ds_write_b64 v115, v[12:13] offset:20608\n"
    "ds_read_b128 a[80:83], v88 offset:20480\n"
    "ds_read_b128 a[84:87], v90 offset:21504\n"
    "ds_read_b128 a[88:91], v88 offset:22528\n"
    "ds_read_b128 a[92:95], v90 offset:23552\n"
    "s_waitcnt lgkmcnt(3)\n"
    "buffer_store_dwordx4 a[80:83], v89, s[36:39], s42 offen offset:256 nt\n"
    "s_waitcnt lgkmcnt(2)\n"
    "buffer_store_dwordx4 a[84:87], v89, s[36:39], s42 offen offset:2304 nt\n"
    "s_waitcnt lgkmcnt(1)\n"
    "buffer_store_dwordx4 a[88:91], v89, s[36:39], s90 offen offset:256 nt\n"
    "s_waitcnt lgkmcnt(0)\n"
    "buffer_store_dwordx4 a[92:95], v89, s[36:39], s90 offen offset:2304 nt\n"
    "s_branch FIN_%=\n"
    "FIN_%=:\n"
    "s_waitcnt vmcnt(0)\n"
    "s_branch END_%=\n"
    "STUBb4c0_%=:\n"
    "s_getpc_b64 s[18:19]\n"
    "s_branch RARE01_%=\n"
    "s_branch RETb4c0_%=\n"
    "STUBb5c0_%=:\n"
    "s_getpc_b64 s[18:19]\n"
    "s_branch RARE11_%=\n"
    "s_branch RETb5c0_%=\n"
    "STUBb6c0_%=:\n"
    "s_getpc_b64 s[18:19]\n"
    "s_branch RARE01_%=\n"
    "s_branch RETb6c0_%=\n"
    "STUBb7c0_%=:\n"
    "s_getpc_b64 s[18:19]\n"
    "s_branch RARE11_%=\n"
    "s_branch RETb7c0_%=\n"
    "STUBb8c0_%=:\n"
    "s_getpc_b64 s[18:19]\n"
    "s_branch RARE01_%=\n"
    "s_branch RETb8c0_%=\n"
    "STUBb9c0_%=:\n"
    "s_getpc_b64 s[18:19]\n"
    "s_branch RARE11_%=\n"
    "s_branch RETb9c0_%=\n"
    "STUBb10c0_%=:\n"
    "s_getpc_b64 s[18:19]\n"
    "s_branch RARE01_%=\n"
    "s_branch RETb10c0_%=\n"
    "STUBb11c0_%=:\n"
    "s_getpc_b64 s[18:19]\n"
    "s_branch RARE11_%=\n"
    "s_branch RETb11c0_%=\n"
    "STUBb12c0_%=:\n"
    "s_getpc_b64 s[18:19]\n"
    "s_branch RARE01_%=\n"
    "s_branch RETb12c0_%=\n"
    "STUBb13c0_%=:\n"
    "s_getpc_b64 s[18:19]\n"
    "s_branch RARE11_%=\n"
    "s_branch RETb13c0_%=\n"
    "STUBb14c0_%=:\n"
    "s_getpc_b64 s[18:19]\n"
    "s_branch RARE01_%=\n"
    "s_branch RETb14c0_%=\n"
    "STUBb15c0_%=:\n"
    "s_getpc_b64 s[18:19]\n"
    "s_branch RARE11_%=\n"
    "s_branch RETb15c0_%=\n"
    "STUBb16c0_%=:\n"
    "s_getpc_b64 s[18:19]\n"
    "s_branch RARE01_%=\n"
    "s_branch RETb16c0_%=\n"
    "STUBb17c0_%=:\n"
    "s_getpc_b64 s[18:19]\n"
    "s_branch RARE11_%=\n"
    "s_branch RETb17c0_%=\n"
    "STUBb18c0_%=:\n"
    "s_getpc_b64 s[18:19]\n"
    "s_branch RARE01_%=\n"
    "s_branch RETb18c0_%=\n"
    "STUBb19c0_%=:\n"
    "s_getpc_b64 s[18:19]\n"
    "s_branch RARE11_%=\n"
    "s_branch RETb19c0_%=\n"
    "STUBb20c0_%=:\n"
    "s_getpc_b64 s[18:19]\n"
    "s_branch RARE01_%=\n"
    "s_branch RETb20c0_%=\n"
    "STUBb21c0_%=:\n"
    "s_getpc_b64 s[18:19]\n"
    "s_branch RARE11_%=\n"
    "s_branch RETb21c0_%=\n"
    "STUBb22c0_%=:\n"
    "s_getpc_b64 s[18:19]\n"
    "s_branch RARE01_%=\n"
    "s_branch RETb22c0_%=\n"
    "STUBb23c0_%=:\n"
    "s_getpc_b64 s[18:19]\n"
    "s_branch RARE11_%=\n"
    "s_branch RETb23c0_%=\n"
    "STUBb1c0_%=:\n"
    "s_getpc_b64 s[18:19]\n"
    "s_branch RARE10_%=\n"
    "s_branch RETb1c0_%=\n"
    "STUBb2c0_%=:\n"
    "s_getpc_b64 s[18:19]\n"
    "s_branch RARE00_%=\n"
    "s_branch RETb2c0_%=\n"
    "STUBb3c0_%=:\n"
    "s_getpc_b64 s[18:19]\n"
    "s_branch RARE10_%=\n"
    "s_branch RETb3c0_%=\n"
    "STUBt23c0_%=:\n"
    "s_getpc_b64 s[18:19]\n"
    "s_branch RARE11_%=\n"
    "s_branch RETt23c0_%=\n"
    "RARE00_%=:\n"
    "s_nop 7\n"
    "s_nop 7\n"
    "v_max3_f32 v97, v0, v1, v2\n"
    "v_max3_f32 v97, v97, v3, 0\n"
    "v_max3_f32 v98, v4, v5, v6\n"
    "v_max3_f32 v98, v98, v7, 0\n"
    "v_max3_f32 v99, v8, v9, v10\n"
    "v_max3_f32 v99, v99, v11, 0\n"
    "v_mov_b32 v100, v97\n"
    "v_mov_b32 v101, v98\n"
    "v_mov_b32 v102, v99\n"
    "s_nop 1\n"
    "v_permlane32_swap_b32 v97, v100\n"
    "v_permlane32_swap_b32 v98, v101\n"
    "v_permlane32_swap_b32 v99, v102\n"
    "s_nop 1\n"
    "v_max_f32 v97, v97, v100\n"
    "v_max_f32 v98, v98, v101\n"
    "v_max_f32 v99, v99, v102\n"
    "v_mov_b32 v100, v97\n"
    "v_mov_b32 v101, v98\n"
    "v_mov_b32 v102, v99\n"
    "s_nop 1\n"
    "v_permlane16_swap_b32 v97, v100\n"
    "v_permlane16_swap_b32 v98, v101\n"
    "v_permlane16_swap_b32 v99, v102\n"
    "s_nop 1\n"
    "v_max_f32 v97, v97, v100\n"
    "v_max_f32 v98, v98, v101\n"
    "v_max_f32 v99, v99, v102\n"
    "v_ceil_f32 v97, v97\n"
    "v_exp_f32 v100, -v97\n"
    "v_ceil_f32 v98, v98\n"
    "v_exp_f32 v101, -v98\n"
    "v_ceil_f32 v99, v99\n"
    "v_exp_f32 v102, -v99\n"
    "v_cmp_lt_f32 vcc, 0, v97\n"
    "s_cbranch_vccz RSK000_%=\n"
    "v_sub_f32 v91, v91, v97\n"
    "v_sub_f32 v0, v0, v97\n"
    "v_sub_f32 v1, v1, v97\n"
    "v_sub_f32 v2, v2, v97\n"
    "v_sub_f32 v3, v3, v97\n"
    "v_lshlrev_b32 v106, 16, v24\n"
    "v_and_b32 v107, 0xffff0000, v24\n"
    "v_pk_mul_f32 v[106:107], v[106:107], v[100:101] op_sel_hi:[1,0]\n"
    "v_cvt_pk_bf16_f32 v24, v106, v107\n"
    "v_lshlrev_b32 v106, 16, v25\n"
    "v_and_b32 v107, 0xffff0000, v25\n"
    "v_pk_mul_f32 v[106:107], v[106:107], v[100:101] op_sel_hi:[1,0]\n"
    "v_cvt_pk_bf16_f32 v25, v106, v107\n"
    "v_lshlrev_b32 v106, 16, v26\n"
    "v_and_b32 v107, 0xffff0000, v26\n"
    "v_pk_mul_f32 v[106:107], v[106:107], v[100:101] op_sel_hi:[1,0]\n"
    "v_cvt_pk_bf16_f32 v26, v106, v107\n"
    "v_lshlrev_b32 v106, 16, v27\n"
    "v_and_b32 v107, 0xffff0000, v27\n"
    "v_pk_mul_f32 v[106:107], v[106:107], v[100:101] op_sel_hi:[1,0]\n"
    "v_cvt_pk_bf16_f32 v27, v106, v107\n"
    "v_lshlrev_b32 v106, 16, v28\n"
    "v_and_b32 v107, 0xffff0000, v28\n"
    "v_pk_mul_f32 v[106:107], v[106:107], v[100:101] op_sel_hi:[1,0]\n"
    "v_cvt_pk_bf16_f32 v28, v106, v107\n"
    "v_lshlrev_b32 v106, 16, v29\n"
    "v_and_b32 v107, 0xffff0000, v29\n"
    "v_pk_mul_f32 v[106:107], v[106:107], v[100:101] op_sel_hi:[1,0]\n"
    "v_cvt_pk_bf16_f32 v29, v106, v107\n"
    "v_lshlrev_b32 v106, 16, v30\n"
    "v_and_b32 v107, 0xffff0000, v30\n"
    "v_pk_mul_f32 v[106:107], v[106:107], v[100:101] op_sel_hi:[1,0]\n"
    "v_cvt_pk_bf16_f32 v30, v106, v107\n"
    "v_lshlrev_b32 v106, 16, v31\n"
    "v_and_b32 v107, 0xffff0000, v31\n"
    "v_pk_mul_f32 v[106:107], v[106:107], v[100:101] op_sel_hi:[1,0]\n"
    "v_cvt_pk_bf16_f32 v31, v106, v107\n"
    "RSK000_%=:\n"
    "v_cmp_lt_f32 vcc, 0, v98\n"
    "s_cbranch_vccz RSK001_%=\n"
    "v_sub_f32 v92, v92, v98\n"
    "v_sub_f32 v4, v4, v98\n"
    "v_sub_f32 v5, v5, v98\n"
    "v_sub_f32 v6, v6, v98\n"
    "v_sub_f32 v7, v7, v98\n"
    "v_lshlrev_b32 v106, 16, v32\n"
    "v_and_b32 v107, 0xffff0000, v32\n"
    "v_pk_mul_f32 v[106:107], v[106:107], v[100:101] op_sel:[0,1] op_sel_hi:[1,1]\n"
    "v_cvt_pk_bf16_f32 v32, v106, v107\n"
    "v_lshlrev_b32 v106, 16, v33\n"
    "v_and_b32 v107, 0xffff0000, v33\n"
    "v_pk_mul_f32 v[106:107], v[106:107], v[100:101] op_sel:[0,1] op_sel_hi:[1,1]\n"
    "v_cvt_pk_bf16_f32 v33, v106, v107\n"
    "v_lshlrev_b32 v106, 16, v34\n"
    "v_and_b32 v107, 0xffff0000, v34\n"
    "v_pk_mul_f32 v[106:107], v[106:107], v[100:101] op_sel:[0,1] op_sel_hi:[1,1]\n"
    "v_cvt_pk_bf16_f32 v34, v106, v107\n"
    "v_lshlrev_b32 v106, 16, v35\n"
    "v_and_b32 v107, 0xffff0000, v35\n"
    "v_pk_mul_f32 v[106:107], v[106:107], v[100:101] op_sel:[0,1] op_sel_hi:[1,1]\n"
    "v_cvt_pk_bf16_f32 v35, v106, v107\n"
    "v_lshlrev_b32 v106, 16, v36\n"
    "v_and_b32 v107, 0xffff0000, v36\n"
    "v_pk_mul_f32 v[106:107], v[106:107], v[100:101] op_sel:[0,1] op_sel_hi:[1,1]\n"
    "v_cvt_pk_bf16_f32 v36, v106, v107\n"
    "v_lshlrev_b32 v106, 16, v37\n"
    "v_and_b32 v107, 0xffff0000, v37\n"
    "v_pk_mul_f32 v[106:107], v[106:107], v[100:101] op_sel:[0,1] op_sel_hi:[1,1]\n"
    "v_cvt_pk_bf16_f32 v37, v106, v107\n"
    "v_lshlrev_b32 v106, 16, v38\n"
    "v_and_b32 v107, 0xffff0000, v38\n"
    "v_pk_mul_f32 v[106:107], v[106:107], v[100:101] op_sel:[0,1] op_sel_hi:[1,1]\n"
    "v_cvt_pk_bf16_f32 v38, v106, v107\n"
    "v_lshlrev_b32 v106, 16, v39\n"
    "v_and_b32 v107, 0xffff0000, v39\n"
    "v_pk_mul_f32 v[106:107], v[106:107], v[100:101] op_sel:[0,1] op_sel_hi:[1,1]\n"
    "v_cvt_pk_bf16_f32 v39, v106, v107\n"
    "RSK001_%=:\n"
    "v_cmp_lt_f32 vcc, 0, v99\n"
    "s_cbranch_vccz RSK002_%=\n"
    "v_sub_f32 v93, v93, v99\n"
    "v_sub_f32 v8, v8, v99\n"
    "v_sub_f32 v9, v9, v99\n"
    "v_sub_f32 v10, v10, v99\n"
    "v_sub_f32 v11, v11, v99\n"
    "v_lshlrev_b32 v106, 16, v40\n"
    "v_and_b32 v107, 0xffff0000, v40\n"
    "v_pk_mul_f32 v[106:107], v[106:107], v[102:103] op_sel_hi:[1,0]\n"
    "v_cvt_pk_bf16_f32 v40, v106, v107\n"
    "v_lshlrev_b32 v106, 16, v41\n"
    "v_and_b32 v107, 0xffff0000, v41\n"
    "v_pk_mul_f32 v[106:107], v[106:107], v[102:103] op_sel_hi:[1,0]\n"
    "v_cvt_pk_bf16_f32 v41, v106, v107\n"
    "v_lshlrev_b32 v106, 16, v42\n"
    "v_and_b32 v107, 0xffff0000, v42\n"
    "v_pk_mul_f32 v[106:107], v[106:107], v[102:103] op_sel_hi:[1,0]\n"
    "v_cvt_pk_bf16_f32 v42, v106, v107\n"
    "v_lshlrev_b32 v106, 16, v43\n"
    "v_and_b32 v107, 0xffff0000, v43\n"
    "v_pk_mul_f32 v[106:107], v[106:107], v[102:103] op_sel_hi:[1,0]\n"
    "v_cvt_pk_bf16_f32 v43, v106, v107\n"
    "v_lshlrev_b32 v106, 16, v44\n"
    "v_and_b32 v107, 0xffff0000, v44\n"
    "v_pk_mul_f32 v[106:107], v[106:107], v[102:103] op_sel_hi:[1,0]\n"
    "v_cvt_pk_bf16_f32 v44, v106, v107\n"
    "v_lshlrev_b32 v106, 16, v45\n"
    "v_and_b32 v107, 0xffff0000, v45\n"
    "v_pk_mul_f32 v[106:107], v[106:107], v[102:103] op_sel_hi:[1,0]\n"
    "v_cvt_pk_bf16_f32 v45, v106, v107\n"
    "v_lshlrev_b32 v106, 16, v46\n"
    "v_and_b32 v107, 0xffff0000, v46\n"
    "v_pk_mul_f32 v[106:107], v[106:107], v[102:103] op_sel_hi:[1,0]\n"
    "v_cvt_pk_bf16_f32 v46, v106, v107\n"
    "v_lshlrev_b32 v106, 16, v47\n"
    "v_and_b32 v107, 0xffff0000, v47\n"
    "v_pk_mul_f32 v[106:107], v[106:107], v[102:103] op_sel_hi:[1,0]\n"
    "v_cvt_pk_bf16_f32 v47, v106, v107\n"
    "RSK002_%=:\n"
    "s_add_u32 s18, s18, 4\n"
    "s_addc_u32 s19, s19, 0\n"
    "s_setpc_b64 s[18:19]\n"
    "RARE01_%=:\n"
    "s_nop 7\n"
    "s_nop 7\n"
    "v_max3_f32 v97, v0, v1, v2\n"
    "v_max3_f32 v97, v97, v3, 0\n"
    "v_max3_f32 v98, v4, v5, v6\n"
    "v_max3_f32 v98, v98, v7, 0\n"
    "v_max3_f32 v99, v8, v9, v10\n"
    "v_max3_f32 v99, v99, v11, 0\n"
    "v_mov_b32 v100, v97\n"
    "v_mov_b32 v101, v98\n"
    "v_mov_b32 v102, v99\n"
    "s_nop 1\n"
    "v_permlane32_swap_b32 v97, v100\n"
    "v_permlane32_swap_b32 v98, v101\n"
    "v_permlane32_swap_b32 v99, v102\n"
    "s_nop 1\n"
    "v_max_f32 v97, v97, v100\n"
    "v_max_f32 v98, v98, v101\n"
    "v_max_f32 v99, v99, v102\n"
    "v_mov_b32 v100, v97\n"
    "v_mov_b32 v101, v98\n"
    "v_mov_b32 v102, v99\n"
    "s_nop 1\n"
    "v_permlane16_swap_b32 v97, v100\n"
    "v_permlane16_swap_b32 v98, v101\n"
    "v_permlane16_swap_b32 v99, v102\n"
    "s_nop 1\n"
    "v_max_f32 v97, v97, v100\n"
    "v_max_f32 v98, v98, v101\n"
    "v_max_f32 v99, v99, v102\n"
    "v_ceil_f32 v97, v97\n"
    "v_exp_f32 v100, -v97\n"
    "v_ceil_f32 v98, v98\n"
    "v_exp_f32 v101, -v98\n"
    "v_ceil_f32 v99, v99\n"
    "v_exp_f32 v102, -v99\n"
    "v_cmp_lt_f32 vcc, 0, v97\n"
    "s_cbranch_vccz RSK010_%=\n"
    "v_sub_f32 v91, v91, v97\n"
    "v_sub_f32 v0, v0, v97\n"
    "v_sub_f32 v1, v1, v97\n"
    "v_sub_f32 v2, v2, v97\n"
    "v_sub_f32 v3, v3, v97\n"
    "v_lshlrev_b32 v106, 16, v24\n"
    "v_and_b32 v107, 0xffff0000, v24\n"
    "v_pk_mul_f32 v[106:107], v[106:107], v[100:101] op_sel_hi:[1,0]\n"
    "v_cvt_pk_bf16_f32 v24, v106, v107\n"
    "v_lshlrev_b32 v106, 16, v25\n"
    "v_and_b32 v107, 0xffff0000, v25\n"
    "v_pk_mul_f32 v[106:107], v[106:107], v[100:101] op_sel_hi:[1,0]\n"
    "v_cvt_pk_bf16_f32 v25, v106, v107\n"
    "v_lshlrev_b32 v106, 16, v26\n"
    "v_and_b32 v107, 0xffff0000, v26\n"
    "v_pk_mul_f32 v[106:107], v[106:107], v[100:101] op_sel_hi:[1,0]\n"
    "v_cvt_pk_bf16_f32 v26, v106, v107\n"
    "v_lshlrev_b32 v106, 16, v27\n"
    "v_and_b32 v107, 0xffff0000, v27\n"
    "v_pk_mul_f32 v[106:107], v[106:107], v[100:101] op_sel_hi:[1,0]\n"
    "v_cvt_pk_bf16_f32 v27, v106, v107\n"
    "v_lshlrev_b32 v106, 16, v28\n"
    "v_and_b32 v107, 0xffff0000, v28\n"
    "v_pk_mul_f32 v[106:107], v[106:107], v[100:101] op_sel_hi:[1,0]\n"
    "v_cvt_pk_bf16_f32 v28, v106, v107\n"
    "v_lshlrev_b32 v106, 16, v29\n"
    "v_and_b32 v107, 0xffff0000, v29\n"
    "v_pk_mul_f32 v[106:107], v[106:107], v[100:101] op_sel_hi:[1,0]\n"
    "v_cvt_pk_bf16_f32 v29, v106, v107\n"
    "v_lshlrev_b32 v106, 16, v30\n"
    "v_and_b32 v107, 0xffff0000, v30\n"
    "v_pk_mul_f32 v[106:107], v[106:107], v[100:101] op_sel_hi:[1,0]\n"
    "v_cvt_pk_bf16_f32 v30, v106, v107\n"
    "v_lshlrev_b32 v106, 16, v31\n"
    "v_and_b32 v107, 0xffff0000, v31\n"
    "v_pk_mul_f32 v[106:107], v[106:107], v[100:101] op_sel_hi:[1,0]\n"
    "v_cvt_pk_bf16_f32 v31, v106, v107\n"
    "v_lshlrev_b32 v106, 16, v48\n"
    "v_and_b32 v107, 0xffff0000, v48\n"
    "v_pk_mul_f32 v[106:107], v[106:107], v[100:101] op_sel_hi:[1,0]\n"
    "v_cvt_pk_bf16_f32 v48, v106, v107\n"
    "v_lshlrev_b32 v106, 16, v49\n"
    "v_and_b32 v107, 0xffff0000, v49\n"
    "v_pk_mul_f32 v[106:107], v[106:107], v[100:101] op_sel_hi:[1,0]\n"
    "v_cvt_pk_bf16_f32 v49, v106, v107\n"
    "v_lshlrev_b32 v106, 16, v50\n"
    "v_and_b32 v107, 0xffff0000, v50\n"
    "v_pk_mul_f32 v[106:107], v[106:107], v[100:101] op_sel_hi:[1,0]\n"
    "v_cvt_pk_bf16_f32 v50, v106, v107\n"
    "v_lshlrev_b32 v106, 16, v51\n"
    "v_and_b32 v107, 0xffff0000, v51\n"
    "v_pk_mul_f32 v[106:107], v[106:107], v[100:101] op_sel_hi:[1,0]\n"
    "v_cvt_pk_bf16_f32 v51, v106, v107\n"
    "v_lshlrev_b32 v106, 16, v52\n"
    "v_and_b32 v107, 0xffff0000, v52\n"
    "v_pk_mul_f32 v[106:107], v[106:107], v[100:101] op_sel_hi:[1,0]\n"
    "v_cvt_pk_bf16_f32 v52, v106, v107\n"
    "v_lshlrev_b32 v106, 16, v53\n"
    "v_and_b32 v107, 0xffff0000, v53\n"
    "v_pk_mul_f32 v[106:107], v[106:107], v[100:101] op_sel_hi:[1,0]\n"
    "v_cvt_pk_bf16_f32 v53, v106, v107\n"
    "v_lshlrev_b32 v106, 16, v54\n"
    "v_and_b32 v107, 0xffff0000, v54\n"
    "v_pk_mul_f32 v[106:107], v[106:107], v[100:101] op_sel_hi:[1,0]\n"
    "v_cvt_pk_bf16_f32 v54, v106, v107\n"
    "v_lshlrev_b32 v106, 16, v55\n"
    "v_and_b32 v107, 0xffff0000, v55\n"
    "v_pk_mul_f32 v[106:107], v[106:107], v[100:101] op_sel_hi:[1,0]\n"
    "v_cvt_pk_bf16_f32 v55, v106, v107\n"
    "v_accvgpr_read_b32 v106, a164\n"
    "v_accvgpr_read_b32 v107, a165\n"
    "v_pk_mul_f32 v[106:107], v[106:107], v[100:101] op_sel_hi:[1,0]\n"
    "v_accvgpr_write_b32 a164, v106\n"
    "v_accvgpr_write_b32 a165, v107\n"
    "v_accvgpr_read_b32 v106, a166\n"
    "v_accvgpr_read_b32 v107, a167\n"
    "v_pk_mul_f32 v[106:107], v[106:107], v[100:101] op_sel_hi:[1,0]\n"
    "v_accvgpr_write_b32 a166, v106\n"
    "v_accvgpr_write_b32 a167, v107\n"
    "v_pk_mul_f32 v[116:117], v[116:117], v[100:101] op_sel_hi:[1,0]\n"
    "v_pk_mul_f32 v[118:119], v[118:119], v[100:101] op_sel_hi:[1,0]\n"
    "v_pk_mul_f32 v[128:129], v[128:129], v[100:101] op_sel_hi:[1,0]\n"
    "v_pk_mul_f32 v[130:131], v[130:131], v[100:101] op_sel_hi:[1,0]\n"
    "v_pk_mul_f32 v[140:141], v[140:141], v[100:101] op_sel_hi:[1,0]\n"
    "v_pk_mul_f32 v[142:143], v[142:143], v[100:101] op_sel_hi:[1,0]\n"
    "v_accvgpr_read_b32 v106, a176\n"
    "v_accvgpr_read_b32 v107, a177\n"
    "v_pk_mul_f32 v[106:107], v[106:107], v[100:101] op_sel_hi:[1,0]\n"
    "v_accvgpr_write_b32 a176, v106\n"
    "v_accvgpr_write_b32 a177, v107\n"
    "v_accvgpr_read_b32 v106, a178\n"
    "v_accvgpr_read_b32 v107, a179\n"
    "v_pk_mul_f32 v[106:107], v[106:107], v[100:101] op_sel_hi:[1,0]\n"
    "v_accvgpr_write_b32 a178, v106\n"
    "v_accvgpr_write_b32 a179, v107\n"
    "v_pk_mul_f32 v[152:153], v[152:153], v[100:101] op_sel_hi:[1,0]\n"
    "v_pk_mul_f32 v[154:155], v[154:155], v[100:101] op_sel_hi:[1,0]\n"
    "v_pk_mul_f32 v[164:165], v[164:165], v[100:101] op_sel_hi:[1,0]\n"
    "v_pk_mul_f32 v[166:167], v[166:167], v[100:101] op_sel_hi:[1,0]\n"
    "v_pk_mul_f32 v[176:177], v[176:177], v[100:101] op_sel_hi:[1,0]\n"
    "v_pk_mul_f32 v[178:179], v[178:179], v[100:101] op_sel_hi:[1,0]\n"
    "v_accvgpr_read_b32 v106, a188\n"
    "v_accvgpr_read_b32 v107, a189\n"
    "v_pk_mul_f32 v[106:107], v[106:107], v[100:101] op_sel_hi:[1,0]\n"
    "v_accvgpr_write_b32 a188, v106\n"
    "v_accvgpr_write_b32 a189, v107\n"
    "v_accvgpr_read_b32 v106, a190\n"
    "v_accvgpr_read_b32 v107, a191\n"
    "v_pk_mul_f32 v[106:107], v[106:107], v[100:101] op_sel_hi:[1,0]\n"
    "v_accvgpr_write_b32 a190, v106\n"
    "v_accvgpr_write_b32 a191, v107\n"
    "v_pk_mul_f32 v[188:189], v[188:189], v[100:101] op_sel_hi:[1,0]\n"
    "v_pk_mul_f32 v[190:191], v[190:191], v[100:101] op_sel_hi:[1,0]\n"
    "v_pk_mul_f32 v[200:201], v[200:201], v[100:101] op_sel_hi:[1,0]\n"
    "v_pk_mul_f32 v[202:203], v[202:203], v[100:101] op_sel_hi:[1,0]\n"
    "v_pk_mul_f32 v[212:213], v[212:213], v[100:101] op_sel_hi:[1,0]\n"
    "v_pk_mul_f32 v[214:215], v[214:215], v[100:101] op_sel_hi:[1,0]\n"
    "v_accvgpr_read_b32 v106, a200\n"
    "v_accvgpr_read_b32 v107, a201\n"
    "v_pk_mul_f32 v[106:107], v[106:107], v[100:101] op_sel_hi:[1,0]\n"
    "v_accvgpr_write_b32 a200, v106\n"
    "v_accvgpr_write_b32 a201, v107\n"
    "v_accvgpr_read_b32 v106, a202\n"
    "v_accvgpr_read_b32 v107, a203\n"
    "v_pk_mul_f32 v[106:107], v[106:107], v[100:101] op_sel_hi:[1,0]\n"
    "v_accvgpr_write_b32 a202, v106\n"
    "v_accvgpr_write_b32 a203, v107\n"
    "v_pk_mul_f32 v[224:225], v[224:225], v[100:101] op_sel_hi:[1,0]\n"
    "v_pk_mul_f32 v[226:227], v[226:227], v[100:101] op_sel_hi:[1,0]\n"
    "v_pk_mul_f32 v[236:237], v[236:237], v[100:101] op_sel_hi:[1,0]\n"
    "v_pk_mul_f32 v[238:239], v[238:239], v[100:101] op_sel_hi:[1,0]\n"
    "v_pk_mul_f32 v[248:249], v[248:249], v[100:101] op_sel_hi:[1,0]\n"
    "v_pk_mul_f32 v[250:251], v[250:251], v[100:101] op_sel_hi:[1,0]\n"
    "v_accvgpr_read_b32 v106, a212\n"
    "v_accvgpr_read_b32 v107, a213\n"
    "v_pk_mul_f32 v[106:107], v[106:107], v[100:101] op_sel_hi:[1,0]\n"
    "v_accvgpr_write_b32 a212, v106\n"
    "v_accvgpr_write_b32 a213, v107\n"
    "v_accvgpr_read_b32 v106, a214\n"
    "v_accvgpr_read_b32 v107, a215\n"
    "v_pk_mul_f32 v[106:107], v[106:107], v[100:101] op_sel_hi:[1,0]\n"
    "v_accvgpr_write_b32 a214, v106\n"
    "v_accvgpr_write_b32 a215, v107\n"
    "RSK010_%=:\n"
    "v_cmp_lt_f32 vcc, 0, v98\n"
    "s_cbranch_vccz RSK011_%=\n"
    "v_sub_f32 v92, v92, v98\n"
    "v_sub_f32 v4, v4, v98\n"
    "v_sub_f32 v5, v5, v98\n"
    "v_sub_f32 v6, v6, v98\n"
    "v_sub_f32 v7, v7, v98\n"
    "v_lshlrev_b32 v106, 16, v32\n"
    "v_and_b32 v107, 0xffff0000, v32\n"
    "v_pk_mul_f32 v[106:107], v[106:107], v[100:101] op_sel:[0,1] op_sel_hi:[1,1]\n"
    "v_cvt_pk_bf16_f32 v32, v106, v107\n"
    "v_lshlrev_b32 v106, 16, v33\n"
    "v_and_b32 v107, 0xffff0000, v33\n"
    "v_pk_mul_f32 v[106:107], v[106:107], v[100:101] op_sel:[0,1] op_sel_hi:[1,1]\n"
    "v_cvt_pk_bf16_f32 v33, v106, v107\n"
    "v_lshlrev_b32 v106, 16, v34\n"
    "v_and_b32 v107, 0xffff0000, v34\n"
    "v_pk_mul_f32 v[106:107], v[106:107], v[100:101] op_sel:[0,1] op_sel_hi:[1,1]\n"
    "v_cvt_pk_bf16_f32 v34, v106, v107\n"
    "v_lshlrev_b32 v106, 16, v35\n"
    "v_and_b32 v107, 0xffff0000, v35\n"
    "v_pk_mul_f32 v[106:107], v[106:107], v[100:101] op_sel:[0,1] op_sel_hi:[1,1]\n"
    "v_cvt_pk_bf16_f32 v35, v106, v107\n"
    "v_lshlrev_b32 v106, 16, v36\n"
    "v_and_b32 v107, 0xffff0000, v36\n"
    "v_pk_mul_f32 v[106:107], v[106:107], v[100:101] op_sel:[0,1] op_sel_hi:[1,1]\n"
    "v_cvt_pk_bf16_f32 v36, v106, v107\n"
    "v_lshlrev_b32 v106, 16, v37\n"
    "v_and_b32 v107, 0xffff0000, v37\n"
    "v_pk_mul_f32 v[106:107], v[106:107], v[100:101] op_sel:[0,1] op_sel_hi:[1,1]\n"
    "v_cvt_pk_bf16_f32 v37, v106, v107\n"
    "v_lshlrev_b32 v106, 16, v38\n"
    "v_and_b32 v107, 0xffff0000, v38\n"
    "v_pk_mul_f32 v[106:107], v[106:107], v[100:101] op_sel:[0,1] op_sel_hi:[1,1]\n"
    "v_cvt_pk_bf16_f32 v38, v106, v107\n"
    "v_lshlrev_b32 v106, 16, v39\n"
    "v_and_b32 v107, 0xffff0000, v39\n"
    "v_pk_mul_f32 v[106:107], v[106:107], v[100:101] op_sel:[0,1] op_sel_hi:[1,1]\n"
    "v_cvt_pk_bf16_f32 v39, v106, v107\n"
    "v_lshlrev_b32 v106, 16, v56\n"
    "v_and_b32 v107, 0xffff0000, v56\n"
    "v_pk_mul_f32 v[106:107], v[106:107], v[100:101] op_sel:[0,1] op_sel_hi:[1,1]\n"
    "v_cvt_pk_bf16_f32 v56, v106, v107\n"
    "v_lshlrev_b32 v106, 16, v57\n"
    "v_and_b32 v107, 0xffff0000, v57\n"
    "v_pk_mul_f32 v[106:107], v[106:107], v[100:101] op_sel:[0,1] op_sel_hi:[1,1]\n"
    "v_cvt_pk_bf16_f32 v57, v106, v107\n"
    "v_lshlrev_b32 v106, 16, v58\n"
    "v_and_b32 v107, 0xffff0000, v58\n"
    "v_pk_mul_f32 v[106:107], v[106:107], v[100:101] op_sel:[0,1] op_sel_hi:[1,1]\n"
    "v_cvt_pk_bf16_f32 v58, v106, v107\n"
    "v_lshlrev_b32 v106, 16, v59\n"
    "v_and_b32 v107, 0xffff0000, v59\n"
    "v_pk_mul_f32 v[106:107], v[106:107], v[100:101] op_sel:[0,1] op_sel_hi:[1,1]\n"
    "v_cvt_pk_bf16_f32 v59, v106, v107\n"
    "v_lshlrev_b32 v106, 16, v60\n"
    "v_and_b32 v107, 0xffff0000, v60\n"
    "v_pk_mul_f32 v[106:107], v[106:107], v[100:101] op_sel:[0,1] op_sel_hi:[1,1]\n"
    "v_cvt_pk_bf16_f32 v60, v106, v107\n"
    "v_lshlrev_b32 v106, 16, v61\n"
    "v_and_b32 v107, 0xffff0000, v61\n"
    "v_pk_mul_f32 v[106:107], v[106:107], v[100:101] op_sel:[0,1] op_sel_hi:[1,1]\n"
    "v_cvt_pk_bf16_f32 v61, v106, v107\n"
    "v_lshlrev_b32 v106, 16, v62\n"
    "v_and_b32 v107, 0xffff0000, v62\n"
    "v_pk_mul_f32 v[106:107], v[106:107], v[100:101] op_sel:[0,1] op_sel_hi:[1,1]\n"
    "v_cvt_pk_bf16_f32 v62, v106, v107\n"
    "v_lshlrev_b32 v106, 16, v63\n"
    "v_and_b32 v107, 0xffff0000, v63\n"
    "v_pk_mul_f32 v[106:107], v[106:107], v[100:101] op_sel:[0,1] op_sel_hi:[1,1]\n"
    "v_cvt_pk_bf16_f32 v63, v106, v107\n"
    "v_accvgpr_read_b32 v106, a168\n"
    "v_accvgpr_read_b32 v107, a169\n"
    "v_pk_mul_f32 v[106:107], v[106:107], v[100:101] op_sel:[0,1] op_sel_hi:[1,1]\n"
    "v_accvgpr_write_b32 a168, v106\n"
    "v_accvgpr_write_b32 a169, v107\n"
    "v_accvgpr_read_b32 v106, a170\n"
    "v_accvgpr_read_b32 v107, a171\n"
    "v_pk_mul_f32 v[106:107], v[106:107], v[100:101] op_sel:[0,1] op_sel_hi:[1,1]\n"
    "v_accvgpr_write_b32 a170, v106\n"
    "v_accvgpr_write_b32 a171, v107\n"
    "v_pk_mul_f32 v[120:121], v[120:121], v[100:101] op_sel:[0,1] op_sel_hi:[1,1]\n"
    "v_pk_mul_f32 v[122:123], v[122:123], v[100:101] op_sel:[0,1] op_sel_hi:[1,1]\n"
    "v_pk_mul_f32 v[132:133], v[132:133], v[100:101] op_sel:[0,1] op_sel_hi:[1,1]\n"
    "v_pk_mul_f32 v[134:135], v[134:135], v[100:101] op_sel:[0,1] op_sel_hi:[1,1]\n"
    "v_pk_mul_f32 v[144:145], v[144:145], v[100:101] op_sel:[0,1] op_sel_hi:[1,1]\n"
    "v_pk_mul_f32 v[146:147], v[146:147], v[100:101] op_sel:[0,1] op_sel_hi:[1,1]\n"
    "v_accvgpr_read_b32 v106, a180\n"
    "v_accvgpr_read_b32 v107, a181\n"
    "v_pk_mul_f32 v[106:107], v[106:107], v[100:101] op_sel:[0,1] op_sel_hi:[1,1]\n"
    "v_accvgpr_write_b32 a180, v106\n"
    "v_accvgpr_write_b32 a181, v107\n"
    "v_accvgpr_read_b32 v106, a182\n"
    "v_accvgpr_read_b32 v107, a183\n"
    "v_pk_mul_f32 v[106:107], v[106:107], v[100:101] op_sel:[0,1] op_sel_hi:[1,1]\n"
    "v_accvgpr_write_b32 a182, v106\n"
    "v_accvgpr_write_b32 a183, v107\n"
    "v_pk_mul_f32 v[156:157], v[156:157], v[100:101] op_sel:[0,1] op_sel_hi:[1,1]\n"
    "v_pk_mul_f32 v[158:159], v[158:159], v[100:101] op_sel:[0,1] op_sel_hi:[1,1]\n"
    "v_pk_mul_f32 v[168:169], v[168:169], v[100:101] op_sel:[0,1] op_sel_hi:[1,1]\n"
    "v_pk_mul_f32 v[170:171], v[170:171], v[100:101] op_sel:[0,1] op_sel_hi:[1,1]\n"
    "v_pk_mul_f32 v[180:181], v[180:181], v[100:101] op_sel:[0,1] op_sel_hi:[1,1]\n"
    "v_pk_mul_f32 v[182:183], v[182:183], v[100:101] op_sel:[0,1] op_sel_hi:[1,1]\n"
    "v_accvgpr_read_b32 v106, a192\n"
    "v_accvgpr_read_b32 v107, a193\n"
    "v_pk_mul_f32 v[106:107], v[106:107], v[100:101] op_sel:[0,1] op_sel_hi:[1,1]\n"
    "v_accvgpr_write_b32 a192, v106\n"
    "v_accvgpr_write_b32 a193, v107\n"
    "v_accvgpr_read_b32 v106, a194\n"
    "v_accvgpr_read_b32 v107, a195\n"
    "v_pk_mul_f32 v[106:107], v[106:107], v[100:101] op_sel:[0,1] op_sel_hi:[1,1]\n"
    "v_accvgpr_write_b32 a194, v106\n"
    "v_accvgpr_write_b32 a195, v107\n"
    "v_pk_mul_f32 v[192:193], v[192:193], v[100:101] op_sel:[0,1] op_sel_hi:[1,1]\n"
    "v_pk_mul_f32 v[194:195], v[194:195], v[100:101] op_sel:[0,1] op_sel_hi:[1,1]\n"
    "v_pk_mul_f32 v[204:205], v[204:205], v[100:101] op_sel:[0,1] op_sel_hi:[1,1]\n"
    "v_pk_mul_f32 v[206:207], v[206:207], v[100:101] op_sel:[0,1] op_sel_hi:[1,1]\n"
    "v_pk_mul_f32 v[216:217], v[216:217], v[100:101] op_sel:[0,1] op_sel_hi:[1,1]\n"
    "v_pk_mul_f32 v[218:219], v[218:219], v[100:101] op_sel:[0,1] op_sel_hi:[1,1]\n"
    "v_accvgpr_read_b32 v106, a204\n"
    "v_accvgpr_read_b32 v107, a205\n"
    "v_pk_mul_f32 v[106:107], v[106:107], v[100:101] op_sel:[0,1] op_sel_hi:[1,1]\n"
    "v_accvgpr_write_b32 a204, v106\n"
    "v_accvgpr_write_b32 a205, v107\n"
    "v_accvgpr_read_b32 v106, a206\n"
    "v_accvgpr_read_b32 v107, a207\n"
    "v_pk_mul_f32 v[106:107], v[106:107], v[100:101] op_sel:[0,1] op_sel_hi:[1,1]\n"
    "v_accvgpr_write_b32 a206, v106\n"
    "v_accvgpr_write_b32 a207, v107\n"
    "v_pk_mul_f32 v[228:229], v[228:229], v[100:101] op_sel:[0,1] op_sel_hi:[1,1]\n"
    "v_pk_mul_f32 v[230:231], v[230:231], v[100:101] op_sel:[0,1] op_sel_hi:[1,1]\n"
    "v_pk_mul_f32 v[240:241], v[240:241], v[100:101] op_sel:[0,1] op_sel_hi:[1,1]\n"
    "v_pk_mul_f32 v[242:243], v[242:243], v[100:101] op_sel:[0,1] op_sel_hi:[1,1]\n"
    "v_pk_mul_f32 v[252:253], v[252:253], v[100:101] op_sel:[0,1] op_sel_hi:[1,1]\n"
    "v_pk_mul_f32 v[254:255], v[254:255], v[100:101] op_sel:[0,1] op_sel_hi:[1,1]\n"
    "v_accvgpr_read_b32 v106, a216\n"
    "v_accvgpr_read_b32 v107, a217\n"
    "v_pk_mul_f32 v[106:107], v[106:107], v[100:101] op_sel:[0,1] op_sel_hi:[1,1]\n"
    "v_accvgpr_write_b32 a216, v106\n"
    "v_accvgpr_write_b32 a217, v107\n"
    "v_accvgpr_read_b32 v106, a218\n"
    "v_accvgpr_read_b32 v107, a219\n"
    "v_pk_mul_f32 v[106:107], v[106:107], v[100:101] op_sel:[0,1] op_sel_hi:[1,1]\n"
    "v_accvgpr_write_b32 a218, v106\n"
    "v_accvgpr_write_b32 a219, v107\n"
    "RSK011_%=:\n"
    "v_cmp_lt_f32 vcc, 0, v99\n"
    "s_cbranch_vccz RSK012_%=\n"
    "v_sub_f32 v93, v93, v99\n"
    "v_sub_f32 v8, v8, v99\n"
    "v_sub_f32 v9, v9, v99\n"
    "v_sub_f32 v10, v10, v99\n"
    "v_sub_f32 v11, v11, v99\n"
    "v_lshlrev_b32 v106, 16, v40\n"
    "v_and_b32 v107, 0xffff0000, v40\n"
    "v_pk_mul_f32 v[106:107], v[106:107], v[102:103] op_sel_hi:[1,0]\n"
    "v_cvt_pk_bf16_f32 v40, v106, v107\n"
    "v_lshlrev_b32 v106, 16, v41\n"
    "v_and_b32 v107, 0xffff0000, v41\n"
    "v_pk_mul_f32 v[106:107], v[106:107], v[102:103] op_sel_hi:[1,0]\n"
    "v_cvt_pk_bf16_f32 v41, v106, v107\n"
    "v_lshlrev_b32 v106, 16, v42\n"
    "v_and_b32 v107, 0xffff0000, v42\n"
    "v_pk_mul_f32 v[106:107], v[106:107], v[102:103] op_sel_hi:[1,0]\n"
    "v_cvt_pk_bf16_f32 v42, v106, v107\n"
    "v_lshlrev_b32 v106, 16, v43\n"
    "v_and_b32 v107, 0xffff0000, v43\n"
    "v_pk_mul_f32 v[106:107], v[106:107], v[102:103] op_sel_hi:[1,0]\n"
    "v_cvt_pk_bf16_f32 v43, v106, v107\n"
    "v_lshlrev_b32 v106, 16, v44\n"
    "v_and_b32 v107, 0xffff0000, v44\n"
    "v_pk_mul_f32 v[106:107], v[106:107], v[102:103] op_sel_hi:[1,0]\n"
    "v_cvt_pk_bf16_f32 v44, v106, v107\n"
    "v_lshlrev_b32 v106, 16, v45\n"
    "v_and_b32 v107, 0xffff0000, v45\n"
    "v_pk_mul_f32 v[106:107], v[106:107], v[102:103] op_sel_hi:[1,0]\n"
    "v_cvt_pk_bf16_f32 v45, v106, v107\n"
    "v_lshlrev_b32 v106, 16, v46\n"
    "v_and_b32 v107, 0xffff0000, v46\n"
    "v_pk_mul_f32 v[106:107], v[106:107], v[102:103] op_sel_hi:[1,0]\n"
    "v_cvt_pk_bf16_f32 v46, v106, v107\n"
    "v_lshlrev_b32 v106, 16, v47\n"
    "v_and_b32 v107, 0xffff0000, v47\n"
    "v_pk_mul_f32 v[106:107], v[106:107], v[102:103] op_sel_hi:[1,0]\n"
    "v_cvt_pk_bf16_f32 v47, v106, v107\n"
    "v_lshlrev_b32 v106, 16, v64\n"
    "v_and_b32 v107, 0xffff0000, v64\n"
    "v_pk_mul_f32 v[106:107], v[106:107], v[102:103] op_sel_hi:[1,0]\n"
    "v_cvt_pk_bf16_f32 v64, v106, v107\n"
    "v_lshlrev_b32 v106, 16, v65\n"
    "v_and_b32 v107, 0xffff0000, v65\n"
    "v_pk_mul_f32 v[106:107], v[106:107], v[102:103] op_sel_hi:[1,0]\n"
    "v_cvt_pk_bf16_f32 v65, v106, v107\n"
    "v_lshlrev_b32 v106, 16, v66\n"
    "v_and_b32 v107, 0xffff0000, v66\n"
    "v_pk_mul_f32 v[106:107], v[106:107], v[102:103] op_sel_hi:[1,0]\n"
    "v_cvt_pk_bf16_f32 v66, v106, v107\n"
    "v_lshlrev_b32 v106, 16, v67\n"
    "v_and_b32 v107, 0xffff0000, v67\n"
    "v_pk_mul_f32 v[106:107], v[106:107], v[102:103] op_sel_hi:[1,0]\n"
    "v_cvt_pk_bf16_f32 v67, v106, v107\n"
    "v_lshlrev_b32 v106, 16, v68\n"
    "v_and_b32 v107, 0xffff0000, v68\n"
    "v_pk_mul_f32 v[106:107], v[106:107], v[102:103] op_sel_hi:[1,0]\n"
    "v_cvt_pk_bf16_f32 v68, v106, v107\n"
    "v_lshlrev_b32 v106, 16, v69\n"
    "v_and_b32 v107, 0xffff0000, v69\n"
    "v_pk_mul_f32 v[106:107], v[106:107], v[102:103] op_sel_hi:[1,0]\n"
    "v_cvt_pk_bf16_f32 v69, v106, v107\n"
    "v_lshlrev_b32 v106, 16, v70\n"
    "v_and_b32 v107, 0xffff0000, v70\n"
    "v_pk_mul_f32 v[106:107], v[106:107], v[102:103] op_sel_hi:[1,0]\n"
    "v_cvt_pk_bf16_f32 v70, v106, v107\n"
    "v_lshlrev_b32 v106, 16, v71\n"
    "v_and_b32 v107, 0xffff0000, v71\n"
    "v_pk_mul_f32 v[106:107], v[106:107], v[102:103] op_sel_hi:[1,0]\n"
    "v_cvt_pk_bf16_f32 v71, v106, v107\n"
    "v_accvgpr_read_b32 v106, a172\n"
    "v_accvgpr_read_b32 v107, a173\n"
    "v_pk_mul_f32 v[106:107], v[106:107], v[102:103] op_sel_hi:[1,0]\n"
    "v_accvgpr_write_b32 a172, v106\n"
    "v_accvgpr_write_b32 a173, v107\n"
    "v_accvgpr_read_b32 v106, a174\n"
    "v_accvgpr_read_b32 v107, a175\n"
    "v_pk_mul_f32 v[106:107], v[106:107], v[102:103] op_sel_hi:[1,0]\n"
    "v_accvgpr_write_b32 a174, v106\n"
    "v_accvgpr_write_b32 a175, v107\n"
    "v_pk_mul_f32 v[124:125], v[124:125], v[102:103] op_sel_hi:[1,0]\n"
    "v_pk_mul_f32 v[126:127], v[126:127], v[102:103] op_sel_hi:[1,0]\n"
    "v_pk_mul_f32 v[136:137], v[136:137], v[102:103] op_sel_hi:[1,0]\n"
    "v_pk_mul_f32 v[138:139], v[138:139], v[102:103] op_sel_hi:[1,0]\n"
    "v_pk_mul_f32 v[148:149], v[148:149], v[102:103] op_sel_hi:[1,0]\n"
    "v_pk_mul_f32 v[150:151], v[150:151], v[102:103] op_sel_hi:[1,0]\n"
    "v_accvgpr_read_b32 v106, a184\n"
    "v_accvgpr_read_b32 v107, a185\n"
    "v_pk_mul_f32 v[106:107], v[106:107], v[102:103] op_sel_hi:[1,0]\n"
    "v_accvgpr_write_b32 a184, v106\n"
    "v_accvgpr_write_b32 a185, v107\n"
    "v_accvgpr_read_b32 v106, a186\n"
    "v_accvgpr_read_b32 v107, a187\n"
    "v_pk_mul_f32 v[106:107], v[106:107], v[102:103] op_sel_hi:[1,0]\n"
    "v_accvgpr_write_b32 a186, v106\n"
    "v_accvgpr_write_b32 a187, v107\n"
    "v_pk_mul_f32 v[160:161], v[160:161], v[102:103] op_sel_hi:[1,0]\n"
    "v_pk_mul_f32 v[162:163], v[162:163], v[102:103] op_sel_hi:[1,0]\n"
    "v_pk_mul_f32 v[172:173], v[172:173], v[102:103] op_sel_hi:[1,0]\n"
    "v_pk_mul_f32 v[174:175], v[174:175], v[102:103] op_sel_hi:[1,0]\n"
    "v_pk_mul_f32 v[184:185], v[184:185], v[102:103] op_sel_hi:[1,0]\n"
    "v_pk_mul_f32 v[186:187], v[186:187], v[102:103] op_sel_hi:[1,0]\n"
    "v_accvgpr_read_b32 v106, a196\n"
    "v_accvgpr_read_b32 v107, a197\n"
    "v_pk_mul_f32 v[106:107], v[106:107], v[102:103] op_sel_hi:[1,0]\n"
    "v_accvgpr_write_b32 a196, v106\n"
    "v_accvgpr_write_b32 a197, v107\n"
    "v_accvgpr_read_b32 v106, a198\n"
    "v_accvgpr_read_b32 v107, a199\n"
    "v_pk_mul_f32 v[106:107], v[106:107], v[102:103] op_sel_hi:[1,0]\n"
    "v_accvgpr_write_b32 a198, v106\n"
    "v_accvgpr_write_b32 a199, v107\n"
    "v_pk_mul_f32 v[196:197], v[196:197], v[102:103] op_sel_hi:[1,0]\n"
    "v_pk_mul_f32 v[198:199], v[198:199], v[102:103] op_sel_hi:[1,0]\n"
    "v_pk_mul_f32 v[208:209], v[208:209], v[102:103] op_sel_hi:[1,0]\n"
    "v_pk_mul_f32 v[210:211], v[210:211], v[102:103] op_sel_hi:[1,0]\n"
    "v_pk_mul_f32 v[220:221], v[220:221], v[102:103] op_sel_hi:[1,0]\n"
    "v_pk_mul_f32 v[222:223], v[222:223], v[102:103] op_sel_hi:[1,0]\n"
    "v_accvgpr_read_b32 v106, a208\n"
    "v_accvgpr_read_b32 v107, a209\n"
    "v_pk_mul_f32 v[106:107], v[106:107], v[102:103] op_sel_hi:[1,0]\n"
    "v_accvgpr_write_b32 a208, v106\n"
    "v_accvgpr_write_b32 a209, v107\n"
    "v_accvgpr_read_b32 v106, a210\n"
    "v_accvgpr_read_b32 v107, a211\n"
    "v_pk_mul_f32 v[106:107], v[106:107], v[102:103] op_sel_hi:[1,0]\n"
    "v_accvgpr_write_b32 a210, v106\n"
    "v_accvgpr_write_b32 a211, v107\n"
    "v_pk_mul_f32 v[232:233], v[232:233], v[102:103] op_sel_hi:[1,0]\n"
    "v_pk_mul_f32 v[234:235], v[234:235], v[102:103] op_sel_hi:[1,0]\n"
    "v_pk_mul_f32 v[244:245], v[244:245], v[102:103] op_sel_hi:[1,0]\n"
    "v_pk_mul_f32 v[246:247], v[246:247], v[102:103] op_sel_hi:[1,0]\n"
    "v_accvgpr_read_b32 v106, a224\n"
    "v_accvgpr_read_b32 v107, a225\n"
    "v_pk_mul_f32 v[106:107], v[106:107], v[102:103] op_sel_hi:[1,0]\n"
    "v_accvgpr_write_b32 a224, v106\n"
    "v_accvgpr_write_b32 a225, v107\n"
    "v_accvgpr_read_b32 v106, a226\n"
    "v_accvgpr_read_b32 v107, a227\n"
    "v_pk_mul_f32 v[106:107], v[106:107], v[102:103] op_sel_hi:[1,0]\n"
    "v_accvgpr_write_b32 a226, v106\n"
    "v_accvgpr_write_b32 a227, v107\n"
    "v_accvgpr_read_b32 v106, a220\n"
    "v_accvgpr_read_b32 v107, a221\n"
    "v_pk_mul_f32 v[106:107], v[106:107], v[102:103] op_sel_hi:[1,0]\n"
    "v_accvgpr_write_b32 a220, v106\n"
    "v_accvgpr_write_b32 a221, v107\n"
    "v_accvgpr_read_b32 v106, a222\n"
    "v_accvgpr_read_b32 v107, a223\n"
    "v_pk_mul_f32 v[106:107], v[106:107], v[102:103] op_sel_hi:[1,0]\n"
    "v_accvgpr_write_b32 a222, v106\n"
    "v_accvgpr_write_b32 a223, v107\n"
    "RSK012_%=:\n"
    "s_add_u32 s18, s18, 4\n"
    "s_addc_u32 s19, s19, 0\n"
    "s_setpc_b64 s[18:19]\n"
    "RARE10_%=:\n"
    "s_nop 7\n"
    "s_nop 7\n"
    "v_max3_f32 v97, v12, v13, v14\n"
    "v_max3_f32 v97, v97, v15, 0\n"
    "v_max3_f32 v98, v16, v17, v18\n"
    "v_max3_f32 v98, v98, v19, 0\n"
    "v_max3_f32 v99, v20, v21, v22\n"
    "v_max3_f32 v99, v99, v23, 0\n"
    "v_mov_b32 v100, v97\n"
    "v_mov_b32 v101, v98\n"
    "v_mov_b32 v102, v99\n"
    "s_nop 1\n"
    "v_permlane32_swap_b32 v97, v100\n"
    "v_permlane32_swap_b32 v98, v101\n"
    "v_permlane32_swap_b32 v99, v102\n"
    "s_nop 1\n"
    "v_max_f32 v97, v97, v100\n"
    "v_max_f32 v98, v98, v101\n"
    "v_max_f32 v99, v99, v102\n"
    "v_mov_b32 v100, v97\n"
    "v_mov_b32 v101, v98\n"
    "v_mov_b32 v102, v99\n"
    "s_nop 1\n"
    "v_permlane16_swap_b32 v97, v100\n"
    "v_permlane16_swap_b32 v98, v101\n"
    "v_permlane16_swap_b32 v99, v102\n"
    "s_nop 1\n"
    "v_max_f32 v97, v97, v100\n"
    "v_max_f32 v98, v98, v101\n"
    "v_max_f32 v99, v99, v102\n"
    "v_ceil_f32 v97, v97\n"
    "v_exp_f32 v100, -v97\n"
    "v_ceil_f32 v98, v98\n"
    "v_exp_f32 v101, -v98\n"
    "v_ceil_f32 v99, v99\n"
    "v_exp_f32 v102, -v99\n"
    "v_cmp_lt_f32 vcc, 0, v97\n"
    "s_cbranch_vccz RSK100_%=\n"
    "v_sub_f32 v91, v91, v97\n"
    "v_sub_f32 v12, v12, v97\n"
    "v_sub_f32 v13, v13, v97\n"
    "v_sub_f32 v14, v14, v97\n"
    "v_sub_f32 v15, v15, v97\n"
    "v_lshlrev_b32 v106, 16, v24\n"
    "v_and_b32 v107, 0xffff0000, v24\n"
    "v_pk_mul_f32 v[106:107], v[106:107], v[100:101] op_sel_hi:[1,0]\n"
    "v_cvt_pk_bf16_f32 v24, v106, v107\n"
    "v_lshlrev_b32 v106, 16, v25\n"
    "v_and_b32 v107, 0xffff0000, v25\n"
    "v_pk_mul_f32 v[106:107], v[106:107], v[100:101] op_sel_hi:[1,0]\n"
    "v_cvt_pk_bf16_f32 v25, v106, v107\n"
    "v_lshlrev_b32 v106, 16, v26\n"
    "v_and_b32 v107, 0xffff0000, v26\n"
    "v_pk_mul_f32 v[106:107], v[106:107], v[100:101] op_sel_hi:[1,0]\n"
    "v_cvt_pk_bf16_f32 v26, v106, v107\n"
    "v_lshlrev_b32 v106, 16, v27\n"
    "v_and_b32 v107, 0xffff0000, v27\n"
    "v_pk_mul_f32 v[106:107], v[106:107], v[100:101] op_sel_hi:[1,0]\n"
    "v_cvt_pk_bf16_f32 v27, v106, v107\n"
    "v_lshlrev_b32 v106, 16, v28\n"
    "v_and_b32 v107, 0xffff0000, v28\n"
    "v_pk_mul_f32 v[106:107], v[106:107], v[100:101] op_sel_hi:[1,0]\n"
    "v_cvt_pk_bf16_f32 v28, v106, v107\n"
    "v_lshlrev_b32 v106, 16, v29\n"
    "v_and_b32 v107, 0xffff0000, v29\n"
    "v_pk_mul_f32 v[106:107], v[106:107], v[100:101] op_sel_hi:[1,0]\n"
    "v_cvt_pk_bf16_f32 v29, v106, v107\n"
    "v_lshlrev_b32 v106, 16, v30\n"
    "v_and_b32 v107, 0xffff0000, v30\n"
    "v_pk_mul_f32 v[106:107], v[106:107], v[100:101] op_sel_hi:[1,0]\n"
    "v_cvt_pk_bf16_f32 v30, v106, v107\n"
    "v_lshlrev_b32 v106, 16, v31\n"
    "v_and_b32 v107, 0xffff0000, v31\n"
    "v_pk_mul_f32 v[106:107], v[106:107], v[100:101] op_sel_hi:[1,0]\n"
    "v_cvt_pk_bf16_f32 v31, v106, v107\n"
    "RSK100_%=:\n"
    "v_cmp_lt_f32 vcc, 0, v98\n"
    "s_cbranch_vccz RSK101_%=\n"
    "v_sub_f32 v92, v92, v98\n"
    "v_sub_f32 v16, v16, v98\n"
    "v_sub_f32 v17, v17, v98\n"
    "v_sub_f32 v18, v18, v98\n"
    "v_sub_f32 v19, v19, v98\n"
    "v_lshlrev_b32 v106, 16, v32\n"
    "v_and_b32 v107, 0xffff0000, v32\n"
    "v_pk_mul_f32 v[106:107], v[106:107], v[100:101] op_sel:[0,1] op_sel_hi:[1,1]\n"
    "v_cvt_pk_bf16_f32 v32, v106, v107\n"
    "v_lshlrev_b32 v106, 16, v33\n"
    "v_and_b32 v107, 0xffff0000, v33\n"
    "v_pk_mul_f32 v[106:107], v[106:107], v[100:101] op_sel:[0,1] op_sel_hi:[1,1]\n"
    "v_cvt_pk_bf16_f32 v33, v106, v107\n"
    "v_lshlrev_b32 v106, 16, v34\n"
    "v_and_b32 v107, 0xffff0000, v34\n"
    "v_pk_mul_f32 v[106:107], v[106:107], v[100:101] op_sel:[0,1] op_sel_hi:[1,1]\n"
    "v_cvt_pk_bf16_f32 v34, v106, v107\n"
    "v_lshlrev_b32 v106, 16, v35\n"
    "v_and_b32 v107, 0xffff0000, v35\n"
    "v_pk_mul_f32 v[106:107], v[106:107], v[100:101] op_sel:[0,1] op_sel_hi:[1,1]\n"
    "v_cvt_pk_bf16_f32 v35, v106, v107\n"
    "v_lshlrev_b32 v106, 16, v36\n"
    "v_and_b32 v107, 0xffff0000, v36\n"
    "v_pk_mul_f32 v[106:107], v[106:107], v[100:101] op_sel:[0,1] op_sel_hi:[1,1]\n"
    "v_cvt_pk_bf16_f32 v36, v106, v107\n"
    "v_lshlrev_b32 v106, 16, v37\n"
    "v_and_b32 v107, 0xffff0000, v37\n"
    "v_pk_mul_f32 v[106:107], v[106:107], v[100:101] op_sel:[0,1] op_sel_hi:[1,1]\n"
    "v_cvt_pk_bf16_f32 v37, v106, v107\n"
    "v_lshlrev_b32 v106, 16, v38\n"
    "v_and_b32 v107, 0xffff0000, v38\n"
    "v_pk_mul_f32 v[106:107], v[106:107], v[100:101] op_sel:[0,1] op_sel_hi:[1,1]\n"
    "v_cvt_pk_bf16_f32 v38, v106, v107\n"
    "v_lshlrev_b32 v106, 16, v39\n"
    "v_and_b32 v107, 0xffff0000, v39\n"
    "v_pk_mul_f32 v[106:107], v[106:107], v[100:101] op_sel:[0,1] op_sel_hi:[1,1]\n"
    "v_cvt_pk_bf16_f32 v39, v106, v107\n"
    "RSK101_%=:\n"
    "v_cmp_lt_f32 vcc, 0, v99\n"
    "s_cbranch_vccz RSK102_%=\n"
    "v_sub_f32 v93, v93, v99\n"
    "v_sub_f32 v20, v20, v99\n"
    "v_sub_f32 v21, v21, v99\n"
    "v_sub_f32 v22, v22, v99\n"
    "v_sub_f32 v23, v23, v99\n"
    "v_lshlrev_b32 v106, 16, v40\n"
    "v_and_b32 v107, 0xffff0000, v40\n"
    "v_pk_mul_f32 v[106:107], v[106:107], v[102:103] op_sel_hi:[1,0]\n"
    "v_cvt_pk_bf16_f32 v40, v106, v107\n"
    "v_lshlrev_b32 v106, 16, v41\n"
    "v_and_b32 v107, 0xffff0000, v41\n"
    "v_pk_mul_f32 v[106:107], v[106:107], v[102:103] op_sel_hi:[1,0]\n"
    "v_cvt_pk_bf16_f32 v41, v106, v107\n"
    "v_lshlrev_b32 v106, 16, v42\n"
    "v_and_b32 v107, 0xffff0000, v42\n"
    "v_pk_mul_f32 v[106:107], v[106:107], v[102:103] op_sel_hi:[1,0]\n"
    "v_cvt_pk_bf16_f32 v42, v106, v107\n"
    "v_lshlrev_b32 v106, 16, v43\n"
    "v_and_b32 v107, 0xffff0000, v43\n"
    "v_pk_mul_f32 v[106:107], v[106:107], v[102:103] op_sel_hi:[1,0]\n"
    "v_cvt_pk_bf16_f32 v43, v106, v107\n"
    "v_lshlrev_b32 v106, 16, v44\n"
    "v_and_b32 v107, 0xffff0000, v44\n"
    "v_pk_mul_f32 v[106:107], v[106:107], v[102:103] op_sel_hi:[1,0]\n"
    "v_cvt_pk_bf16_f32 v44, v106, v107\n"
    "v_lshlrev_b32 v106, 16, v45\n"
    "v_and_b32 v107, 0xffff0000, v45\n"
    "v_pk_mul_f32 v[106:107], v[106:107], v[102:103] op_sel_hi:[1,0]\n"
    "v_cvt_pk_bf16_f32 v45, v106, v107\n"
    "v_lshlrev_b32 v106, 16, v46\n"
    "v_and_b32 v107, 0xffff0000, v46\n"
    "v_pk_mul_f32 v[106:107], v[106:107], v[102:103] op_sel_hi:[1,0]\n"
    "v_cvt_pk_bf16_f32 v46, v106, v107\n"
    "v_lshlrev_b32 v106, 16, v47\n"
    "v_and_b32 v107, 0xffff0000, v47\n"
    "v_pk_mul_f32 v[106:107], v[106:107], v[102:103] op_sel_hi:[1,0]\n"
    "v_cvt_pk_bf16_f32 v47, v106, v107\n"
    "RSK102_%=:\n"
    "s_add_u32 s18, s18, 4\n"
    "s_addc_u32 s19, s19, 0\n"
    "s_setpc_b64 s[18:19]\n"
    "RARE11_%=:\n"
    "s_nop 7\n"
    "s_nop 7\n"
    "v_max3_f32 v97, v12, v13, v14\n"
    "v_max3_f32 v97, v97, v15, 0\n"
    "v_max3_f32 v98, v16, v17, v18\n"
    "v_max3_f32 v98, v98, v19, 0\n"
    "v_max3_f32 v99, v20, v21, v22\n"
    "v_max3_f32 v99, v99, v23, 0\n"
    "v_mov_b32 v100, v97\n"
    "v_mov_b32 v101, v98\n"
    "v_mov_b32 v102, v99\n"
    "s_nop 1\n"
    "v_permlane32_swap_b32 v97, v100\n"
    "v_permlane32_swap_b32 v98, v101\n"
    "v_permlane32_swap_b32 v99, v102\n"
    "s_nop 1\n"
    "v_max_f32 v97, v97, v100\n"
    "v_max_f32 v98, v98, v101\n"
    "v_max_f32 v99, v99, v102\n"
    "v_mov_b32 v100, v97\n"
    "v_mov_b32 v101, v98\n"
    "v_mov_b32 v102, v99\n"
    "s_nop 1\n"
    "v_permlane16_swap_b32 v97, v100\n"
    "v_permlane16_swap_b32 v98, v101\n"
    "v_permlane16_swap_b32 v99, v102\n"
    "s_nop 1\n"
    "v_max_f32 v97, v97, v100\n"
    "v_max_f32 v98, v98, v101\n"
    "v_max_f32 v99, v99, v102\n"
    "v_ceil_f32 v97, v97\n"
    "v_exp_f32 v100, -v97\n"
    "v_ceil_f32 v98, v98\n"
    "v_exp_f32 v101, -v98\n"
    "v_ceil_f32 v99, v99\n"
    "v_exp_f32 v102, -v99\n"
    "v_cmp_lt_f32 vcc, 0, v97\n"
    "s_cbranch_vccz RSK110_%=\n"
    "v_sub_f32 v91, v91, v97\n"
    "v_sub_f32 v12, v12, v97\n"
    "v_sub_f32 v13, v13, v97\n"
    "v_sub_f32 v14, v14, v97\n"
    "v_sub_f32 v15, v15, v97\n"
    "v_lshlrev_b32 v106, 16, v24\n"
    "v_and_b32 v107, 0xffff0000, v24\n"
    "v_pk_mul_f32 v[106:107], v[106:107], v[100:101] op_sel_hi:[1,0]\n"
    "v_cvt_pk_bf16_f32 v24, v106, v107\n"
    "v_lshlrev_b32 v106, 16, v25\n"
    "v_and_b32 v107, 0xffff0000, v25\n"
    "v_pk_mul_f32 v[106:107], v[106:107], v[100:101] op_sel_hi:[1,0]\n"
    "v_cvt_pk_bf16_f32 v25, v106, v107\n"
    "v_lshlrev_b32 v106, 16, v26\n"
    "v_and_b32 v107, 0xffff0000, v26\n"
    "v_pk_mul_f32 v[106:107], v[106:107], v[100:101] op_sel_hi:[1,0]\n"
    "v_cvt_pk_bf16_f32 v26, v106, v107\n"
    "v_lshlrev_b32 v106, 16, v27\n"
    "v_and_b32 v107, 0xffff0000, v27\n"
    "v_pk_mul_f32 v[106:107], v[106:107], v[100:101] op_sel_hi:[1,0]\n"
    "v_cvt_pk_bf16_f32 v27, v106, v107\n"
    "v_lshlrev_b32 v106, 16, v28\n"
    "v_and_b32 v107, 0xffff0000, v28\n"
    "v_pk_mul_f32 v[106:107], v[106:107], v[100:101] op_sel_hi:[1,0]\n"
    "v_cvt_pk_bf16_f32 v28, v106, v107\n"
    "v_lshlrev_b32 v106, 16, v29\n"
    "v_and_b32 v107, 0xffff0000, v29\n"
    "v_pk_mul_f32 v[106:107], v[106:107], v[100:101] op_sel_hi:[1,0]\n"
    "v_cvt_pk_bf16_f32 v29, v106, v107\n"
    "v_lshlrev_b32 v106, 16, v30\n"
    "v_and_b32 v107, 0xffff0000, v30\n"
    "v_pk_mul_f32 v[106:107], v[106:107], v[100:101] op_sel_hi:[1,0]\n"
    "v_cvt_pk_bf16_f32 v30, v106, v107\n"
    "v_lshlrev_b32 v106, 16, v31\n"
    "v_and_b32 v107, 0xffff0000, v31\n"
    "v_pk_mul_f32 v[106:107], v[106:107], v[100:101] op_sel_hi:[1,0]\n"
    "v_cvt_pk_bf16_f32 v31, v106, v107\n"
    "v_lshlrev_b32 v106, 16, v48\n"
    "v_and_b32 v107, 0xffff0000, v48\n"
    "v_pk_mul_f32 v[106:107], v[106:107], v[100:101] op_sel_hi:[1,0]\n"
    "v_cvt_pk_bf16_f32 v48, v106, v107\n"
    "v_lshlrev_b32 v106, 16, v49\n"
    "v_and_b32 v107, 0xffff0000, v49\n"
    "v_pk_mul_f32 v[106:107], v[106:107], v[100:101] op_sel_hi:[1,0]\n"
    "v_cvt_pk_bf16_f32 v49, v106, v107\n"
    "v_lshlrev_b32 v106, 16, v50\n"
    "v_and_b32 v107, 0xffff0000, v50\n"
    "v_pk_mul_f32 v[106:107], v[106:107], v[100:101] op_sel_hi:[1,0]\n"
    "v_cvt_pk_bf16_f32 v50, v106, v107\n"
    "v_lshlrev_b32 v106, 16, v51\n"
    "v_and_b32 v107, 0xffff0000, v51\n"
    "v_pk_mul_f32 v[106:107], v[106:107], v[100:101] op_sel_hi:[1,0]\n"
    "v_cvt_pk_bf16_f32 v51, v106, v107\n"
    "v_lshlrev_b32 v106, 16, v52\n"
    "v_and_b32 v107, 0xffff0000, v52\n"
    "v_pk_mul_f32 v[106:107], v[106:107], v[100:101] op_sel_hi:[1,0]\n"
    "v_cvt_pk_bf16_f32 v52, v106, v107\n"
    "v_lshlrev_b32 v106, 16, v53\n"
    "v_and_b32 v107, 0xffff0000, v53\n"
    "v_pk_mul_f32 v[106:107], v[106:107], v[100:101] op_sel_hi:[1,0]\n"
    "v_cvt_pk_bf16_f32 v53, v106, v107\n"
    "v_lshlrev_b32 v106, 16, v54\n"
    "v_and_b32 v107, 0xffff0000, v54\n"
    "v_pk_mul_f32 v[106:107], v[106:107], v[100:101] op_sel_hi:[1,0]\n"
    "v_cvt_pk_bf16_f32 v54, v106, v107\n"
    "v_lshlrev_b32 v106, 16, v55\n"
    "v_and_b32 v107, 0xffff0000, v55\n"
    "v_pk_mul_f32 v[106:107], v[106:107], v[100:101] op_sel_hi:[1,0]\n"
    "v_cvt_pk_bf16_f32 v55, v106, v107\n"
    "v_accvgpr_read_b32 v106, a164\n"
    "v_accvgpr_read_b32 v107, a165\n"
    "v_pk_mul_f32 v[106:107], v[106:107], v[100:101] op_sel_hi:[1,0]\n"
    "v_accvgpr_write_b32 a164, v106\n"
    "v_accvgpr_write_b32 a165, v107\n"
    "v_accvgpr_read_b32 v106, a166\n"
    "v_accvgpr_read_b32 v107, a167\n"
    "v_pk_mul_f32 v[106:107], v[106:107], v[100:101] op_sel_hi:[1,0]\n"
    "v_accvgpr_write_b32 a166, v106\n"
    "v_accvgpr_write_b32 a167, v107\n"
    "v_pk_mul_f32 v[116:117], v[116:117], v[100:101] op_sel_hi:[1,0]\n"
    "v_pk_mul_f32 v[118:119], v[118:119], v[100:101] op_sel_hi:[1,0]\n"
    "v_pk_mul_f32 v[128:129], v[128:129], v[100:101] op_sel_hi:[1,0]\n"
    "v_pk_mul_f32 v[130:131], v[130:131], v[100:101] op_sel_hi:[1,0]\n"
    "v_pk_mul_f32 v[140:141], v[140:141], v[100:101] op_sel_hi:[1,0]\n"
    "v_pk_mul_f32 v[142:143], v[142:143], v[100:101] op_sel_hi:[1,0]\n"
    "v_accvgpr_read_b32 v106, a176\n"
    "v_accvgpr_read_b32 v107, a177\n"
    "v_pk_mul_f32 v[106:107], v[106:107], v[100:101] op_sel_hi:[1,0]\n"
    "v_accvgpr_write_b32 a176, v106\n"
    "v_accvgpr_write_b32 a177, v107\n"
    "v_accvgpr_read_b32 v106, a178\n"
    "v_accvgpr_read_b32 v107, a179\n"
    "v_pk_mul_f32 v[106:107], v[106:107], v[100:101] op_sel_hi:[1,0]\n"
    "v_accvgpr_write_b32 a178, v106\n"
    "v_accvgpr_write_b32 a179, v107\n"
    "v_pk_mul_f32 v[152:153], v[152:153], v[100:101] op_sel_hi:[1,0]\n"
    "v_pk_mul_f32 v[154:155], v[154:155], v[100:101] op_sel_hi:[1,0]\n"
    "v_pk_mul_f32 v[164:165], v[164:165], v[100:101] op_sel_hi:[1,0]\n"
    "v_pk_mul_f32 v[166:167], v[166:167], v[100:101] op_sel_hi:[1,0]\n"
    "v_pk_mul_f32 v[176:177], v[176:177], v[100:101] op_sel_hi:[1,0]\n"
    "v_pk_mul_f32 v[178:179], v[178:179], v[100:101] op_sel_hi:[1,0]\n"
    "v_accvgpr_read_b32 v106, a188\n"
    "v_accvgpr_read_b32 v107, a189\n"
    "v_pk_mul_f32 v[106:107], v[106:107], v[100:101] op_sel_hi:[1,0]\n"
    "v_accvgpr_write_b32 a188, v106\n"
    "v_accvgpr_write_b32 a189, v107\n"
    "v_accvgpr_read_b32 v106, a190\n"
    "v_accvgpr_read_b32 v107, a191\n"
    "v_pk_mul_f32 v[106:107], v[106:107], v[100:101] op_sel_hi:[1,0]\n"
    "v_accvgpr_write_b32 a190, v106\n"
    "v_accvgpr_write_b32 a191, v107\n"
    "v_pk_mul_f32 v[188:189], v[188:189], v[100:101] op_sel_hi:[1,0]\n"
    "v_pk_mul_f32 v[190:191], v[190:191], v[100:101] op_sel_hi:[1,0]\n"
    "v_pk_mul_f32 v[200:201], v[200:201], v[100:101] op_sel_hi:[1,0]\n"
    "v_pk_mul_f32 v[202:203], v[202:203], v[100:101] op_sel_hi:[1,0]\n"
    "v_pk_mul_f32 v[212:213], v[212:213], v[100:101] op_sel_hi:[1,0]\n"
    "v_pk_mul_f32 v[214:215], v[214:215], v[100:101] op_sel_hi:[1,0]\n"
    "v_accvgpr_read_b32 v106, a200\n"
    "v_accvgpr_read_b32 v107, a201\n"
    "v_pk_mul_f32 v[106:107], v[106:107], v[100:101] op_sel_hi:[1,0]\n"
    "v_accvgpr_write_b32 a200, v106\n"
    "v_accvgpr_write_b32 a201, v107\n"
    "v_accvgpr_read_b32 v106, a202\n"
    "v_accvgpr_read_b32 v107, a203\n"
    "v_pk_mul_f32 v[106:107], v[106:107], v[100:101] op_sel_hi:[1,0]\n"
    "v_accvgpr_write_b32 a202, v106\n"
    "v_accvgpr_write_b32 a203, v107\n"
    "v_pk_mul_f32 v[224:225], v[224:225], v[100:101] op_sel_hi:[1,0]\n"
    "v_pk_mul_f32 v[226:227], v[226:227], v[100:101] op_sel_hi:[1,0]\n"
    "v_pk_mul_f32 v[236:237], v[236:237], v[100:101] op_sel_hi:[1,0]\n"
    "v_pk_mul_f32 v[238:239], v[238:239], v[100:101] op_sel_hi:[1,0]\n"
    "v_pk_mul_f32 v[248:249], v[248:249], v[100:101] op_sel_hi:[1,0]\n"
    "v_pk_mul_f32 v[250:251], v[250:251], v[100:101] op_sel_hi:[1,0]\n"
    "v_accvgpr_read_b32 v106, a212\n"
    "v_accvgpr_read_b32 v107, a213\n"
    "v_pk_mul_f32 v[106:107], v[106:107], v[100:101] op_sel_hi:[1,0]\n"
    "v_accvgpr_write_b32 a212, v106\n"
    "v_accvgpr_write_b32 a213, v107\n"
    "v_accvgpr_read_b32 v106, a214\n"
    "v_accvgpr_read_b32 v107, a215\n"
    "v_pk_mul_f32 v[106:107], v[106:107], v[100:101] op_sel_hi:[1,0]\n"
    "v_accvgpr_write_b32 a214, v106\n"
    "v_accvgpr_write_b32 a215, v107\n"
    "RSK110_%=:\n"
    "v_cmp_lt_f32 vcc, 0, v98\n"
    "s_cbranch_vccz RSK111_%=\n"
    "v_sub_f32 v92, v92, v98\n"
    "v_sub_f32 v16, v16, v98\n"
    "v_sub_f32 v17, v17, v98\n"
    "v_sub_f32 v18, v18, v98\n"
    "v_sub_f32 v19, v19, v98\n"
    "v_lshlrev_b32 v106, 16, v32\n"
    "v_and_b32 v107, 0xffff0000, v32\n"
    "v_pk_mul_f32 v[106:107], v[106:107], v[100:101] op_sel:[0,1] op_sel_hi:[1,1]\n"
    "v_cvt_pk_bf16_f32 v32, v106, v107\n"
    "v_lshlrev_b32 v106, 16, v33\n"
    "v_and_b32 v107, 0xffff0000, v33\n"
    "v_pk_mul_f32 v[106:107], v[106:107], v[100:101] op_sel:[0,1] op_sel_hi:[1,1]\n"
    "v_cvt_pk_bf16_f32 v33, v106, v107\n"
    "v_lshlrev_b32 v106, 16, v34\n"
    "v_and_b32 v107, 0xffff0000, v34\n"
    "v_pk_mul_f32 v[106:107], v[106:107], v[100:101] op_sel:[0,1] op_sel_hi:[1,1]\n"
    "v_cvt_pk_bf16_f32 v34, v106, v107\n"
    "v_lshlrev_b32 v106, 16, v35\n"
    "v_and_b32 v107, 0xffff0000, v35\n"
    "v_pk_mul_f32 v[106:107], v[106:107], v[100:101] op_sel:[0,1] op_sel_hi:[1,1]\n"
    "v_cvt_pk_bf16_f32 v35, v106, v107\n"
    "v_lshlrev_b32 v106, 16, v36\n"
    "v_and_b32 v107, 0xffff0000, v36\n"
    "v_pk_mul_f32 v[106:107], v[106:107], v[100:101] op_sel:[0,1] op_sel_hi:[1,1]\n"
    "v_cvt_pk_bf16_f32 v36, v106, v107\n"
    "v_lshlrev_b32 v106, 16, v37\n"
    "v_and_b32 v107, 0xffff0000, v37\n"
    "v_pk_mul_f32 v[106:107], v[106:107], v[100:101] op_sel:[0,1] op_sel_hi:[1,1]\n"
    "v_cvt_pk_bf16_f32 v37, v106, v107\n"
    "v_lshlrev_b32 v106, 16, v38\n"
    "v_and_b32 v107, 0xffff0000, v38\n"
    "v_pk_mul_f32 v[106:107], v[106:107], v[100:101] op_sel:[0,1] op_sel_hi:[1,1]\n"
    "v_cvt_pk_bf16_f32 v38, v106, v107\n"
    "v_lshlrev_b32 v106, 16, v39\n"
    "v_and_b32 v107, 0xffff0000, v39\n"
    "v_pk_mul_f32 v[106:107], v[106:107], v[100:101] op_sel:[0,1] op_sel_hi:[1,1]\n"
    "v_cvt_pk_bf16_f32 v39, v106, v107\n"
    "v_lshlrev_b32 v106, 16, v56\n"
    "v_and_b32 v107, 0xffff0000, v56\n"
    "v_pk_mul_f32 v[106:107], v[106:107], v[100:101] op_sel:[0,1] op_sel_hi:[1,1]\n"
    "v_cvt_pk_bf16_f32 v56, v106, v107\n"
    "v_lshlrev_b32 v106, 16, v57\n"
    "v_and_b32 v107, 0xffff0000, v57\n"
    "v_pk_mul_f32 v[106:107], v[106:107], v[100:101] op_sel:[0,1] op_sel_hi:[1,1]\n"
    "v_cvt_pk_bf16_f32 v57, v106, v107\n"
    "v_lshlrev_b32 v106, 16, v58\n"
    "v_and_b32 v107, 0xffff0000, v58\n"
    "v_pk_mul_f32 v[106:107], v[106:107], v[100:101] op_sel:[0,1] op_sel_hi:[1,1]\n"
    "v_cvt_pk_bf16_f32 v58, v106, v107\n"
    "v_lshlrev_b32 v106, 16, v59\n"
    "v_and_b32 v107, 0xffff0000, v59\n"
    "v_pk_mul_f32 v[106:107], v[106:107], v[100:101] op_sel:[0,1] op_sel_hi:[1,1]\n"
    "v_cvt_pk_bf16_f32 v59, v106, v107\n"
    "v_lshlrev_b32 v106, 16, v60\n"
    "v_and_b32 v107, 0xffff0000, v60\n"
    "v_pk_mul_f32 v[106:107], v[106:107], v[100:101] op_sel:[0,1] op_sel_hi:[1,1]\n"
    "v_cvt_pk_bf16_f32 v60, v106, v107\n"
    "v_lshlrev_b32 v106, 16, v61\n"
    "v_and_b32 v107, 0xffff0000, v61\n"
    "v_pk_mul_f32 v[106:107], v[106:107], v[100:101] op_sel:[0,1] op_sel_hi:[1,1]\n"
    "v_cvt_pk_bf16_f32 v61, v106, v107\n"
    "v_lshlrev_b32 v106, 16, v62\n"
    "v_and_b32 v107, 0xffff0000, v62\n"
    "v_pk_mul_f32 v[106:107], v[106:107], v[100:101] op_sel:[0,1] op_sel_hi:[1,1]\n"
    "v_cvt_pk_bf16_f32 v62, v106, v107\n"
    "v_lshlrev_b32 v106, 16, v63\n"
    "v_and_b32 v107, 0xffff0000, v63\n"
    "v_pk_mul_f32 v[106:107], v[106:107], v[100:101] op_sel:[0,1] op_sel_hi:[1,1]\n"
    "v_cvt_pk_bf16_f32 v63, v106, v107\n"
    "v_accvgpr_read_b32 v106, a168\n"
    "v_accvgpr_read_b32 v107, a169\n"
    "v_pk_mul_f32 v[106:107], v[106:107], v[100:101] op_sel:[0,1] op_sel_hi:[1,1]\n"
    "v_accvgpr_write_b32 a168, v106\n"
    "v_accvgpr_write_b32 a169, v107\n"
    "v_accvgpr_read_b32 v106, a170\n"
    "v_accvgpr_read_b32 v107, a171\n"
    "v_pk_mul_f32 v[106:107], v[106:107], v[100:101] op_sel:[0,1] op_sel_hi:[1,1]\n"
    "v_accvgpr_write_b32 a170, v106\n"
    "v_accvgpr_write_b32 a171, v107\n"
    "v_pk_mul_f32 v[120:121], v[120:121], v[100:101] op_sel:[0,1] op_sel_hi:[1,1]\n"
    "v_pk_mul_f32 v[122:123], v[122:123], v[100:101] op_sel:[0,1] op_sel_hi:[1,1]\n"
    "v_pk_mul_f32 v[132:133], v[132:133], v[100:101] op_sel:[0,1] op_sel_hi:[1,1]\n"
    "v_pk_mul_f32 v[134:135], v[134:135], v[100:101] op_sel:[0,1] op_sel_hi:[1,1]\n"
    "v_pk_mul_f32 v[144:145], v[144:145], v[100:101] op_sel:[0,1] op_sel_hi:[1,1]\n"
    "v_pk_mul_f32 v[146:147], v[146:147], v[100:101] op_sel:[0,1] op_sel_hi:[1,1]\n"
    "v_accvgpr_read_b32 v106, a180\n"
    "v_accvgpr_read_b32 v107, a181\n"
    "v_pk_mul_f32 v[106:107], v[106:107], v[100:101] op_sel:[0,1] op_sel_hi:[1,1]\n"
    "v_accvgpr_write_b32 a180, v106\n"
    "v_accvgpr_write_b32 a181, v107\n"
    "v_accvgpr_read_b32 v106, a182\n"
    "v_accvgpr_read_b32 v107, a183\n"
    "v_pk_mul_f32 v[106:107], v[106:107], v[100:101] op_sel:[0,1] op_sel_hi:[1,1]\n"
    "v_accvgpr_write_b32 a182, v106\n"
    "v_accvgpr_write_b32 a183, v107\n"
    "v_pk_mul_f32 v[156:157], v[156:157], v[100:101] op_sel:[0,1] op_sel_hi:[1,1]\n"
    "v_pk_mul_f32 v[158:159], v[158:159], v[100:101] op_sel:[0,1] op_sel_hi:[1,1]\n"
    "v_pk_mul_f32 v[168:169], v[168:169], v[100:101] op_sel:[0,1] op_sel_hi:[1,1]\n"
    "v_pk_mul_f32 v[170:171], v[170:171], v[100:101] op_sel:[0,1] op_sel_hi:[1,1]\n"
    "v_pk_mul_f32 v[180:181], v[180:181], v[100:101] op_sel:[0,1] op_sel_hi:[1,1]\n"
    "v_pk_mul_f32 v[182:183], v[182:183], v[100:101] op_sel:[0,1] op_sel_hi:[1,1]\n"
    "v_accvgpr_read_b32 v106, a192\n"
    "v_accvgpr_read_b32 v107, a193\n"
    "v_pk_mul_f32 v[106:107], v[106:107], v[100:101] op_sel:[0,1] op_sel_hi:[1,1]\n"
    "v_accvgpr_write_b32 a192, v106\n"
    "v_accvgpr_write_b32 a193, v107\n"
    "v_accvgpr_read_b32 v106, a194\n"
    "v_accvgpr_read_b32 v107, a195\n"
    "v_pk_mul_f32 v[106:107], v[106:107], v[100:101] op_sel:[0,1] op_sel_hi:[1,1]\n"
    "v_accvgpr_write_b32 a194, v106\n"
    "v_accvgpr_write_b32 a195, v107\n"
    "v_pk_mul_f32 v[192:193], v[192:193], v[100:101] op_sel:[0,1] op_sel_hi:[1,1]\n"
    "v_pk_mul_f32 v[194:195], v[194:195], v[100:101] op_sel:[0,1] op_sel_hi:[1,1]\n"
    "v_pk_mul_f32 v[204:205], v[204:205], v[100:101] op_sel:[0,1] op_sel_hi:[1,1]\n"
    "v_pk_mul_f32 v[206:207], v[206:207], v[100:101] op_sel:[0,1] op_sel_hi:[1,1]\n"
    "v_pk_mul_f32 v[216:217], v[216:217], v[100:101] op_sel:[0,1] op_sel_hi:[1,1]\n"
    "v_pk_mul_f32 v[218:219], v[218:219], v[100:101] op_sel:[0,1] op_sel_hi:[1,1]\n"
    "v_accvgpr_read_b32 v106, a204\n"
    "v_accvgpr_read_b32 v107, a205\n"
    "v_pk_mul_f32 v[106:107], v[106:107], v[100:101] op_sel:[0,1] op_sel_hi:[1,1]\n"
    "v_accvgpr_write_b32 a204, v106\n"
    "v_accvgpr_write_b32 a205, v107\n"
    "v_accvgpr_read_b32 v106, a206\n"
    "v_accvgpr_read_b32 v107, a207\n"
    "v_pk_mul_f32 v[106:107], v[106:107], v[100:101] op_sel:[0,1] op_sel_hi:[1,1]\n"
    "v_accvgpr_write_b32 a206, v106\n"
    "v_accvgpr_write_b32 a207, v107\n"
    "v_pk_mul_f32 v[228:229], v[228:229], v[100:101] op_sel:[0,1] op_sel_hi:[1,1]\n"
    "v_pk_mul_f32 v[230:231], v[230:231], v[100:101] op_sel:[0,1] op_sel_hi:[1,1]\n"
    "v_pk_mul_f32 v[240:241], v[240:241], v[100:101] op_sel:[0,1] op_sel_hi:[1,1]\n"
    "v_pk_mul_f32 v[242:243], v[242:243], v[100:101] op_sel:[0,1] op_sel_hi:[1,1]\n"
    "v_pk_mul_f32 v[252:253], v[252:253], v[100:101] op_sel:[0,1] op_sel_hi:[1,1]\n"
    "v_pk_mul_f32 v[254:255], v[254:255], v[100:101] op_sel:[0,1] op_sel_hi:[1,1]\n"
    "v_accvgpr_read_b32 v106, a216\n"
    "v_accvgpr_read_b32 v107, a217\n"
    "v_pk_mul_f32 v[106:107], v[106:107], v[100:101] op_sel:[0,1] op_sel_hi:[1,1]\n"
    "v_accvgpr_write_b32 a216, v106\n"
    "v_accvgpr_write_b32 a217, v107\n"
    "v_accvgpr_read_b32 v106, a218\n"
    "v_accvgpr_read_b32 v107, a219\n"
    "v_pk_mul_f32 v[106:107], v[106:107], v[100:101] op_sel:[0,1] op_sel_hi:[1,1]\n"
    "v_accvgpr_write_b32 a218, v106\n"
    "v_accvgpr_write_b32 a219, v107\n"
    "RSK111_%=:\n"
    "v_cmp_lt_f32 vcc, 0, v99\n"
    "s_cbranch_vccz RSK112_%=\n"
    "v_sub_f32 v93, v93, v99\n"
    "v_sub_f32 v20, v20, v99\n"
    "v_sub_f32 v21, v21, v99\n"
    "v_sub_f32 v22, v22, v99\n"
    "v_sub_f32 v23, v23, v99\n"
    "v_lshlrev_b32 v106, 16, v40\n"
    "v_and_b32 v107, 0xffff0000, v40\n"
    "v_pk_mul_f32 v[106:107], v[106:107], v[102:103] op_sel_hi:[1,0]\n"
    "v_cvt_pk_bf16_f32 v40, v106, v107\n"
    "v_lshlrev_b32 v106, 16, v41\n"
    "v_and_b32 v107, 0xffff0000, v41\n"
    "v_pk_mul_f32 v[106:107], v[106:107], v[102:103] op_sel_hi:[1,0]\n"
    "v_cvt_pk_bf16_f32 v41, v106, v107\n"
    "v_lshlrev_b32 v106, 16, v42\n"
    "v_and_b32 v107, 0xffff0000, v42\n"
    "v_pk_mul_f32 v[106:107], v[106:107], v[102:103] op_sel_hi:[1,0]\n"
    "v_cvt_pk_bf16_f32 v42, v106, v107\n"
    "v_lshlrev_b32 v106, 16, v43\n"
    "v_and_b32 v107, 0xffff0000, v43\n"
    "v_pk_mul_f32 v[106:107], v[106:107], v[102:103] op_sel_hi:[1,0]\n"
    "v_cvt_pk_bf16_f32 v43, v106, v107\n"
    "v_lshlrev_b32 v106, 16, v44\n"
    "v_and_b32 v107, 0xffff0000, v44\n"
    "v_pk_mul_f32 v[106:107], v[106:107], v[102:103] op_sel_hi:[1,0]\n"
    "v_cvt_pk_bf16_f32 v44, v106, v107\n"
    "v_lshlrev_b32 v106, 16, v45\n"
    "v_and_b32 v107, 0xffff0000, v45\n"
    "v_pk_mul_f32 v[106:107], v[106:107], v[102:103] op_sel_hi:[1,0]\n"
    "v_cvt_pk_bf16_f32 v45, v106, v107\n"
    "v_lshlrev_b32 v106, 16, v46\n"
    "v_and_b32 v107, 0xffff0000, v46\n"
    "v_pk_mul_f32 v[106:107], v[106:107], v[102:103] op_sel_hi:[1,0]\n"
    "v_cvt_pk_bf16_f32 v46, v106, v107\n"
    "v_lshlrev_b32 v106, 16, v47\n"
    "v_and_b32 v107, 0xffff0000, v47\n"
    "v_pk_mul_f32 v[106:107], v[106:107], v[102:103] op_sel_hi:[1,0]\n"
    "v_cvt_pk_bf16_f32 v47, v106, v107\n"
    "v_lshlrev_b32 v106, 16, v64\n"
    "v_and_b32 v107, 0xffff0000, v64\n"
    "v_pk_mul_f32 v[106:107], v[106:107], v[102:103] op_sel_hi:[1,0]\n"
    "v_cvt_pk_bf16_f32 v64, v106, v107\n"
    "v_lshlrev_b32 v106, 16, v65\n"
    "v_and_b32 v107, 0xffff0000, v65\n"
    "v_pk_mul_f32 v[106:107], v[106:107], v[102:103] op_sel_hi:[1,0]\n"
    "v_cvt_pk_bf16_f32 v65, v106, v107\n"
    "v_lshlrev_b32 v106, 16, v66\n"
    "v_and_b32 v107, 0xffff0000, v66\n"
    "v_pk_mul_f32 v[106:107], v[106:107], v[102:103] op_sel_hi:[1,0]\n"
    "v_cvt_pk_bf16_f32 v66, v106, v107\n"
    "v_lshlrev_b32 v106, 16, v67\n"
    "v_and_b32 v107, 0xffff0000, v67\n"
    "v_pk_mul_f32 v[106:107], v[106:107], v[102:103] op_sel_hi:[1,0]\n"
    "v_cvt_pk_bf16_f32 v67, v106, v107\n"
    "v_lshlrev_b32 v106, 16, v68\n"
    "v_and_b32 v107, 0xffff0000, v68\n"
    "v_pk_mul_f32 v[106:107], v[106:107], v[102:103] op_sel_hi:[1,0]\n"
    "v_cvt_pk_bf16_f32 v68, v106, v107\n"
    "v_lshlrev_b32 v106, 16, v69\n"
    "v_and_b32 v107, 0xffff0000, v69\n"
    "v_pk_mul_f32 v[106:107], v[106:107], v[102:103] op_sel_hi:[1,0]\n"
    "v_cvt_pk_bf16_f32 v69, v106, v107\n"
    "v_lshlrev_b32 v106, 16, v70\n"
    "v_and_b32 v107, 0xffff0000, v70\n"
    "v_pk_mul_f32 v[106:107], v[106:107], v[102:103] op_sel_hi:[1,0]\n"
    "v_cvt_pk_bf16_f32 v70, v106, v107\n"
    "v_lshlrev_b32 v106, 16, v71\n"
    "v_and_b32 v107, 0xffff0000, v71\n"
    "v_pk_mul_f32 v[106:107], v[106:107], v[102:103] op_sel_hi:[1,0]\n"
    "v_cvt_pk_bf16_f32 v71, v106, v107\n"
    "v_accvgpr_read_b32 v106, a172\n"
    "v_accvgpr_read_b32 v107, a173\n"
    "v_pk_mul_f32 v[106:107], v[106:107], v[102:103] op_sel_hi:[1,0]\n"
    "v_accvgpr_write_b32 a172, v106\n"
    "v_accvgpr_write_b32 a173, v107\n"
    "v_accvgpr_read_b32 v106, a174\n"
    "v_accvgpr_read_b32 v107, a175\n"
    "v_pk_mul_f32 v[106:107], v[106:107], v[102:103] op_sel_hi:[1,0]\n"
    "v_accvgpr_write_b32 a174, v106\n"
    "v_accvgpr_write_b32 a175, v107\n"
    "v_pk_mul_f32 v[124:125], v[124:125], v[102:103] op_sel_hi:[1,0]\n"
    "v_pk_mul_f32 v[126:127], v[126:127], v[102:103] op_sel_hi:[1,0]\n"
    "v_pk_mul_f32 v[136:137], v[136:137], v[102:103] op_sel_hi:[1,0]\n"
    "v_pk_mul_f32 v[138:139], v[138:139], v[102:103] op_sel_hi:[1,0]\n"
    "v_pk_mul_f32 v[148:149], v[148:149], v[102:103] op_sel_hi:[1,0]\n"
    "v_pk_mul_f32 v[150:151], v[150:151], v[102:103] op_sel_hi:[1,0]\n"
    "v_accvgpr_read_b32 v106, a184\n"
    "v_accvgpr_read_b32 v107, a185\n"
    "v_pk_mul_f32 v[106:107], v[106:107], v[102:103] op_sel_hi:[1,0]\n"
    "v_accvgpr_write_b32 a184, v106\n"
    "v_accvgpr_write_b32 a185, v107\n"
    "v_accvgpr_read_b32 v106, a186\n"
    "v_accvgpr_read_b32 v107, a187\n"
    "v_pk_mul_f32 v[106:107], v[106:107], v[102:103] op_sel_hi:[1,0]\n"
    "v_accvgpr_write_b32 a186, v106\n"
    "v_accvgpr_write_b32 a187, v107\n"
    "v_pk_mul_f32 v[160:161], v[160:161], v[102:103] op_sel_hi:[1,0]\n"
    "v_pk_mul_f32 v[162:163], v[162:163], v[102:103] op_sel_hi:[1,0]\n"
    "v_pk_mul_f32 v[172:173], v[172:173], v[102:103] op_sel_hi:[1,0]\n"
    "v_pk_mul_f32 v[174:175], v[174:175], v[102:103] op_sel_hi:[1,0]\n"
    "v_pk_mul_f32 v[184:185], v[184:185], v[102:103] op_sel_hi:[1,0]\n"
    "v_pk_mul_f32 v[186:187], v[186:187], v[102:103] op_sel_hi:[1,0]\n"
    "v_accvgpr_read_b32 v106, a196\n"
    "v_accvgpr_read_b32 v107, a197\n"
    "v_pk_mul_f32 v[106:107], v[106:107], v[102:103] op_sel_hi:[1,0]\n"
    "v_accvgpr_write_b32 a196, v106\n"
    "v_accvgpr_write_b32 a197, v107\n"
    "v_accvgpr_read_b32 v106, a198\n"
    "v_accvgpr_read_b32 v107, a199\n"
    "v_pk_mul_f32 v[106:107], v[106:107], v[102:103] op_sel_hi:[1,0]\n"
    "v_accvgpr_write_b32 a198, v106\n"
    "v_accvgpr_write_b32 a199, v107\n"
    "v_pk_mul_f32 v[196:197], v[196:197], v[102:103] op_sel_hi:[1,0]\n"
    "v_pk_mul_f32 v[198:199], v[198:199], v[102:103] op_sel_hi:[1,0]\n"
    "v_pk_mul_f32 v[208:209], v[208:209], v[102:103] op_sel_hi:[1,0]\n"
    "v_pk_mul_f32 v[210:211], v[210:211], v[102:103] op_sel_hi:[1,0]\n"
    "v_pk_mul_f32 v[220:221], v[220:221], v[102:103] op_sel_hi:[1,0]\n"
    "v_pk_mul_f32 v[222:223], v[222:223], v[102:103] op_sel_hi:[1,0]\n"
    "v_accvgpr_read_b32 v106, a208\n"
    "v_accvgpr_read_b32 v107, a209\n"
    "v_pk_mul_f32 v[106:107], v[106:107], v[102:103] op_sel_hi:[1,0]\n"
    "v_accvgpr_write_b32 a208, v106\n"
    "v_accvgpr_write_b32 a209, v107\n"
    "v_accvgpr_read_b32 v106, a210\n"
    "v_accvgpr_read_b32 v107, a211\n"
    "v_pk_mul_f32 v[106:107], v[106:107], v[102:103] op_sel_hi:[1,0]\n"
    "v_accvgpr_write_b32 a210, v106\n"
    "v_accvgpr_write_b32 a211, v107\n"
    "v_pk_mul_f32 v[232:233], v[232:233], v[102:103] op_sel_hi:[1,0]\n"
    "v_pk_mul_f32 v[234:235], v[234:235], v[102:103] op_sel_hi:[1,0]\n"
    "v_pk_mul_f32 v[244:245], v[244:245], v[102:103] op_sel_hi:[1,0]\n"
    "v_pk_mul_f32 v[246:247], v[246:247], v[102:103] op_sel_hi:[1,0]\n"
    "v_accvgpr_read_b32 v106, a224\n"
    "v_accvgpr_read_b32 v107, a225\n"
    "v_pk_mul_f32 v[106:107], v[106:107], v[102:103] op_sel_hi:[1,0]\n"
    "v_accvgpr_write_b32 a224, v106\n"
    "v_accvgpr_write_b32 a225, v107\n"
    "v_accvgpr_read_b32 v106, a226\n"
    "v_accvgpr_read_b32 v107, a227\n"
    "v_pk_mul_f32 v[106:107], v[106:107], v[102:103] op_sel_hi:[1,0]\n"
    "v_accvgpr_write_b32 a226, v106\n"
    "v_accvgpr_write_b32 a227, v107\n"
    "v_accvgpr_read_b32 v106, a220\n"
    "v_accvgpr_read_b32 v107, a221\n"
    "v_pk_mul_f32 v[106:107], v[106:107], v[102:103] op_sel_hi:[1,0]\n"
    "v_accvgpr_write_b32 a220, v106\n"
    "v_accvgpr_write_b32 a221, v107\n"
    "v_accvgpr_read_b32 v106, a222\n"
    "v_accvgpr_read_b32 v107, a223\n"
    "v_pk_mul_f32 v[106:107], v[106:107], v[102:103] op_sel_hi:[1,0]\n"
    "v_accvgpr_write_b32 a222, v106\n"
    "v_accvgpr_write_b32 a223, v107\n"
    "RSK112_%=:\n"
    "s_add_u32 s18, s18, 4\n"
    "s_addc_u32 s19, s19, 0\n"
    "s_setpc_b64 s[18:19]\n"
    "END_%=:\n"
    :
    : [q] "s"(q), [k] "s"(k), [v] "s"(v), [o] "s"(o), [bid] "s"(bid), [wid] "s"(wid), [lds] "s"(ldsb)
    : "v0", "v1", "v2", "v3", "v4", "v5", "v6", "v7", "v8", "v9", "v10", "v11", "v12", "v13", "v14", "v15", "v16", "v17", "v18", "v19", "v20", "v21", "v22",
      "v23", "v24", "v25", "v26", "v27", "v28", "v29", "v30", "v31", "v32", "v33", "v34", "v35", "v36", "v37", "v38", "v39", "v40", "v41", "v42", "v43",
      "v44", "v45", "v46", "v47", "v48", "v49", "v50", "v51", "v52", "v53", "v54", "v55", "v56", "v57", "v58", "v59", "v60", "v61", "v62", "v63", "v64",
      "v65", "v66", "v67", "v68", "v69", "v70", "v71", "v72", "v73", "v74", "v75", "v76", "v77", "v78", "v79", "v80", "v81", "v82", "v83", "v84", "v85",
      "v86", "v87", "v88", "v89", "v90", "v91", "v92", "v93", "v94", "v95", "v96", "v97", "v98", "v99", "v100", "v101", "v102", "v103", "v104", "v105",
      "v106", "v107", "v108", "v109", "v110", "v111", "v112", "v113", "v114", "v115", "v116", "v117", "v118", "v119", "v120", "v121", "v122", "v123",
      "v124", "v125", "v126", "v127", "v128", "v129", "v130", "v131", "v132", "v133", "v134", "v135", "v136", "v137", "v138", "v139", "v140", "v141",
      "v142", "v143", "v144", "v145", "v146", "v147", "v148", "v149", "v150", "v151", "v152", "v153", "v154", "v155", "v156", "v157", "v158", "v159",
      "v160", "v161", "v162", "v163", "v164", "v165", "v166", "v167", "v168", "v169", "v170", "v171", "v172", "v173", "v174", "v175", "v176", "v177",
      "v178", "v179", "v180", "v181", "v182", "v183", "v184", "v185", "v186", "v187", "v188", "v189", "v190", "v191", "v192", "v193", "v194", "v195",
      "v196", "v197", "v198", "v199", "v200", "v201", "v202", "v203", "v204", "v205", "v206", "v207", "v208", "v209", "v210", "v211", "v212", "v213",
      "v214", "v215", "v216", "v217", "v218", "v219", "v220", "v221", "v222", "v223", "v224", "v225", "v226", "v227", "v228", "v229", "v230", "v231",
      "v232", "v233", "v234", "v235", "v236", "v237", "v238", "v239", "v240", "v241", "v242", "v243", "v244", "v245", "v246", "v247", "v248", "v249",
      "v250", "v251", "v252", "v253", "v254", "v255", "a0", "a1", "a2", "a3", "a4", "a5", "a6", "a7", "a8", "a9", "a10", "a11", "a12", "a13", "a14", "a15",
      "a16", "a17", "a18", "a19", "a20", "a21", "a22", "a23", "a24", "a25", "a26", "a27", "a28", "a29", "a30", "a31", "a32", "a33", "a34", "a35", "a36",
      "a37", "a38", "a39", "a40", "a41", "a42", "a43", "a44", "a45", "a46", "a47", "a48", "a49", "a50", "a51", "a52", "a53", "a54", "a55", "a56", "a57",
      "a58", "a59", "a60", "a61", "a62", "a63", "a64", "a65", "a66", "a67", "a68", "a69", "a70", "a71", "a72", "a73", "a74", "a75", "a76", "a77", "a78",
      "a79", "a80", "a81", "a82", "a83", "a84", "a85", "a86", "a87", "a88", "a89", "a90", "a91", "a92", "a93", "a94", "a95", "a96", "a97", "a98", "a99",
      "a100", "a101", "a102", "a103", "a104", "a105", "a106", "a107", "a108", "a109", "a110", "a111", "a112", "a113", "a114", "a115", "a116", "a117",
      "a118", "a119", "a120", "a121", "a122", "a123", "a124", "a125", "a126", "a127", "a128", "a129", "a130", "a131", "a132", "a133", "a134", "a135",
      "a136", "a137", "a138", "a139", "a140", "a141", "a142", "a143", "a144", "a145", "a146", "a147", "a148", "a149", "a150", "a151", "a152", "a153",
      "a154", "a155", "a156", "a157", "a158", "a159", "a160", "a161", "a162", "a163", "a164", "a165", "a166", "a167", "a168", "a169", "a170", "a171",
      "a172", "a173", "a174", "a175", "a176", "a177", "a178", "a179", "a180", "a181", "a182", "a183", "a184", "a185", "a186", "a187", "a188", "a189",
      "a190", "a191", "a192", "a193", "a194", "a195", "a196", "a197", "a198", "a199", "a200", "a201", "a202", "a203", "a204", "a205", "a206", "a207",
      "a208", "a209", "a210", "a211", "a212", "a213", "a214", "a215", "a216", "a217", "a218", "a219", "a220", "a221", "a222", "a223", "a224", "a225",
      "a226", "a227", "a228", "a229", "a230", "a231", "a232", "a233", "a234", "a235", "a236", "a237", "a238", "a239", "a240", "a241", "a242", "a243",
      "a244", "a245", "a246", "a247", "a248", "a249", "a250", "a251", "a252", "a253", "a254", "a255", "s14", "s15", "s16", "s17", "s18", "s19", "s20",
      "s21", "s22", "s23", "s24", "s25", "s26", "s27", "s28", "s29", "s30", "s31", "s34", "s35", "s36", "s37", "s38", "s39", "s40", "s41", "s42", "s43",
      "s44", "s45", "s46", "s47", "s48", "s49", "s50", "s51", "s52", "s53", "s54", "s55", "s56", "s57", "s58", "s59", "s60", "s61", "s62", "s63", "s64",
      "s65", "s66", "s67", "s68", "s69", "s70", "s71", "s72", "s73", "s74", "s75", "s76", "s77", "s78", "s79", "s80", "s81", "s82", "s83", "s84", "s85",
      "s86", "s87", "s88", "s89", "s90", "s91", "s92", "s93", "s94", "s95", "s96", "s97", "s98", "s99", "vcc", "scc", "memory");
}

extern "C" void kernel_launch(void* const* d_in, const int* in_sizes, int n_in, void* d_out, int out_size, void* d_ws, size_t ws_size,
                              hipStream_t stream) {
  (void)in_sizes; (void)n_in; (void)out_size; (void)d_ws; (void)ws_size;
  (void)hipFuncSetAttribute((const void*)mha_fwd_kernel, hipFuncAttributeMaxDynamicSharedMemorySize, 163840);
  mha_fwd_kernel<<<256, 256, 163840, stream>>>(d_in[0], d_in[1], d_in[2], d_out);
}
```
